```python
import torch

def _softmax(x, axis=-1):
    return torch.softmax(x, dim=axis)
def _gelu(x):
    return torch.nn.functional.gelu(x, approximate="tanh")
def _silu(x):
    return torch.nn.functional.silu(x)
def _sigmoid(x):
    return torch.sigmoid(x)
def _layernorm(x, axis=-1, eps=1e-5):
    m = x.mean(dim=axis, keepdim=True)
    v = x.var(dim=axis, keepdim=True, unbiased=False)
    return (x - m) / torch.sqrt(v + eps)
def _scatter_add(tgt, idx, src, axis=0):
    return tgt.clone().index_add_(axis, idx, src)
def _segment_sum(src, seg, n):
    out = torch.zeros((n,) + src.shape[1:], dtype=src.dtype)
    return out.index_add_(0, seg, src)
def _pad(x, pw):
    flat = tuple(v for pair in reversed(pw) for v in pair)
    return torch.nn.functional.pad(x, flat)

kernel_name = 'interleave_layers__sliding_block__mixer_token_mlp__prompt'

def setup_inputs():

    _w = (lambda a: (lambda t: t.to(torch.bfloat16) if t.is_floating_point() else t)(torch.as_tensor(a)))
    def _A_setup():
        B, S, D = 1, 2048, 448
        return {
            "x": _w((lambda *s: torch.randn(*s, dtype=torch.bfloat16) * 0.05)(B, S, D)),
            "wqkv": _w((lambda *s: torch.randn(*s, dtype=torch.bfloat16) * 0.05)(D, 3 * D)),
            "wo": _w((lambda *s: torch.randn(*s, dtype=torch.bfloat16) * 0.05)(D, D)),
        }
    _A_in = _A_setup()

    def _B_setup():
        B, S, D, F = 1, 2048, 448, 1792
        return {
            "x": _w((lambda *s: torch.randn(*s, dtype=torch.bfloat16) * 0.05)(B, S, D)),
            "w1": _w((lambda *s: torch.randn(*s, dtype=torch.bfloat16) * 1.6)(S, F) * (S ** -0.5)),
            "b1": _w((lambda *s: torch.randn(*s, dtype=torch.bfloat16) * 12.8)(F) * 0.02),
            "w2": _w((lambda *s: torch.randn(*s, dtype=torch.bfloat16) * 0.8)(F, S) * (F ** -0.5)),
            "b2": _w((lambda *s: torch.randn(*s, dtype=torch.bfloat16) * 12.8)(S) * 0.02),
        }
    _B_in = _B_setup()

    return {
        "A_x": _A_in["x"],
        "A_wqkv": _A_in["wqkv"],
        "A_wo": _A_in["wo"],
        "B_x": _B_in["x"],
        "B_w1": _B_in["w1"],
        "B_b1": _B_in["b1"],
        "B_w2": _B_in["w2"],
        "B_b2": _B_in["b2"],
    }

def reference(A_x, A_wqkv, A_wo, B_x, B_w1, B_b1, B_w2, B_b2):
    def _A_ref(x, wqkv, wo):
        B, S, D = x.shape
        H = 16; Dh = D // H; W = 256
        qkv = (lambda x, *d: x.permute(*d))((x @ wqkv).reshape(B, S, 3, H, Dh), 2, 0, 3, 1, 4)
        q, k, v = qkv[0], qkv[1], qkv[2]
        scores = torch.einsum("bhqd,bhkd->bhqk", q, k) * (Dh ** -0.5)
        idx = torch.arange(S)
        mask = (idx[:, None] - idx[None, :] >= 0) & (idx[:, None] - idx[None, :] < W)
        scores = torch.where(mask[None, None], scores, -1e9)
        p = _softmax(scores, axis=-1)
        o = torch.einsum("bhqk,bhkd->bhqd", p, v)
        return (lambda x, *d: x.permute(*d))(o, 0, 2, 1, 3).reshape(B, S, D) @ wo

    def _B_ref(x, w1, b1, w2, b2):
        B, S, D = x.shape
        h = _layernorm(x, axis=-1)
        u = torch.einsum("bsd,sf->bdf", h, w1) + b1
        u = _gelu(u)
        y = torch.einsum("bdf,fs->bds", u, w2) + b2
        return (lambda x, *d: x.permute(*d))(y, 0, 2, 1)

    x = A_x
    x = x + _A_ref(_layernorm(x, axis=-1), A_wqkv, A_wo)
    x = x + _B_ref(_layernorm(x, axis=-1), B_w1, B_b1, B_w2, B_b2)
    x = x + _A_ref(_layernorm(x, axis=-1), A_wqkv, A_wo)
    x = x + _B_ref(_layernorm(x, axis=-1), B_w1, B_b1, B_w2, B_b2)
    return x
```

```cpp
#include <hip/hip_runtime.h>
#include <hip/hip_bf16.h>
typedef __hip_bfloat16 bf16;
constexpr int S=2048, D=448, H=16, DH=28, W=256, F=1792;
__device__ inline float b2f(bf16 x){return __bfloat162float(x);}
__device__ inline bf16 f2b(float x){return __float2bfloat16(x);}
__global__ void ln_k(const bf16* x, bf16* h, int twice){
  int row=blockIdx.x*4+(threadIdx.x>>6), lane=threadIdx.x&63;
  float v[7];
  for(int i=0;i<7;i++) v[i]=b2f(x[row*D+lane+64*i]);
  for(int rep=0;rep<=twice;rep++){
    float s=0; for(int i=0;i<7;i++) s+=v[i];
    for(int o=32;o;o>>=1) s+=__shfl_xor(s,o);
    float m=s/D; float q=0; for(int i=0;i<7;i++) q+=(v[i]-m)*(v[i]-m);
    for(int o=32;o;o>>=1) q+=__shfl_xor(q,o);
    float r=rsqrtf(q/D+1e-5f);
    for(int i=0;i<7;i++) v[i]=b2f(f2b((v[i]-m)*r));
  }
  for(int i=0;i<7;i++) h[row*D+lane+64*i]=f2b(v[i]);
}
__global__ void gemm_k(const bf16* A,int sam,int sak,const bf16* B,int M,int N,int K,bf16* C,const bf16* res,const bf16* bias,int mode){
  __shared__ float As[32][33], Bs[32][33];
  int tx=threadIdx.x&31, ty=threadIdx.x>>5;
  int m0=blockIdx.y*32,n0=blockIdx.x*32;
  float acc[4]={0,0,0,0};
  for(int k0=0;k0<K;k0+=32){
    for(int i=0;i<4;i++){int r=ty+8*i;
      if(sak==1) As[r][tx]=b2f(A[(size_t)(m0+r)*sam+(k0+tx)]); else As[tx][r]=b2f(A[(size_t)(m0+tx)*sam+(size_t)(k0+r)*sak]);
      Bs[r][tx]=b2f(B[(size_t)(k0+r)*N+n0+tx]);}
    __syncthreads();
    for(int k=0;k<32;k++){float b=Bs[k][tx]; for(int i=0;i<4;i++) acc[i]+=As[ty+8*i][k]*b;}
    __syncthreads();
  }
  for(int i=0;i<4;i++){int m=m0+ty+8*i,n=n0+tx; float a=acc[i];
    if(mode==0) C[(size_t)m*N+n]=f2b(a);
    else if(mode==1) C[(size_t)m*N+n]=f2b(b2f(res[(size_t)m*N+n])+b2f(f2b(a)));
    else if(mode==2){float u=b2f(f2b(a))+b2f(bias[n]); u=b2f(f2b(u)); float t=0.7978845608f*(u+0.044715f*u*u*u); C[(size_t)m*N+n]=f2b(0.5f*u*(1.f+tanhf(t)));}
    else {float y=b2f(f2b(b2f(f2b(a))+b2f(bias[n]))); C[(size_t)n*M+m]=f2b(b2f(res[(size_t)n*M+m])+y);}
  }
}
__global__ void attn_k(const bf16* qkv, bf16* o){
  int q=blockIdx.x*4+(threadIdx.x>>6), h=blockIdx.y, lane=threadIdx.x&63;
  float qv[DH]; for(int d=0;d<DH;d++) qv[d]=b2f(qkv[(size_t)q*3*D+h*DH+d]);
  float s[4]; float mx=-1e30f;
  for(int i=0;i<4;i++){int k=q-lane-64*i; s[i]=-1e30f; if(k>=0){float a=0; for(int d=0;d<DH;d++) a+=qv[d]*b2f(qkv[(size_t)k*3*D+D+h*DH+d]); s[i]=a*0.1889822365f;} mx=fmaxf(mx,s[i]);}
  for(int o2=32;o2;o2>>=1) mx=fmaxf(mx,__shfl_xor(mx,o2));
  float l=0; for(int i=0;i<4;i++){s[i]=__expf(s[i]-mx); l+=s[i];}
  for(int o2=32;o2;o2>>=1) l+=__shfl_xor(l,o2);
  for(int d=0;d<DH;d++){float a=0; for(int i=0;i<4;i++){int k=q-lane-64*i; if(k>=0) a+=s[i]*b2f(qkv[(size_t)k*3*D+2*D+h*DH+d]);}
    for(int o2=32;o2;o2>>=1) a+=__shfl_xor(a,o2);
    if(lane==0) o[(size_t)q*D+h*DH+d]=f2b(a/l);}
}
extern "C" void kernel_launch(void* const* d_in, const int* in_sizes, int n_in, void* d_out, int out_size, void* d_ws, size_t ws_size, hipStream_t stream){
  const bf16* x0=(const bf16*)d_in[0]; const bf16* wqkv=(const bf16*)d_in[1]; const bf16* wo=(const bf16*)d_in[2];
  const bf16* w1=(const bf16*)d_in[4]; const bf16* b1=(const bf16*)d_in[5]; const bf16* w2=(const bf16*)d_in[6]; const bf16* b2=(const bf16*)d_in[7];
  char* ws=(char*)d_ws;
  bf16* h=(bf16*)ws; bf16* qkv=(bf16*)(ws+(4<<20)); bf16* o=(bf16*)(ws+(12<<20)); bf16* x1=(bf16*)(ws+(16<<20)); bf16* u=(bf16*)(ws+(20<<20)); bf16* x2=(bf16*)(ws+(24<<20));
  const bf16* x=x0;
  for(int l=0;l<2;l++){
    ln_k<<<S/4,256,0,stream>>>(x,h,0);
    gemm_k<<<dim3(3*D/32,S/32),256,0,stream>>>(h,D,1,wqkv,S,3*D,D,qkv,nullptr,nullptr,0);
    attn_k<<<dim3(S/4,H),256,0,stream>>>(qkv,o);
    gemm_k<<<dim3(D/32,S/32),256,0,stream>>>(o,D,1,wo,S,D,D,x1,x,nullptr,1);
    ln_k<<<S/4,256,0,stream>>>(x1,h,1);
    gemm_k<<<dim3(F/32,D/32),256,0,stream>>>(h,1,D,w1,D,F,S,u,nullptr,b1,2);
    bf16* xo = l==0? x2 : (bf16*)d_out;
    gemm_k<<<dim3(S/32,D/32),256,0,stream>>>(u,F,1,w2,D,S,F,xo,x1,b2,3);
    x=xo;
  }
}
```

```cpp
#include <hip/hip_runtime.h>
#include <hip/hip_bf16.h>
#include <type_traits>

typedef unsigned u32;
typedef unsigned long long u64;
typedef u32 v4u __attribute__((ext_vector_type(4)));
typedef u32 v2u __attribute__((ext_vector_type(2)));
typedef float f32x16 __attribute__((ext_vector_type(16)));
typedef float f32x4 __attribute__((ext_vector_type(4)));
typedef float f32x2 __attribute__((ext_vector_type(2)));
typedef __bf16 bf16x8 __attribute__((ext_vector_type(8)));
typedef __bf16 bf16x2 __attribute__((ext_vector_type(2)));
typedef short s16x4 __attribute__((ext_vector_type(4)));
#define LAS __attribute__((address_space(3)))
#define GAS __attribute__((address_space(1)))
#define DI __device__ __forceinline__
#define RLX_AGENT __ATOMIC_RELAXED, __HIP_MEMORY_SCOPE_AGENT
#define SB() __builtin_amdgcn_sched_barrier(0)
typedef __amdgpu_buffer_rsrc_t rsrc_t;
typedef LAS unsigned char* lptr;

#define DBG_STAMP 0
#define DBG_WAVE 0
#define LA4 6
#define LA5 6
#define LA1 12
#define LA3 6
#define ROT4 (ft & 1)
#define ROT5 (st & 1)
#define ROT1 (head & 1)
#define WARM2 1

constexpr int S_ = 2048, D_ = 448, F_ = 1792;
constexpr u32 WS_EPOCH = 0, WS_TMO = 128;
constexpr u32 WS_FLAGS = 4096;
constexpr u32 WS_STATS = WS_FLAGS + 2 * 5 * 256 * 128;
constexpr u32 STATS_L = 64 * 14 * 512;
constexpr u32 WS_KV = 2u << 20, KV_L = 4u << 20;
constexpr u32 WS_OF = WS_KV + 2 * KV_L, OF_L = 2u << 20;
constexpr u32 WS_HF = WS_OF + 2 * OF_L, HF_L = 2u << 20;
constexpr u32 WS_UF = WS_HF + 2 * HF_L, UF_L = 2u << 20;
constexpr u32 WS_XF = WS_UF + 2 * UF_L;
constexpr u32 WS_SQ = WS_XF + 64 * 28 * 1024;
constexpr u32 WS_END = WS_XF + (2u << 20);
constexpr u32 WS_DBG = WS_END;
static_assert(WS_STATS + 2 * STATS_L <= WS_KV, "ws map");
constexpr u32 L_RING = 0, RING_W = 24576;
constexpr u32 L_INBOX = 98304, INBOX_W = 12288;
constexpr u32 L_WO = 86016;
constexpr u32 L_CSP = 147456;
constexpr u32 L_CSF = L_CSP + 1536;
constexpr u32 L_MR = L_CSF + 2048;
constexpr u32 L_TR = L_MR + 1024;
constexpr u32 L_END = L_TR + 4 * 2304;
constexpr u32 kSpinCap = 1u << 16;
constexpr float EPS = 1e-5f;

struct Params {
  const void* x; const void* wqkv; const void* wo; const void* w1; const void* b1; const void* w2; const void* b2;
  void* out; unsigned char* ws;
};

DI f32x16 mfma(v4u a, v4u b, f32x16 c) {
  return __builtin_amdgcn_mfma_f32_32x32x16_bf16(__builtin_bit_cast(bf16x8, a), __builtin_bit_cast(bf16x8, b), c, 0, 0, 0);
}
DI f32x16 qk1(v4u k0, v4u q0, f32x16 ci) {
  f32x16 c;
  asm volatile("v_mfma_f32_32x32x16_bf16 %0, %1, %2, %3" : "=&v"(c) : "a"(k0), "v"(q0), "v"(ci));
  return c;
}
DI f32x16 qk1z(v4u k0, v4u q0) {
  f32x16 c;
  asm volatile("v_mfma_f32_32x32x16_bf16 %0, %1, %2, 0" : "=&v"(c) : "a"(k0), "v"(q0));
  return c;
}
DI void qk2(f32x16& c, v4u k1, v4u q1) { asm volatile("v_mfma_f32_32x32x16_bf16 %0, %1, %2, %0" : "+v"(c) : "a"(k1), "v"(q1)); }
DI f32x16 qk_own(v4u k0, v4u k1, v4u q0, v4u q1) {
  f32x16 c;
  asm volatile("s_nop 1\n\tv_mfma_f32_32x32x16_bf16 %0, %1, %3, 0\n\tv_mfma_f32_32x32x16_bf16 %0, %2, %4, %0" : "=&v"(c) : "v"(k0), "v"(k1), "v"(q0), "v"(q1));
  return c;
}
DI f32x16 qk_own_c(v4u k0, v4u k1, v4u q0, v4u q1, f32x16 ci) {
  f32x16 c;
  asm volatile("s_nop 1\n\tv_mfma_f32_32x32x16_bf16 %0, %1, %3, %5\n\tv_mfma_f32_32x32x16_bf16 %0, %2, %4, %0" : "=&v"(c) : "v"(k0), "v"(k1), "v"(q0), "v"(q1), "v"(ci));
  return c;
}
DI void tie(f32x16& c) { asm volatile("" : "+v"(c)); }
DI void tie_far(f32x16& c) { asm volatile("s_nop 7\n\ts_nop 4" : "+v"(c)); }
DI void nop2() { asm volatile("s_nop 1"); }
DI u32 pk(float lo, float hi) { f32x2 v = {lo, hi}; return __builtin_bit_cast(u32, __builtin_convertvector(v, bf16x2)); }
DI float asf(u32 v) { return __builtin_bit_cast(float, v); }
DI u32 asu(float v) { return __builtin_bit_cast(u32, v); }
DI bf16x2 asbf2(u32 v) { return __builtin_bit_cast(bf16x2, v); }
DI float blo(u32 v) { return __builtin_bit_cast(float, v << 16); }
DI float bhi(u32 v) { return __builtin_bit_cast(float, v & 0xffff0000u); }
DI void bar() { asm volatile("s_waitcnt lgkmcnt(0)\n\ts_barrier" ::: "memory"); }
DI void drain() { asm volatile("s_waitcnt vmcnt(0)" ::: "memory"); }
DI void lgk0() { asm volatile("s_waitcnt lgkmcnt(0)" ::: "memory"); }
template <u32 SI, u32 LI> DI void dma16(rsrc_t rs, u32 voff, u32 soff, u32 lds) {
  u32 t;
  asm volatile("s_add_u32 m0, %1, %5\n\ts_add_u32 %0, %4, %6\n\tbuffer_load_dwordx4 %2, %3, %0 offen lds"
               : "=&s"(t) : "s"(lds), "v"(voff), "s"(rs), "s"(soff), "i"(LI), "i"(SI) : "memory", "scc");
}
template <int K> DI v4u lda(rsrc_t rs, u32 voff, u32 soff) {
  v4u d; u32 t;
  asm volatile("s_add_u32 %1, %4, %5\n\tbuffer_load_dwordx4 %0, %2, %3, %1 offen offset:%6 sc1"
               : "=v"(d), "=&s"(t) : "v"(voff), "s"(rs), "s"(soff), "i"((K >> 2) * 4096), "i"((K & 3) * 1024) : "scc");
  return d;
}
template <int IMM> DI v4u ldp(rsrc_t rs, u32 voff) {
  v4u d;
  asm volatile("buffer_load_dwordx4 %0, %1, %2, 0 offen offset:%3" : "=v"(d) : "v"(voff), "s"(rs), "i"(IMM));
  return d;
}
template <int IMM> DI u32 lda1(rsrc_t rs, u32 voff, u32 soff) {
  u32 d;
  asm volatile("buffer_load_dword %0, %1, %2, %3 offen offset:%4 sc1" : "=v"(d) : "v"(voff), "s"(rs), "s"(soff), "i"(IMM));
  return d;
}
template <int N> DI void vmw0() { asm volatile("s_waitcnt vmcnt(%0)" ::"i"(N) : "memory"); }
template <int ST> DI void quad_stage(v4u& a, v4u& c) {
  u32 o0, o1, o2, o3, p0, p1, p2, p3;
  const u32 a0 = a[0], a1 = a[1], a2 = a[2], a3 = a[3], c0 = c[0], c1 = c[1], c2 = c[2], c3 = c[3];
#define QP(D, S, K) "v_cndmask_b32_dpp " D ", " S ", " K ", vcc quad_perm:[%c16,%c17,%c18,%c19] row_mask:0xf bank_mask:0xf\n\t"
  asm volatile("s_mov_b32 vcc_lo, %20\n\ts_mov_b32 vcc_hi, %20\n\ts_nop 0\n\t" QP("%0", "%12", "%8") QP("%1", "%13", "%9") QP("%2", "%14", "%10") QP("%3", "%15", "%11")
               "s_not_b64 vcc, vcc\n\t" QP("%4", "%8", "%12") QP("%5", "%9", "%13") QP("%6", "%10", "%14") QP("%7", "%11", "%15")
               : "=&v"(o0), "=&v"(o1), "=&v"(o2), "=&v"(o3), "=&v"(p0), "=&v"(p1), "=&v"(p2), "=&v"(p3)
               : "v"(a0), "v"(a1), "v"(a2), "v"(a3), "v"(c0), "v"(c1), "v"(c2), "v"(c3),
                 "i"(0 ^ ST), "i"(1 ^ ST), "i"(2 ^ ST), "i"(3 ^ ST), "i"(ST == 1 ? 0x55555555 : 0x33333333) : "vcc", "scc");
#undef QP
  a = v4u{o0, o1, o2, o3}; c = v4u{p0, p1, p2, p3};
}
template <int N> DI void vmw(v4u& a) { asm volatile("s_waitcnt vmcnt(%1)" : "+v"(a) : "i"(N)); }
template <int N> DI void vmw(v4u& a, v4u& b, v4u& c, v4u& d) { asm volatile("s_waitcnt vmcnt(%4)" : "+v"(a), "+v"(b), "+v"(c), "+v"(d) : "i"(N)); }
template <int N> DI void vmw(v4u& a, v4u& b) { asm volatile("s_waitcnt vmcnt(%2)" : "+v"(a), "+v"(b) : "i"(N)); }
template <int I, int N, class Fn> DI void sfor(Fn&& f) { if constexpr (I < N) { f(std::integral_constant<int, I>{}); sfor<I + 1, N>(f); } }
DI u32 opq(u32 v) { asm volatile("" : "+s"(v)); return v; }
DI u32 opqv(u32 v) { asm volatile("" : "+v"(v)); return v; }
DI v2u trd(lptr p) { return __builtin_bit_cast(v2u, __builtin_amdgcn_ds_read_tr16_b64_v4i16((LAS s16x4*)p)); }
DI v4u trd2(lptr p0, lptr p1) { v2u a = trd(p0), b = trd(p1); return v4u{a.x, a.y, b.x, b.y}; }
DI v4u ld_sc1(rsrc_t rs, u32 voff, u32 soff) { return __builtin_amdgcn_raw_buffer_load_b128(rs, voff, soff, 16); }
DI void st_sc1(v4u v, rsrc_t rs, u32 voff, u32 soff) { __builtin_amdgcn_raw_buffer_store_b128(v, rs, voff, soff, 16); }
DI rsrc_t mkrs(const void* p, u32 bytes) { return __builtin_amdgcn_make_buffer_rsrc((void*)p, 0, bytes, 0x00020000); }
DI float half_add(float v) {
  auto r = __builtin_amdgcn_permlane32_swap(asu(v), asu(v), false, false);
  const u32 a = r[0], b = r[1];
  return asf(a) + asf(b);
}
DI float half_max(float v) {
  auto r = __builtin_amdgcn_permlane32_swap(asu(v), asu(v), false, false);
  const u32 a = r[0], b = r[1];
  return fmaxf(asf(a), asf(b));
}
DI bool spin_out(unsigned char* ws, u32& spins, u32 tl) {
  ++spins;
  if ((spins & 63u) == 0u) {
    GAS u32* tmo = (GAS u32*)(ws + WS_TMO);
    if (__hip_atomic_load(tmo, RLX_AGENT) == tl) return true;
    if (spins > kSpinCap) { __hip_atomic_store(tmo, tl, RLX_AGENT); return true; }
  }
  return false;
}
DI void wait_flags(unsigned char* ws, u32 off, u64 tag) {
  GAS u64* fp = (GAS u64*)(ws + off);
  for (u32 spins = 0;;) {
    const u64 v = __hip_atomic_load(fp, RLX_AGENT);
    if (__all(v == tag)) break;
    if (spin_out(ws, spins, (u32)tag)) break;
  }
  asm volatile("" ::: "memory");
}
DI void publish(unsigned char* ws, u32 off, u64 tag) {
  asm volatile("s_waitcnt vmcnt(0) lgkmcnt(0)\n\ts_barrier" ::: "memory");
  if (threadIdx.x == 0) __hip_atomic_store((GAS u64*)(ws + off), tag, RLX_AGENT);
  asm volatile("" ::: "memory");
}
DI u32 flag_off(int l, int type, int idx) { return WS_FLAGS + (u32)(((l * 5 + type) << 8) + idx) * 128u; }

#define STAMP(i) do {} while (0)

DI f32x16 reduce4(lptr L, const f32x16 (&acc)[4], int w, int lane) {
#pragma unroll
  for (int q = 1; q < 4; ++q) {
    lptr dst = L + L_INBOX + (u32)(w ^ q) * INBOX_W + (q - 1) * 4096 + lane * 16;
#pragma unroll
    for (int i = 0; i < 4; ++i)
      *(LAS f32x4*)(dst + i * 1024) = f32x4{acc[q][4 * i], acc[q][4 * i + 1], acc[q][4 * i + 2], acc[q][4 * i + 3]};
  }
  bar();
  f32x16 r = acc[0];
  lptr src = L + L_INBOX + (u32)w * INBOX_W + lane * 16;
#pragma unroll
  for (int q = 0; q < 3; ++q)
#pragma unroll
    for (int i = 0; i < 4; ++i) {
      const f32x4 v = *(LAS f32x4*)(src + q * 4096 + i * 1024);
      r[4 * i] += v[0]; r[4 * i + 1] += v[1]; r[4 * i + 2] += v[2]; r[4 * i + 3] += v[3];
    }
  return r;
}

template <int P0, int P1, u32 RS> DI void w_issue(rsrc_t rs, u32 voff, u32 soff, u32 ring) {
  sfor<P0, P1>([&](auto I) { constexpr int i = I; dma16<i * 8 * RS, (i % 24) * 1024>(rs, voff, soff, ring); });
}
template <int P0, int P1, u32 RS, int HP> DI void w_issue2(rsrc_t rs, u32 voff, u32 s0, u32 s1, u32 ring) {
  sfor<P0, P1>([&](auto I) { constexpr int i = I; dma16<(i % HP) * 8 * RS, (i % 24) * 1024>(rs, voff, i < HP ? s0 : s1, ring); });
}
template <int K0, int K1, int TSTEP, int NR> DI void w_take(lptr t0, lptr t1, v4u (&bw)[NR][2]) {
#pragma unroll
  for (int kk = K0; kk < K1; ++kk) {
    const int sl = (kk % 12) * 2048;
    bw[kk][0] = trd2(t0 + sl, t0 + sl + TSTEP);
    bw[kk][1] = trd2(t1 + sl, t1 + sl + TSTEP);
  }
}
template <int NR> DI void w_undef(v4u (&bw)[NR][2], int k0, int k1) {
#pragma unroll
  for (int kk = 0; kk < NR; ++kk) if (kk >= k0 && kk < k1) { asm volatile("" : "=v"(bw[kk][0])); asm volatile("" : "=v"(bw[kk][1])); }
}

template <int NK, int NR, int TSTEP, bool WA, int LA, class Tail>
DI void gemm_main(rsrc_t ws, u32 voff, u32 a0, u32 a1, u32 rot, lptr t0, lptr t1, const v4u (&bw)[NR][2], f32x16 (&acc)[4], Tail&& tail) {
  static_assert(NK - NR == 12 && NK - LA >= 13, "ring is 12 slots and must be consumed before the tail");
  v4u a[NK][2];
  constexpr int HK = NK / 2;
  const u32 b0[2] = {a0 + rot * (HK * 1024), a0 + (rot ^ 1u) * (HK * 1024)}, b1[2] = {a1 + rot * (HK * 1024), a1 + (rot ^ 1u) * (HK * 1024)};
  sfor<0, LA>([&](auto I) { constexpr int n = I, k = (n + NR) % NK; a[n][0] = lda<k % HK>(ws, voff, b0[k / HK]); a[n][1] = lda<k % HK>(ws, voff, b1[k / HK]); });
  v4u wl[12][2];
  wl[0][0] = trd2(t0 + (NR % 12) * 2048, t0 + (NR % 12) * 2048 + TSTEP); wl[0][1] = trd2(t1 + (NR % 12) * 2048, t1 + (NR % 12) * 2048 + TSTEP);
#pragma unroll
  for (int q = 0; q < 4; ++q)
#pragma unroll
    for (int i = 0; i < 16; ++i) acc[q][i] = 0.f;
  wl[1][0] = trd2(t0 + ((NR + 1) % 12) * 2048, t0 + ((NR + 1) % 12) * 2048 + TSTEP); wl[1][1] = trd2(t1 + ((NR + 1) % 12) * 2048, t1 + ((NR + 1) % 12) * 2048 + TSTEP);
  SB();
  sfor<0, NK>([&](auto I) {
    constexpr int n = I, kn = (n + LA + NR) % NK, sl = ((NR + n + 2) % 12) * 2048;
    vmw<2 * LA - 2>(a[n][0], a[n][1]);
    v4u w0, w1;
    if constexpr (n < 12) { w0 = wl[n][0]; w1 = wl[n][1]; } else { w0 = bw[n - 12][0]; w1 = bw[n - 12][1]; }
    acc[1] = WA ? mfma(w0, a[n][1], acc[1]) : mfma(a[n][0], w1, acc[1]);
    SB();
    if constexpr (n + LA < NK) a[n + LA][0] = lda<kn % HK>(ws, voff, b0[kn / HK]); else tail(std::integral_constant<int, 2 * (n + LA - NK)>{});
    SB();
    acc[2] = WA ? mfma(w1, a[n][0], acc[2]) : mfma(a[n][1], w0, acc[2]);
    SB();
    if constexpr (n + 2 < 12) wl[n + 2][0] = trd2(t0 + sl, t0 + sl + TSTEP);
    SB();
    acc[3] = WA ? mfma(w1, a[n][1], acc[3]) : mfma(a[n][1], w1, acc[3]);
    SB();
    if constexpr (n + LA < NK) a[n + LA][1] = lda<kn % HK>(ws, voff, b1[kn / HK]); else tail(std::integral_constant<int, 2 * (n + LA - NK) + 1>{});
    SB();
    acc[0] = WA ? mfma(w0, a[n][0], acc[0]) : mfma(a[n][0], w0, acc[0]);
    SB();
    if constexpr (n + 2 < 12) wl[n + 2][1] = trd2(t1 + sl, t1 + sl + TSTEP);
    SB();
  });
}

__global__ __launch_bounds__(256, 1) void fused_fwd(Params p) {
  extern __shared__ __attribute__((aligned(16))) unsigned char smem[];
  lptr L = (lptr)smem;
  const u32 lds0 = (u32)(size_t)L;
#define IDS \
  const int tid = (int)opqv(threadIdx.x), lane = tid & 63, r = lane & 31, h = lane >> 5; \
  const int w = (int)opq(w_), b = (int)opq(blockIdx.x), g = b & 7, bi = b >> 3, pj = g >> 1; \
  const int T = 2 * g + (bi >> 4), head = bi & 15; \
  const int d3 = (bi * 43) >> 7, m3 = bi - 3 * d3; \
  const int st = 8 * pj + ((g & 1) ? d3 : (bi >> 2)), dt = (g & 1) ? 4 + m3 : (bi & 3); \
  const int ft = 7 * pj + ((g & 1) ? (bi >> 2) : d3), dt4 = (g & 1) ? (bi & 3) : 4 + m3; \
  const int wa = w >> 1, wb = w & 1; \
  const int ch16 = (lane >> 4) & 1, tq = (lane & 15) >> 2, tp = lane & 3; \
  const u32 ringw = L_RING + (u32)w * RING_W; \
  const u32 dmarow = lane >> 3, dmach = ((lane & 7) ^ (((lane >> 4) & 1) << 2)) * 16; \
  const u32 sw = (u32)((tq >> 1) & 1) << 2, cpart = 2 * ch16 + (tp >> 1); \
  (void)r; (void)h; (void)T; (void)head; (void)st; (void)dt; (void)ft; (void)dt4; (void)wa; (void)wb; (void)ch16; (void)tq; (void)tp; (void)ringw; (void)dmarow; (void)dmach; (void)sw; (void)cpart;
  const u32 w_ = __builtin_amdgcn_readfirstlane(threadIdx.x >> 6);
  const bool p35 = (blockIdx.x & 1) == 0 || (blockIdx.x >> 3) < 24;
  const bool p4 = (blockIdx.x >> 3) < ((blockIdx.x & 1) ? 28u : 21u);

  const rsrc_t rws = mkrs(p.ws, WS_END);
#define WQ_OFFS \
  u32 wqo[3]; \
  _Pragma("unroll") for (int q = 0; q < 3; ++q) { \
    const u32 cg = q * 64 + lane, row = cg / 12, ch = cg % 12; \
    wqo[q] = row * 2688u + (ch >> 2) * 896u + (ch & 3) * 16u + (u32)(head * 56 - (head & 1) * 8); \
  } \
  const u32 wqs = (u32)(8 * w) * 43008u;

  const u64 e0 = __hip_atomic_load((GAS u64*)(p.ws + WS_EPOCH), RLX_AGENT);
  u64 tag = 0;
  u32 xres[8];
  v4u wf[8][2];
#pragma unroll
  for (int i = 0; i < 8; ++i) { xres[i] = 0; asm volatile("" : "=a"(wf[i][0])); asm volatile("" : "=a"(wf[i][1])); }

#pragma unroll 1
  for (int l = 0; l < 2; ++l) {
    STAMP(0);
    const rsrc_t rw1 = mkrs(p.w1, p4 ? S_ * F_ * 2 : 0);
#define W1_ARGS rw1, dmarow * 3584u + dmach + (u32)ft * 128u, (u32)(512 * w + 256 * ROT4) * 3584u, (u32)(512 * w + 256 * (ROT4 ^ 1)) * 3584u, lds0 + ringw
    {
    IDS
    const int tb = 4 * T + w;
    const u32 shift = (head & 1) * 8;
    const u32 rot = l ? ROT1 : 0;
    lptr wtb = L + (32 * h + tq) * 192 + shift + ch16 * 32 + tp * 8;
    lptr wt3[3] = {wtb + rot * 36864, wtb + (rot ^ 1u) * 36864, wtb + 73728};
    v4u X[28];
    f32x16 kt, vv;
#pragma unroll
    for (int i = 0; i < 16; ++i) { kt[i] = 0.f; vv[i] = 0.f; }
    float ssq;
    v4u wk[28], wv[28], wq[28];
    constexpr int PD = 3;
#define WF(k, seg) trd2(wt3[(k) / 12] + (((k) % 12) >> 2) * 12288 + ((k) & 3) * 1536 + (seg) * 64, wt3[(k) / 12] + (((k) % 12) >> 2) * 12288 + ((k) & 3) * 1536 + (seg) * 64 + 768)
    auto kv_head = [&]() {
#pragma unroll
      for (int k = 0; k < PD; ++k) { wk[k] = WF(k, 1); wv[k] = WF(k, 2); }
      SB();
    };
    auto kv_step = [&](auto I, auto&& mem) {
      constexpr int k = I;
      kt = mfma(wk[k], X[k], kt);
      SB();
      if constexpr (k + PD < 28) wk[k + PD] = WF(k + PD, 1); else wq[2 * (k + PD - 28)] = WF(2 * (k + PD - 28), 0);
      mem();
      SB();
      vv = mfma(X[k], wv[k], vv);
      SB();
      if constexpr (k + PD < 28) wv[k + PD] = WF(k + PD, 2); else wq[2 * (k + PD - 28) + 1] = WF(2 * (k + PD - 28) + 1, 0);
      SB();
    };
    if (l == 0) {
      const rsrc_t rx = mkrs(p.x, S_ * D_ * 2);
      const u32 vo = (u32)(tb * 32 + (r & 28)) * 896u + (u32)(4 * h + (lane & 3)) * 16u;
      sfor<0, 28>([&](auto I) { constexpr int k = I; X[k] = ldp<(k & 3) * 896 + (k >> 2) * 128>(rx, vo); });
      {
        WQ_OFFS
        (void)wqs;
        const rsrc_t rwq = mkrs(p.wqkv, D_ * 3 * D_ * 2);
        sfor<0, 21>([&](auto I) { constexpr int i = I; dma16<(i / 3) * 43008u, i * 1024>(rwq, wqo[i % 3], (u32)(7 * w) * 43008u, lds0 + (u32)(7 * w) * 3072u); });
      }
      float sq4[4] = {0.f, 0.f, 0.f, 0.f};
      sfor<0, 7>([&](auto I) {
        constexpr int G = I;
        vmw<21 + 24 - 4 * G>(X[4 * G], X[4 * G + 1], X[4 * G + 2], X[4 * G + 3]);
        quad_stage<1>(X[4 * G], X[4 * G + 1]); quad_stage<1>(X[4 * G + 2], X[4 * G + 3]);
        quad_stage<2>(X[4 * G], X[4 * G + 2]); quad_stage<2>(X[4 * G + 1], X[4 * G + 3]);
#pragma unroll
        for (int i = 0; i < 16; ++i) { const bf16x2 xx = asbf2(X[4 * G + (i >> 2)][i & 3]); sq4[i & 3] = __builtin_amdgcn_fdot2_f32_bf16(xx, xx, sq4[i & 3], false); }
        asm volatile("" : "+v"(sq4[0]), "+v"(sq4[1]), "+v"(sq4[2]), "+v"(sq4[3]));
        SB();
      });
      ssq = (sq4[0] + sq4[1]) + (sq4[2] + sq4[3]);
      const v4u ones = {0x3F803F80u, 0x3F803F80u, 0x3F803F80u, 0x3F803F80u};
      f32x16 c[3];
#pragma unroll
      for (int s = 0; s < 3; ++s)
#pragma unroll
        for (int i = 0; i < 16; ++i) c[s][i] = 0.f;
      lptr wtw = L + (112 * w + 8 * h + tq) * 192 + shift + ch16 * 32 + tp * 8;
      sfor<0, 7>([&](auto I) {
        constexpr int G = I;
        vmw0<18 - 3 * G>();
        v4u f[3];
#pragma unroll
        for (int s = 0; s < 3; ++s) f[s] = trd2(wtw + G * 3072 + s * 64, wtw + G * 3072 + s * 64 + 768);
        SB();
#pragma unroll
        for (int s = 0; s < 3; ++s) c[s] = mfma(ones, f[s], c[s]);
        SB();
      });
      tag = (((u64)__builtin_amdgcn_readfirstlane((u32)(e0 >> 32)) << 32) | (u64)__builtin_amdgcn_readfirstlane((u32)e0)) + 1ull;
      {
        lptr c28 = L + (112 * w + lane) * 192 + 64 + shift + 56;
        *(LAS unsigned short*)c28 = 0x3F80;
        if (lane < 48) *(LAS unsigned short*)(c28 + 64 * 192) = 0x3F80;
      }
      *(LAS float*)(L + L_CSP + w * 384 + lane * 4) = h ? c[1][0] : c[0][0];
      *(LAS float*)(L + L_CSP + w * 384 + 256 + r * 4) = c[2][0];
      bar();
      kv_head();
      float a0 = 0.f, a1 = 0.f;
#pragma unroll
      for (int v = 0; v < 4; ++v) { a0 += *(LAS float*)(L + L_CSP + v * 384 + lane * 4); a1 += *(LAS float*)(L + L_CSP + v * 384 + 256 + r * 4); }
      *(LAS float*)(L + L_CSF + w * 512 + lane * 4) = a0;
      *(LAS float*)(L + L_CSF + w * 512 + 256 + r * 4) = a1;
      STAMP(1);
      const rsrc_t rwo = mkrs(p.wo, D_ * D_ * 2);
      const u32 wov = dmarow * 896u + dmach + (u32)dt * 128u, wos = (u32)(112 * w) * 896u, wol = lds0 + L_WO + (u32)w * 15360u;
      SB();
      sfor<0, 28>([&](auto I) { constexpr int k = I; kv_step(I, [&]() { if constexpr (k < 15) w_issue<k, k + 1, 896u>(rwo, wov, wos, wol); }); });
      {
        const u32 xo = (u32)((2 * st + wb) * 32 + r) * 896u + (u32)(dt * 64 + wa * 32 + 4 * h) * 2u;
#pragma unroll
        for (int i = 0; i < 4; ++i) {
          const v2u v = __builtin_amdgcn_raw_buffer_load_b64(rx, xo + i * 16, 0, 0);
          xres[2 * i] = v.x; xres[2 * i + 1] = v.y;
        }
      }
      if (head < 14) st_sc1(v4u{0u, 0u, 0u, 0u}, rws, WS_STATS + h * STATS_L + (u32)((tb * 14 + head) * 512 + r * 16), 0);
    } else {
      drain();
      lptr c28 = L + (128 * w + lane) * 192 + 64 + shift + 56;
      *(LAS unsigned short*)c28 = 0x3F80;
      *(LAS unsigned short*)(c28 + 64 * 192) = 0x3F80;
      bar();
      kv_head();
      wait_flags(p.ws, flag_off(0, 4, (2 * T + (w >> 1)) * 8 + (lane < 7 ? lane : 0)), tag);
      STAMP(1);
      drain();
      const u32 xb = WS_XF + (u32)(tb * 28) * 1024u;
      const u32 xb3[3] = {xb + rot * 12288, xb + (rot ^ 1u) * 12288, xb + 24576};
      const u32 sqv = (u32)(7 * h) * 128u + r * 4, sqs = WS_SQ + (u32)tb * 1792u;
      u32 sp[7];
      sfor<0, LA1>([&](auto I) { constexpr int k = I; X[k] = lda<k % 12>(rws, lane * 16, xb3[k / 12]); });
      sfor<0, 28>([&](auto I) {
        constexpr int k = I;
        vmw<(k + LA1 <= 35) ? LA1 - 1 : 34 - k>(X[k]);
        kv_step(I, [&]() {
          if constexpr (k + LA1 < 28) X[k + LA1] = lda<(k + LA1) % 12>(rws, lane * 16, xb3[(k + LA1) / 12]);
          else if constexpr (k + LA1 < 35) sp[k + LA1 - 28] = lda1<(k + LA1 - 28) * 128>(rws, sqv, sqs);
        });
      });
      asm volatile("s_waitcnt vmcnt(0)" : "+v"(sp[0]), "+v"(sp[1]), "+v"(sp[2]), "+v"(sp[3]), "+v"(sp[4]), "+v"(sp[5]), "+v"(sp[6]));
      ssq = ((asf(sp[0]) + asf(sp[1])) + (asf(sp[2]) + asf(sp[3]))) + ((asf(sp[4]) + asf(sp[5])) + asf(sp[6]));
    }
    STAMP(18);
    float mean, rstd;
    {
      const float sum = half_add(h ? kt[12] : 0.f);
      const float sq = half_add(ssq);
      mean = sum * (1.f / 448.f);
      const float var = fmaxf(sq * (1.f / 448.f) - mean * mean, 0.f);
      rstd = __builtin_amdgcn_rsqf(var + EPS);
    }
    const float mr = mean * rstd;
    *(LAS float*)(L + L_MR + w * 256 + h * 128 + r * 4) = h ? mr : rstd;
    const u32 kvbase = WS_KV + l * KV_L + (u32)head * (64u * 4096u);
    lptr kvl = L + L_INBOX + lane * 16;
    v4u K0, K1, V0, V1;
    {
      u32 o[8];
#pragma unroll
      for (int i = 0; i < 4; ++i) {
        const f32x4 cs = *(LAS f32x4*)(L + L_CSF + w * 512 + 128 + (8 * i + 4 * h) * 4);
        o[2 * i] = pk(kt[4 * i] * rstd - mr * cs[0], kt[4 * i + 1] * rstd - mr * cs[1]);
        o[2 * i + 1] = pk(kt[4 * i + 2] * rstd - mr * cs[2], kt[4 * i + 3] * rstd - mr * cs[3]);
      }
      if (h) { o[6] = 0u; o[7] = 0u; }
      K0 = v4u{o[0], o[1], o[2], o[3]}; K1 = v4u{o[4], o[5], o[6], o[7]};
      st_sc1(K0, rws, lane * 16, kvbase + tb * 4096);
      st_sc1(K1, rws, lane * 16 + 1024, kvbase + tb * 4096);
      const float csv = *(LAS float*)(L + L_CSF + w * 512 + 256 + r * 4);
#pragma unroll
      for (int i = 0; i < 4; ++i) {
        const f32x4 rr = *(LAS f32x4*)(L + L_MR + w * 256 + (8 * i + 4 * h) * 4);
        const f32x4 mm = *(LAS f32x4*)(L + L_MR + w * 256 + 128 + (8 * i + 4 * h) * 4);
        o[2 * i] = pk(vv[4 * i] * rr[0] - mm[0] * csv, vv[4 * i + 1] * rr[1] - mm[1] * csv);
        o[2 * i + 1] = pk(vv[4 * i + 2] * rr[2] - mm[2] * csv, vv[4 * i + 3] * rr[3] - mm[3] * csv);
      }
      if (r >= 28) {
#pragma unroll
        for (int i = 0; i < 8; ++i) o[i] = 0x3F803F80u;
      }
      V0 = v4u{o[0], o[1], o[2], o[3]}; V1 = v4u{o[4], o[5], o[6], o[7]};
      st_sc1(V0, rws, lane * 16 + 2048, kvbase + tb * 4096);
      st_sc1(V1, rws, lane * 16 + 3072, kvbase + tb * 4096);
    }
    STAMP(19);
    if (l == 0) {
      vmw0<4>();
      lptr t0 = L + L_WO + w * 15360 + (4 * h + tq) * 128 + (((4 * wa + cpart) ^ sw) * 16) + (tp & 1) * 8;
      lptr t1 = L + L_WO + w * 15360 + (4 * h + tq) * 128 + (((4 * (wa ^ 1) + cpart) ^ sw) * 16) + (tp & 1) * 8;
#pragma unroll
      for (int k = 0; k < 8; ++k) {
        const int ro = (28 * (k >> 1) + 16 * (k & 1)) * 128;
        wf[k][0] = trd2(t0 + ro, t0 + ro + 1024); wf[k][1] = trd2(t1 + ro, t1 + ro + 1024);
      }
    }
    publish(p.ws, flag_off(l, 0, T * 16 + head), tag);
    STAMP(2);
    {
      lptr own = kvl + (8 + w) * 4096;
      *(LAS v4u*)own = K0; *(LAS v4u*)(own + 1024) = K1; *(LAS v4u*)(own + 2048) = V0; *(LAS v4u*)(own + 3072) = V1;
    }
    v4u Q0, Q1;
    {
      f32x16 qt;
#pragma unroll
      for (int i = 0; i < 16; ++i) qt[i] = 0.f;
      sfor<0, 28>([&](auto I) {
        constexpr int k = I;
        qt = mfma(wq[k], X[k], qt);
        SB();
        if constexpr (k + 2 * PD < 28) wq[k + 2 * PD] = WF(k + 2 * PD, 0);
        SB();
      });
      const float sc = 0.18898223650461362f * 1.4426950408889634f;
      const float rs = rstd * sc, ms = mr * sc;
      u32 o[8];
#pragma unroll
      for (int i = 0; i < 4; ++i) {
        const f32x4 cs = *(LAS f32x4*)(L + L_CSF + w * 512 + (8 * i + 4 * h) * 4);
        o[2 * i] = pk(qt[4 * i] * rs - ms * cs[0], qt[4 * i + 1] * rs - ms * cs[1]);
        o[2 * i + 1] = pk(qt[4 * i + 2] * rs - ms * cs[2], qt[4 * i + 3] * rs - ms * cs[3]);
      }
      if (h) { o[6] = 0x00003F80u; o[7] = 0u; }
      Q0 = v4u{o[0], o[1], o[2], o[3]}; Q1 = v4u{o[4], o[5], o[6], o[7]};
    }
    {
      u32 em[16], pm[8];
      f32x16 c0;
#pragma unroll
      for (int i = 0; i < 16; ++i) { const int key = (i & 3) + 8 * (i >> 2) + 4 * h; em[i] = key > r ? 0xffffffffu : 0u; c0[i] = key > r ? 0.f : -1e30f; }
#pragma unroll
      for (int i = 0; i < 8; ++i) pm[i] = (em[2 * i] & 0x0000ffffu) | (em[2 * i + 1] & 0xffff0000u);
      float m4[4] = {-3e38f, -3e38f, -3e38f, -3e38f};
      {
        f32x16 s8 = qk_own(K0, K1, Q0, Q1);
        tie_far(s8);
#pragma unroll
        for (int i = 0; i < 16; ++i) m4[i & 3] = fmaxf(m4[i & 3], em[i] ? -1e30f : s8[i]);
      }
#pragma unroll
      for (int i = 0; i < 16; ++i) asm volatile("" : "+v"(em[i]));
#pragma unroll
      for (int i = 0; i < 8; ++i) asm volatile("" : "+v"(pm[i]));
      asm volatile("" : "+v"(c0), "+v"(m4[0]), "+v"(m4[1]), "+v"(m4[2]), "+v"(m4[3]));
      const int tsrc = T - 2 + (w >> 1);
      wait_flags(p.ws, flag_off(l, 0, (tsrc < 0 ? T : tsrc) * 16 + head), tag);
      STAMP(3);
      {
        const u32 so = tsrc >= 0 ? kvbase + (u32)(4 * T - 8 + 2 * w) * 4096u : 0x40000000u;
        v4u t[8];
#pragma unroll
        for (int i = 0; i < 4; ++i) t[i] = ld_sc1(rws, lane * 16 + (i & 1) * 1024, so + (i >> 1) * 4096);
#pragma unroll
        for (int i = 0; i < 4; ++i) t[4 + i] = ld_sc1(rws, lane * 16 + 2048 + (i & 1) * 1024, so + (i >> 1) * 4096);
        SB();
        if (tsrc < 0 && h) { t[1][2] = 0xF149u; t[3][2] = 0xF149u; }
        lptr dst = kvl + w * 8192;
#pragma unroll
        for (int i = 0; i < 4; ++i) *(LAS v4u*)(dst + (i & 1) * 1024 + (i >> 1) * 4096) = t[i];
#pragma unroll
        for (int i = 0; i < 4; ++i) *(LAS v4u*)(dst + 2048 + (i & 1) * 1024 + (i >> 1) * 4096) = t[4 + i];
      }
      bar();
      STAMP(17);
      lptr kq = kvl + w * 4096;
      v4u Kr[8][2], Vr[8][2];
#pragma unroll
      for (int j = 0; j < 4; ++j) { Kr[j][0] = *(LAS v4u*)(kq + j * 4096); Kr[j][1] = *(LAS v4u*)(kq + j * 4096 + 1024); }
      SB();
      {
        f32x16 s[8];
        auto mx = [&](const f32x16& t, int half) {
#pragma unroll
          for (int i = 0; i < 4; ++i) m4[i] = fmaxf(fmaxf(m4[i], t[8 * half + 2 * i]), t[8 * half + 2 * i + 1]);
        };
        sfor<0, 8>([&](auto I) {
          constexpr int n = I;
          if constexpr (n == 0) s[0] = qk1(Kr[0][0], Q0, c0); else s[n] = qk1z(Kr[n][0], Q0);
          SB();
          if constexpr (n < 4) Kr[n + 4][0] = *(LAS v4u*)(kq + (n + 4) * 4096);
          Vr[n][0] = *(LAS v4u*)(kq + n * 4096 + 2048);
          if constexpr (n >= 2) mx(s[n - 2], 1);
          SB();
          qk2(s[n], Kr[n][1], Q1);
          SB();
          if constexpr (n < 4) Kr[n + 4][1] = *(LAS v4u*)(kq + (n + 4) * 4096 + 1024);
          Vr[n][1] = *(LAS v4u*)(kq + n * 4096 + 3072);
          if constexpr (n >= 1) { tie(s[n - 1]); mx(s[n - 1], 0); }
          SB();
        });
        mx(s[6], 1);
        SB();
        asm volatile("s_nop 7" : "+v"(s[7]));
        mx(s[7], 0); mx(s[7], 1);
      }
      const float nm = -half_max(fmaxf(fmaxf(m4[0], m4[1]), fmaxf(m4[2], m4[3])));
      f32x16 cm, o;
#pragma unroll
      for (int i = 0; i < 16; ++i) { cm[i] = nm; o[i] = 0.f; }
      {
        f32x16 s[9], s8;
        u32 pp[9][8];
        float e[16];
        asm volatile("" : "+v"(cm));
        SB(); nop2();
        s[1] = qk1(Kr[1][0], Q0, cm); qk2(s[1], Kr[1][1], Q1);
        s8 = qk_own_c(K0, K1, Q0, Q1, cm);
        SB();
        sfor<1, 9>([&](auto I) {
          constexpr int c = I, nx = (c + 1) & 7;
          tie(s[c]);
          if constexpr (c == 8) {
            tie(s8);
#pragma unroll
            for (int i = 0; i < 16; ++i) s[8][i] = asf((asu(s[8][i]) & em[i]) | (asu(s8[i]) & ~em[i]));
          }
          auto ex = [&](int q) {
#pragma unroll
            for (int i = 0; i < 4; ++i) e[4 * q + i] = __builtin_amdgcn_exp2f(s[c][4 * q + i]);
          };
          auto cv = [&](int q) { pp[c][2 * q] = pk(e[4 * q], e[4 * q + 1]); pp[c][2 * q + 1] = pk(e[4 * q + 2], e[4 * q + 3]); };
          if constexpr (c < 8) s[c + 1] = qk1(Kr[nx][0], Q0, cm);
          SB(); if constexpr (c < 7) w_issue2<4 * c - 4, 4 * c - 3, 3584u, 32>(W1_ARGS); ex(0); SB();
          if constexpr (c < 8) qk2(s[c + 1], Kr[nx][1], Q1);
          SB(); if constexpr (c < 7) w_issue2<4 * c - 3, 4 * c - 2, 3584u, 32>(W1_ARGS); ex(1); cv(0); SB();
          if constexpr (c > 1) o = mfma(Vr[c - 1][1], v4u{pp[c - 1][4], pp[c - 1][5], pp[c - 1][6], pp[c - 1][7]}, o);
          SB(); if constexpr (c < 7) w_issue2<4 * c - 2, 4 * c - 1, 3584u, 32>(W1_ARGS); ex(2); cv(1); SB();
          if constexpr (c < 8) o = mfma(Vr[c][0], v4u{pp[c][0], pp[c][1], pp[c][2], pp[c][3]}, o);
          else {
            o = mfma(Vr[0][0], v4u{pp[8][0] & pm[0], pp[8][1] & pm[1], pp[8][2] & pm[2], pp[8][3] & pm[3]}, o);
            o = mfma(V0, v4u{pp[8][0] & ~pm[0], pp[8][1] & ~pm[1], pp[8][2] & ~pm[2], pp[8][3] & ~pm[3]}, o);
          }
          SB(); if constexpr (c < 7) w_issue2<4 * c - 1, 4 * c, 3584u, 32>(W1_ARGS); ex(3); cv(2); cv(3); SB();
        });
        o = mfma(Vr[0][1], v4u{pp[8][4] & pm[4], pp[8][5] & pm[5], pp[8][6] & pm[6], pp[8][7] & pm[7]}, o);
        o = mfma(V1, v4u{pp[8][4] & ~pm[4], pp[8][5] & ~pm[5], pp[8][6] & ~pm[6], pp[8][7] & ~pm[7]}, o);
      }
      const float inv = __builtin_amdgcn_rcpf(half_add(h ? o[12] : 0.f));
      u32 oo[8];
#pragma unroll
      for (int i = 0; i < 8; ++i) oo[i] = pk(o[2 * i] * inv, o[2 * i + 1] * inv);
      if (h) { oo[6] = 0u; oo[7] = 0u; }
      const u32 ob = WS_OF + l * OF_L + (u32)(tb * 16 + head) * 2048u;
      st_sc1(v4u{oo[0], oo[1], oo[2], oo[3]}, rws, lane * 16, ob);
      st_sc1(v4u{oo[4], oo[5], oo[6], oo[7]}, rws, lane * 16 + 1024, ob);
      publish(p.ws, flag_off(l, 1, T * 16 + head), tag);
      STAMP(4);
    }
    }
    v4u bw4[20][2];
#define T4_PTRS \
    lptr n0 = L + ringw + (8 * h + tq) * 128 + (((4 * wa + cpart) ^ sw) * 16) + (tp & 1) * 8; \
    lptr n1 = L + ringw + (8 * h + tq) * 128 + (((4 * (wa ^ 1) + cpart) ^ sw) * 16) + (tp & 1) * 8;
    {
      IDS
      T4_PTRS
      w_take<0, 12, 512>(n0, n1, bw4);
      lgk0();
      w_issue2<24, 48, 3584u, 32>(W1_ARGS);
    }
    if (p35) {
      IDS
      wait_flags(p.ws, flag_off(l, 1, (st >> 1) * 16 + 4 * w + (lane & 3)), tag);
      STAMP(5);
      drain();
      const u32 o0 = WS_OF + l * OF_L + (u32)((2 * st + wb) * 16 + 4 * w) * 2048u;
      const u32 o1 = WS_OF + l * OF_L + (u32)((2 * st + (wb ^ 1)) * 16 + 4 * w) * 2048u;
      v4u of[8][2];
      sfor<0, LA3>([&](auto I) { constexpr int k = I; of[k][0] = lda<k>(rws, lane * 16, o0); of[k][1] = lda<k>(rws, lane * 16, o1); });
      f32x16 acc[4];
#pragma unroll
      for (int q = 0; q < 4; ++q)
#pragma unroll
        for (int i = 0; i < 16; ++i) acc[q][i] = 0.f;
      sfor<0, 8>([&](auto I) {
        constexpr int k = I;
        vmw<(k + LA3 <= 8) ? 2 * LA3 - 2 : 14 - 2 * k>(of[k][0], of[k][1]);
        acc[1] = mfma(wf[k][0], of[k][1], acc[1]);
        SB();
        if constexpr (k + LA3 < 8) of[k + LA3][0] = lda<k + LA3>(rws, lane * 16, o0);
        SB();
        acc[2] = mfma(wf[k][1], of[k][0], acc[2]);
        acc[3] = mfma(wf[k][1], of[k][1], acc[3]);
        SB();
        if constexpr (k + LA3 < 8) of[k + LA3][1] = lda<k + LA3>(rws, lane * 16, o1);
        SB();
        acc[0] = mfma(wf[k][0], of[k][0], acc[0]);
        if constexpr (k < 7) SB();
      });
      const f32x16 y = reduce4(L, acc, w, lane);
      float xv[16], s1 = 0.f, s2 = 0.f;
#pragma unroll
      for (int i = 0; i < 8; ++i) {
        xres[i] = pk(blo(xres[i]) + y[2 * i], bhi(xres[i]) + y[2 * i + 1]);
        xv[2 * i] = blo(xres[i]); xv[2 * i + 1] = bhi(xres[i]);
        s1 += xv[2 * i] + xv[2 * i + 1];
        s2 += xv[2 * i] * xv[2 * i] + xv[2 * i + 1] * xv[2 * i + 1];
      }
      s1 = half_add(s1); s2 = half_add(s2);
      const u32 tg = (u32)tag | 1u;
      const u32 sb = WS_STATS + l * STATS_L + (u32)((2 * st + wb) * 14) * 512u;
      if (h == 0) st_sc1(v4u{asu(s1), tg, asu(s2), tg}, rws, r * 16, sb + (u32)(2 * dt + wa) * 512u);
      STAMP(6);
      {
        T4_PTRS
        w_take<12, 20, 512>(n0, n1, bw4);
        lgk0();
        w_issue2<48, 64, 3584u, 32>(W1_ARGS);
      }
      float t1s, t2s;
      for (u32 spins = 0;;) {
        v4u gr[7];
#pragma unroll
        for (int i = 0; i < 7; ++i) gr[i] = ld_sc1(rws, (u32)(7 * h) * 512u + r * 16 + i * 512, sb);
        bool ok = true; t1s = 0.f; t2s = 0.f;
#pragma unroll
        for (int i = 0; i < 7; ++i) { ok &= (gr[i].y == tg) & (gr[i].w == tg); t1s += asf(gr[i].x); t2s += asf(gr[i].z); }
        if (__all(ok)) break;
        asm volatile("" ::: "memory");
        if (spin_out(p.ws, spins, (u32)tag)) break;
      }
      STAMP(7);
      t1s = half_add(t1s); t2s = half_add(t2s);
      const float m1 = t1s * (1.f / 448.f);
      const float var = fmaxf(t2s * (1.f / 448.f) - m1 * m1, 0.f);
      const float r1 = __builtin_amdgcn_rsqf(var + EPS);
      const float r2 = __builtin_amdgcn_rsqf(var * r1 * r1 + EPS);
      const float rr = r1 * r2, mm = m1 * rr;
      lptr tw = L + L_TR + w * 2304;
#pragma unroll
      for (int i = 0; i < 4; ++i)
        *(LAS v2u*)(tw + r * 72 + i * 16 + h * 8) = v2u{pk(xv[4 * i] * rr - mm, xv[4 * i + 1] * rr - mm), pk(xv[4 * i + 2] * rr - mm, xv[4 * i + 3] * rr - mm)};
      lgk0();
      lptr tr = tw + (8 * h + tq) * 72 + ch16 * 32 + tp * 8;
      const u32 hb = WS_HF + l * HF_L + (u32)((2 * dt + wa) * 128 + (2 * st + wb) * 2) * 1024u;
      st_sc1(trd2(tr, tr + 4 * 72), rws, lane * 16, hb);
      st_sc1(trd2(tr + 16 * 72, tr + 20 * 72), rws, lane * 16 + 1024, hb);
      publish(p.ws, flag_off(l, 2, dt * 32 + st), tag);
      STAMP(8);
    } else {
      IDS
      T4_PTRS
      drain();
      w_take<12, 20, 512>(n0, n1, bw4);
      lgk0();
      w_issue2<48, 64, 3584u, 32>(W1_ARGS);
      drain();
    }
    const rsrc_t rw2 = mkrs(p.w2, p35 ? F_ * S_ * 2 : 0);
#define W2_ARGS rw2, dmarow * 4096u + dmach + (u32)st * 128u, (u32)(448 * w + 224 * ROT5) * 4096u, (u32)(448 * w + 224 * (ROT5 ^ 1)) * 4096u, lds0 + ringw
    if (p4) {
      IDS
      T4_PTRS
      const rsrc_t rb1 = mkrs(p.b1, F_ * 2);
      v2u bias[4];
#pragma unroll
      for (int i = 0; i < 4; ++i) bias[i] = __builtin_amdgcn_raw_buffer_load_b64(rb1, (u32)(ft * 64 + wa * 32 + 8 * i + 4 * h) * 2u, 0, 0);
      STAMP(9);
      wait_flags(p.ws, flag_off(l, 2, dt4 * 32 + 8 * w + (lane & 7)), tag);
      STAMP(10);
      asm volatile("s_waitcnt vmcnt(0)" : "+v"(bias[0]), "+v"(bias[1]), "+v"(bias[2]), "+v"(bias[3])::"memory");
      f32x16 acc[4];
      const u32 hb = WS_HF + l * HF_L + (u32)(32 * w) * 1024u;
      gemm_main<32, 20, 512, true, LA4>(rws, lane * 16, hb + (u32)(2 * dt4 + wb) * 131072u, hb + (u32)(2 * dt4 + (wb ^ 1)) * 131072u, ROT4, n0, n1, bw4, acc,
                                        [&](auto I) { constexpr int i = I; w_issue2<i, i + 1, 4096u, 28>(W2_ARGS); });
      STAMP(11);
      const f32x16 y = reduce4(L, acc, w, lane);
      w_issue2<2 * LA4, 24, 4096u, 28>(W2_ARGS);
      u32 o[8];
#pragma unroll
      for (int i = 0; i < 8; ++i) {
        const u32 bb = (i & 1) ? bias[i >> 1].y : bias[i >> 1].x;
        float gv[2];
#pragma unroll
        for (int e = 0; e < 2; ++e) {
          const float u = y[2 * i + e] + (e ? bhi(bb) : blo(bb));
          const float z = u * (-2.302208198f - 0.1029432397f * u * u);
          gv[e] = u * __builtin_amdgcn_rcpf(1.f + __builtin_amdgcn_exp2f(z));
        }
        o[i] = pk(gv[0], gv[1]);
      }
      const u32 ub = WS_UF + l * UF_L + (u32)((2 * dt4 + wb) * 112 + 4 * ft + 2 * wa) * 1024u;
      st_sc1(v4u{o[0], o[1], o[2], o[3]}, rws, lane * 16, ub);
      st_sc1(v4u{o[4], o[5], o[6], o[7]}, rws, lane * 16 + 1024, ub);
      publish(p.ws, flag_off(l, 3, dt4 * 32 + ft), tag);
      STAMP(12);
    } else {
      IDS
      {
        const rsrc_t rwf = mkrs(p.w2, F_ * S_ * 2);
        const int nb = (g & 1) ? 4 : 11, ib = bi - ((g & 1) ? 28 : 21);
        for (int row = ib * 4 + w; row < F_; row += nb * 4 * 8) {
          sfor<0, 8>([&](auto I) { constexpr int i = I; dma16<0, i * 1024>(rwf, lane * 16 + pj * 1024, (u32)(row + i * nb * 4) * 4096u, lds0 + ringw); });
          asm volatile("s_waitcnt vmcnt(16)" ::: "memory");
        }
        drain();
      }
      w_issue2<0, 24, 4096u, 28>(W2_ARGS);
      drain();
    }
    const rsrc_t rwq = mkrs(p.wqkv, l == 0 ? D_ * 3 * D_ * 2 : 0);
    if (p35) {
      IDS
      lptr t0 = L + ringw + (4 * h + tq) * 128 + (((4 * wb + cpart) ^ sw) * 16) + (tp & 1) * 8;
      lptr t1 = L + ringw + (4 * h + tq) * 128 + (((4 * (wb ^ 1) + cpart) ^ sw) * 16) + (tp & 1) * 8;
      v4u bw[16][2];
      w_take<0, 12, 1024>(t0, t1, bw);
      lgk0();
      w_issue2<24, 48, 4096u, 28>(W2_ARGS);
      asm volatile("s_waitcnt vmcnt(16)" ::: "memory");
      w_take<12, 16, 1024>(t0, t1, bw);
      lgk0();
      w_issue2<48, 56, 4096u, 28>(W2_ARGS);
      const rsrc_t rb2 = mkrs(p.b2, S_ * 2);
      u32 bias = __builtin_amdgcn_raw_buffer_load_b16(rb2, (u32)((2 * st + wb) * 32 + r) * 2u, 0, 0);
      STAMP(13);
      wait_flags(p.ws, flag_off(l, 3, dt * 32 + 7 * w + (lane < 7 ? lane : 0)), tag);
      STAMP(14);
      asm volatile("s_waitcnt vmcnt(0)" : "+v"(bias)::"memory");
      WQ_OFFS
      f32x16 acc[4];
      const u32 ub = WS_UF + l * UF_L + (u32)(28 * w) * 1024u;
      gemm_main<28, 16, 1024, false, LA5>(rws, lane * 16, ub + (u32)(2 * dt + wa) * 114688u, ub + (u32)(2 * dt + (wa ^ 1)) * 114688u, ROT5, t0, t1, bw, acc,
                                          [&](auto I) { constexpr int i = I; dma16<(i / 3) * 43008u, i * 1024>(rwq, wqo[i % 3], wqs, lds0 + ringw); });
      STAMP(15);
      const f32x16 y = reduce4(L, acc, w, lane);
      const float bv = blo(bias);
#pragma unroll
      for (int i = 0; i < 8; ++i) xres[i] = pk(blo(xres[i]) + (y[2 * i] + bv), bhi(xres[i]) + (y[2 * i + 1] + bv));
      v4u xo[2];
#pragma unroll
      for (int s = 0; s < 2; ++s) {
        auto a = __builtin_amdgcn_permlane32_swap(xres[4 * s], xres[4 * s + 2], false, false);
        auto c = __builtin_amdgcn_permlane32_swap(xres[4 * s + 1], xres[4 * s + 3], false, false);
        xo[s] = v4u{a[0], c[0], a[1], c[1]};
      }
      if (l == 0) {
        const u32 xb = WS_XF + (u32)((2 * st + wb) * 28 + 4 * dt) * 1024u + wa * 512;
        st_sc1(xo[0], rws, h * 1024 + r * 16, xb);
        st_sc1(xo[1], rws, h * 1024 + r * 16 + 2048, xb);
        float q0 = 0.f, q1 = 0.f;
#pragma unroll
        for (int i = 0; i < 8; i += 2) { q0 = __builtin_amdgcn_fdot2_f32_bf16(asbf2(xres[i]), asbf2(xres[i]), q0, false); q1 = __builtin_amdgcn_fdot2_f32_bf16(asbf2(xres[i + 1]), asbf2(xres[i + 1]), q1, false); }
        const float q = half_add(q0 + q1);
        if (h == 0) __builtin_amdgcn_raw_buffer_store_b32(asu(q), rws, r * 4, WS_SQ + (u32)((2 * st + wb) * 14 + 2 * dt + wa) * 128u, 16);
        publish(p.ws, flag_off(0, 4, st * 8 + dt), tag);
        sfor<2 * LA5, 24>([&](auto I) { constexpr int i = I; dma16<(i / 3) * 43008u, i * 1024>(rwq, wqo[i % 3], wqs, lds0 + ringw); });
      } else {
        const rsrc_t rout = mkrs(p.out, S_ * D_ * 2);
        const u32 oo = (u32)((2 * st + wb) * 32 + r) * 896u + (u32)(dt * 64 + wa * 32 + 8 * h) * 2u;
        __builtin_amdgcn_raw_buffer_store_b128(xo[0], rout, oo, 0, 0);
        __builtin_amdgcn_raw_buffer_store_b128(xo[1], rout, oo + 32, 0, 0);
        if (b == 0 && tid == 0) __hip_atomic_store((GAS u64*)(p.ws + WS_EPOCH), tag, RLX_AGENT);
      }
      STAMP(16);
    } else {
      IDS
      WQ_OFFS
      sfor<0, 24>([&](auto I) { constexpr int i = I; dma16<(i / 3) * 43008u, i * 1024>(rwq, wqo[i % 3], wqs, lds0 + ringw); });
    }
  }
}

extern "C" void kernel_launch(void* const* d_in, const int* in_sizes, int n_in, void* d_out, int out_size, void* d_ws, size_t ws_size,
                              hipStream_t stream) {
  Params p;
  p.x = d_in[0]; p.wqkv = d_in[1]; p.wo = d_in[2]; p.w1 = d_in[4]; p.b1 = d_in[5]; p.w2 = d_in[6]; p.b2 = d_in[7];
  p.out = d_out; p.ws = (unsigned char*)d_ws;
  (void)hipFuncSetAttribute((const void*)fused_fwd, hipFuncAttributeMaxDynamicSharedMemorySize, L_END);
  hipLaunchKernelGGL(fused_fwd, dim3(256), dim3(256), L_END, stream, p);
}
```

```cpp
#include <hip/hip_runtime.h>
#include <hip/hip_bf16.h>
#include <type_traits>

typedef unsigned u32;
typedef unsigned long long u64;
typedef u32 v4u __attribute__((ext_vector_type(4)));
typedef u32 v2u __attribute__((ext_vector_type(2)));
typedef float f32x16 __attribute__((ext_vector_type(16)));
typedef float f32x4 __attribute__((ext_vector_type(4)));
typedef float f32x2 __attribute__((ext_vector_type(2)));
typedef __bf16 bf16x8 __attribute__((ext_vector_type(8)));
typedef __bf16 bf16x2 __attribute__((ext_vector_type(2)));
typedef short s16x4 __attribute__((ext_vector_type(4)));
#define LAS __attribute__((address_space(3)))
#define GAS __attribute__((address_space(1)))
#define DI __device__ __forceinline__
#define RLX_AGENT __ATOMIC_RELAXED, __HIP_MEMORY_SCOPE_AGENT
#define SB() __builtin_amdgcn_sched_barrier(0)
typedef __amdgpu_buffer_rsrc_t rsrc_t;
typedef LAS unsigned char* lptr;

#define DBG_STAMP 0
#define DBG_WAVE 0
#define POLL_GAP "3"
#define TOUCH 1
#define QP0 12
#define NB 10
#define LA4 6
#define LA5 6
#define LA1 12
#define LA3 6
#define ROT4 0
#define ROT5 0
#define ROT1 (head & 1)
#define WARM2 1

constexpr int S_ = 2048, D_ = 448, F_ = 1792;
constexpr u32 WS_EPOCH = 0, WS_TMO = 128;
constexpr u32 WS_FLAGS = 4096;
constexpr u32 WS_STATS = WS_FLAGS + 2 * 5 * 256 * 128;
constexpr u32 STATS_L = 64 * 14 * 512;
constexpr u32 WS_KV = 2u << 20, KV_L = 4u << 20;
constexpr u32 WS_OF = WS_KV + 2 * KV_L, OF_L = 2u << 20;
constexpr u32 WS_HF = WS_OF + 2 * OF_L, HF_L = 2u << 20;
constexpr u32 WS_UF = WS_HF + 2 * HF_L, UF_L = 2u << 20;
constexpr u32 WS_XF = WS_UF + 2 * UF_L;
constexpr u32 WS_SQ = WS_XF + 64 * 28 * 1024;
constexpr u32 WS_WI = WS_XF + (2u << 20);
constexpr u32 WS_END = WS_WI + (2u << 20);
constexpr u32 WS_DBG = WS_END;
static_assert(WS_STATS + 2 * STATS_L <= WS_KV, "ws map");
constexpr u32 L_RING = 0, RING_W = 24576;
constexpr u32 L_INBOX = 98304, INBOX_W = 12288;
constexpr u32 L_WQ = 57344;
constexpr u32 L_WO = 86016;
constexpr u32 L_CSP = 147456;
constexpr u32 L_CSF = L_CSP + 1536;
constexpr u32 L_MR = L_CSF + 2048;
constexpr u32 L_TR = L_MR + 1024;
constexpr u32 L_END = L_TR + 4 * 2304;
constexpr u32 kSpinCap = 1u << 16;
constexpr float EPS = 1e-5f;

struct Params {
  const void* x; const void* wqkv; const void* wo; const void* w1; const void* b1; const void* w2; const void* b2;
  void* out; unsigned char* ws;
};

DI f32x16 mfma(v4u a, v4u b, f32x16 c) {
  return __builtin_amdgcn_mfma_f32_32x32x16_bf16(__builtin_bit_cast(bf16x8, a), __builtin_bit_cast(bf16x8, b), c, 0, 0, 0);
}
DI f32x16 qk1(v4u k0, v4u q0, f32x16 ci) {
  f32x16 c;
  asm volatile("v_mfma_f32_32x32x16_bf16 %0, %1, %2, %3" : "=&v"(c) : "a"(k0), "v"(q0), "v"(ci));
  return c;
}
DI f32x16 qk1z(v4u k0, v4u q0) {
  f32x16 c;
  asm volatile("v_mfma_f32_32x32x16_bf16 %0, %1, %2, 0" : "=&v"(c) : "a"(k0), "v"(q0));
  return c;
}
DI void qk2(f32x16& c, v4u k1, v4u q1) { asm volatile("v_mfma_f32_32x32x16_bf16 %0, %1, %2, %0" : "+v"(c) : "a"(k1), "v"(q1)); }
DI f32x16 qk_own(v4u k0, v4u k1, v4u q0, v4u q1) {
  f32x16 c;
  asm volatile("s_nop 1\n\tv_mfma_f32_32x32x16_bf16 %0, %1, %3, 0\n\tv_mfma_f32_32x32x16_bf16 %0, %2, %4, %0" : "=&v"(c) : "v"(k0), "v"(k1), "v"(q0), "v"(q1));
  return c;
}
DI f32x16 qk_own_c(v4u k0, v4u k1, v4u q0, v4u q1, f32x16 ci) {
  f32x16 c;
  asm volatile("s_nop 1\n\tv_mfma_f32_32x32x16_bf16 %0, %1, %3, %5\n\tv_mfma_f32_32x32x16_bf16 %0, %2, %4, %0" : "=&v"(c) : "v"(k0), "v"(k1), "v"(q0), "v"(q1), "v"(ci));
  return c;
}
DI void tie(f32x16& c) { asm volatile("" : "+v"(c)); }
DI void tie_far(f32x16& c) { asm volatile("s_nop 7\n\ts_nop 4" : "+v"(c)); }
DI void nop2() { asm volatile("s_nop 1"); }
DI u32 pk(float lo, float hi) { f32x2 v = {lo, hi}; return __builtin_bit_cast(u32, __builtin_convertvector(v, bf16x2)); }
DI float asf(u32 v) { return __builtin_bit_cast(float, v); }
DI u32 asu(float v) { return __builtin_bit_cast(u32, v); }
DI bf16x2 asbf2(u32 v) { return __builtin_bit_cast(bf16x2, v); }
DI float blo(u32 v) { return __builtin_bit_cast(float, v << 16); }
DI float bhi(u32 v) { return __builtin_bit_cast(float, v & 0xffff0000u); }
DI void bar() { asm volatile("s_waitcnt lgkmcnt(0)\n\ts_barrier" ::: "memory"); }
DI void drain() { asm volatile("s_waitcnt vmcnt(0)" ::: "memory"); }
DI void lgk0() { asm volatile("s_waitcnt lgkmcnt(0)" ::: "memory"); }
template <u32 SI, u32 LI> DI void dma16(rsrc_t rs, u32 voff, u32 soff, u32 lds) {
  u32 t;
  asm volatile("s_add_u32 m0, %1, %5\n\ts_add_u32 %0, %4, %6\n\tbuffer_load_dwordx4 %2, %3, %0 offen lds"
               : "=&s"(t) : "s"(lds), "v"(voff), "s"(rs), "s"(soff), "i"(LI), "i"(SI) : "memory", "scc");
}
template <int K> DI v4u lda(rsrc_t rs, u32 voff, u32 soff) {
  v4u d; u32 t;
  asm volatile("s_add_u32 %1, %4, %5\n\tbuffer_load_dwordx4 %0, %2, %3, %1 offen offset:%6 sc1"
               : "=v"(d), "=&s"(t) : "v"(voff), "s"(rs), "s"(soff), "i"((K >> 2) * 4096), "i"((K & 3) * 1024) : "scc");
  return d;
}
template <int IMM> DI v4u ldp(rsrc_t rs, u32 voff, u32 soff) {
  v4u d;
  asm volatile("buffer_load_dwordx4 %0, %1, %2, %3 offen offset:%4" : "=v"(d) : "v"(voff), "s"(rs), "s"(soff), "i"(IMM));
  return d;
}
template <int IMM> DI v2u ldp2(rsrc_t rs, u32 voff) {
  v2u d;
  asm volatile("buffer_load_dwordx2 %0, %1, %2, 0 offen offset:%3" : "=v"(d) : "v"(voff), "s"(rs), "i"(IMM));
  return d;
}
DI void pin4(float (&v)[4]) { asm volatile("" : "+v"(v[0]), "+v"(v[1]), "+v"(v[2]), "+v"(v[3])); }
template <int IMM> DI u32 lda1(rsrc_t rs, u32 voff, u32 soff) {
  u32 d;
  asm volatile("buffer_load_dword %0, %1, %2, %3 offen offset:%4 sc1" : "=v"(d) : "v"(voff), "s"(rs), "s"(soff), "i"(IMM));
  return d;
}
template <int N> DI void vmw0() { asm volatile("s_waitcnt vmcnt(%0)" ::"i"(N) : "memory"); }
template <int N> DI void vmw(v4u& a) { asm volatile("s_waitcnt vmcnt(%1)" : "+v"(a) : "i"(N)); }
template <int N> DI void vmw(v4u& a, v4u& b, v4u& c, v4u& d) { asm volatile("s_waitcnt vmcnt(%4)" : "+v"(a), "+v"(b), "+v"(c), "+v"(d) : "i"(N)); }
template <int N> DI void vmw(v4u& a, v4u& b) { asm volatile("s_waitcnt vmcnt(%2)" : "+v"(a), "+v"(b) : "i"(N)); }
template <int I, int N, class Fn> DI void sfor(Fn&& f) { if constexpr (I < N) { f(std::integral_constant<int, I>{}); sfor<I + 1, N>(f); } }
DI u32 opq(u32 v) { asm volatile("" : "+s"(v)); return v; }
DI u32 opqv(u32 v) { asm volatile("" : "+v"(v)); return v; }
DI v2u trd(lptr p) { return __builtin_bit_cast(v2u, __builtin_amdgcn_ds_read_tr16_b64_v4i16((LAS s16x4*)p)); }
DI v4u trd2(lptr p0, lptr p1) { v2u a = trd(p0), b = trd(p1); return v4u{a.x, a.y, b.x, b.y}; }
DI v4u ld_sc1(rsrc_t rs, u32 voff, u32 soff) { return __builtin_amdgcn_raw_buffer_load_b128(rs, voff, soff, 16); }
DI void st_sc1(v4u v, rsrc_t rs, u32 voff, u32 soff) { __builtin_amdgcn_raw_buffer_store_b128(v, rs, voff, soff, 16); }
DI rsrc_t mkrs(const void* p, u32 bytes) {
  u32 fl = 0x00020000;
  asm volatile("" : "+s"(p), "+s"(bytes), "+s"(fl));
  return __builtin_amdgcn_make_buffer_rsrc((void*)p, 0, bytes, fl);
}
DI float half_add(float v) {
  auto r = __builtin_amdgcn_permlane32_swap(asu(v), asu(v), false, false);
  const u32 a = r[0], b = r[1];
  return asf(a) + asf(b);
}
DI float half_max(float v) {
  auto r = __builtin_amdgcn_permlane32_swap(asu(v), asu(v), false, false);
  const u32 a = r[0], b = r[1];
  return fmaxf(asf(a), asf(b));
}
DI bool spin_out(unsigned char* ws, u32& spins, u32 tl) {
  ++spins;
  if ((spins & 63u) == 0u) {
    GAS u32* tmo = (GAS u32*)(ws + WS_TMO);
    if (__hip_atomic_load(tmo, RLX_AGENT) == tl) return true;
    if (spins > kSpinCap) { __hip_atomic_store(tmo, tl, RLX_AGENT); return true; }
  }
  return false;
}
struct Poll { v2u a, b, c; };
#define PL(R, O, B) "global_load_dwordx2 " R ", " O ", " B " sc1\n\t"
DI v2u poll_one(unsigned char* ws, u32 off) {
  v2u r;
  asm volatile(PL("%0", "%1", "%2") : "=v"(r) : "v"(off), "s"(ws) : "memory");
  return r;
}
DI void poll_finish(Poll& q, unsigned char* ws, u32 off, u64 tag) {
  u32 left;
#define PC(R) "s_waitcnt vmcnt(2)\n\tv_cmp_ne_u64 vcc, %6, " R "\n\ts_cbranch_vccz 2f\n\t" PL(R, "%4", "%5")
  asm volatile("s_mov_b32 %3, 0x8000\n1:\n\t" PC("%0") PC("%1") PC("%2") "s_sub_u32 %3, %3, 1\n\ts_cbranch_scc0 1b\n2:"
               : "+v"(q.a), "+v"(q.b), "+v"(q.c), "=&s"(left) : "v"(off), "s"(ws), "s"(tag) : "vcc", "scc", "memory");
#undef PC
  if (left == 0xffffffffu) { drain(); __hip_atomic_store((GAS u32*)(ws + WS_TMO), (u32)tag, RLX_AGENT); }
}
DI Poll wait_flags(unsigned char* ws, u32 off, u64 tag) {
  Poll q;
  q.a = poll_one(ws, off); asm volatile("s_sleep " POLL_GAP);
  q.b = poll_one(ws, off); asm volatile("s_sleep " POLL_GAP);
  q.c = poll_one(ws, off);
  poll_finish(q, ws, off, tag);
  return q;
}
DI void poll_done(const Poll& q) { asm volatile("" ::"v"(q.a), "v"(q.b), "v"(q.c)); }
DI u32 touch(rsrc_t rs, u32 voff, u32 soff) {
  u32 d;
  asm volatile("s_nop 4\n\tbuffer_load_dword %0, %1, %2, %3 offen" : "=v"(d) : "v"(voff), "s"(rs), "s"(soff));
  return d;
}
DI void touch_done(u32 d) { asm volatile("" ::"v"(d)); }
DI void publish(unsigned char* ws, u32 off, u64 tag) {
  asm volatile("s_waitcnt vmcnt(0) lgkmcnt(0)\n\ts_barrier" ::: "memory");
  if (threadIdx.x == 0) __hip_atomic_store((GAS u64*)(ws + off), tag, RLX_AGENT);
  asm volatile("" ::: "memory");
}
DI u32 flag_off(int l, int type, int idx) { return WS_FLAGS + (u32)(((l * 5 + type) << 8) + idx) * 128u; }

#define STAMP(i) do {} while (0)

DI f32x16 reduce4(lptr L, const f32x16 (&acc)[4], int w, int lane) {
#pragma unroll
  for (int q = 1; q < 4; ++q) {
    lptr dst = L + L_INBOX + (u32)(w ^ q) * INBOX_W + (q - 1) * 4096 + lane * 16;
#pragma unroll
    for (int i = 0; i < 4; ++i)
      *(LAS f32x4*)(dst + i * 1024) = f32x4{acc[q][4 * i], acc[q][4 * i + 1], acc[q][4 * i + 2], acc[q][4 * i + 3]};
  }
  bar();
  f32x16 r = acc[0];
  lptr src = L + L_INBOX + (u32)w * INBOX_W + lane * 16;
#pragma unroll
  for (int q = 0; q < 3; ++q)
#pragma unroll
    for (int i = 0; i < 4; ++i) {
      const f32x4 v = *(LAS f32x4*)(src + q * 4096 + i * 1024);
      r[4 * i] += v[0]; r[4 * i + 1] += v[1]; r[4 * i + 2] += v[2]; r[4 * i + 3] += v[3];
    }
  return r;
}

template <int P0, int P1, u32 RS> DI void w_issue(rsrc_t rs, u32 voff, u32 soff, u32 ring) {
  sfor<P0, P1>([&](auto I) { constexpr int i = I; dma16<i * 8 * RS, (i % 24) * 1024>(rs, voff, soff, ring); });
}
template <int P0, int P1, u32 RS, int HP> DI void w_issue2(rsrc_t rs, u32 voff, u32 s0, u32 s1, u32 ring) {
  sfor<P0, P1>([&](auto I) { constexpr int i = I; dma16<(i % HP) * 8 * RS, (i % 24) * 1024>(rs, voff, i < HP ? s0 : s1, ring); });
}
template <int K0, int K1, int TSTEP, int NR> DI void w_take(lptr t0, lptr t1, v4u (&bw)[NR][2]) {
#pragma unroll
  for (int kk = K0; kk < K1; ++kk) {
    const int sl = (kk % 12) * 2048;
    bw[kk][0] = trd2(t0 + sl, t0 + sl + TSTEP);
    bw[kk][1] = trd2(t1 + sl, t1 + sl + TSTEP);
  }
}
template <int NR> DI void w_undef(v4u (&bw)[NR][2], int k0, int k1) {
#pragma unroll
  for (int kk = 0; kk < NR; ++kk) if (kk >= k0 && kk < k1) { asm volatile("" : "=v"(bw[kk][0])); asm volatile("" : "=v"(bw[kk][1])); }
}

template <int NK, int NR, int TSTEP, bool WA, int LA, class Tail>
DI void gemm_main(rsrc_t ws, u32 voff, u32 a0, u32 a1, u32 rot, lptr t0, lptr t1, const v4u (&bw)[NR][2], f32x16 (&acc)[4], Tail&& tail) {
  static_assert(NK - NR == 12 && NK - LA >= 13, "ring is 12 slots and must be consumed before the tail");
  v4u a[NK][2];
  constexpr int HK = NK / 2;
  const u32 b0[2] = {a0 + rot * (HK * 1024), a0 + (rot ^ 1u) * (HK * 1024)}, b1[2] = {a1 + rot * (HK * 1024), a1 + (rot ^ 1u) * (HK * 1024)};
  sfor<0, LA>([&](auto I) { constexpr int n = I, k = (n + NR) % NK; a[n][0] = lda<k % HK>(ws, voff, b0[k / HK]); a[n][1] = lda<k % HK>(ws, voff, b1[k / HK]); });
  v4u wl[12][2];
  wl[0][0] = trd2(t0 + (NR % 12) * 2048, t0 + (NR % 12) * 2048 + TSTEP); wl[0][1] = trd2(t1 + (NR % 12) * 2048, t1 + (NR % 12) * 2048 + TSTEP);
#pragma unroll
  for (int q = 0; q < 4; ++q)
#pragma unroll
    for (int i = 0; i < 16; ++i) acc[q][i] = 0.f;
  wl[1][0] = trd2(t0 + ((NR + 1) % 12) * 2048, t0 + ((NR + 1) % 12) * 2048 + TSTEP); wl[1][1] = trd2(t1 + ((NR + 1) % 12) * 2048, t1 + ((NR + 1) % 12) * 2048 + TSTEP);
  SB();
  sfor<0, NK>([&](auto I) {
    constexpr int n = I, kn = (n + LA + NR) % NK, sl = ((NR + n + 2) % 12) * 2048;
    vmw<2 * LA - 2>(a[n][0], a[n][1]);
    v4u w0, w1;
    if constexpr (n < 12) { w0 = wl[n][0]; w1 = wl[n][1]; } else { w0 = bw[n - 12][0]; w1 = bw[n - 12][1]; }
    acc[1] = WA ? mfma(w0, a[n][1], acc[1]) : mfma(a[n][0], w1, acc[1]);
    SB();
    if constexpr (n + LA < NK) a[n + LA][0] = lda<kn % HK>(ws, voff, b0[kn / HK]); else tail(std::integral_constant<int, 2 * (n + LA - NK)>{});
    SB();
    acc[2] = WA ? mfma(w1, a[n][0], acc[2]) : mfma(a[n][1], w0, acc[2]);
    SB();
    if constexpr (n + 2 < 12) wl[n + 2][0] = trd2(t0 + sl, t0 + sl + TSTEP);
    SB();
    acc[3] = WA ? mfma(w1, a[n][1], acc[3]) : mfma(a[n][1], w1, acc[3]);
    SB();
    if constexpr (n + LA < NK) a[n + LA][1] = lda<kn % HK>(ws, voff, b1[kn / HK]); else tail(std::integral_constant<int, 2 * (n + LA - NK) + 1>{});
    SB();
    acc[0] = WA ? mfma(w0, a[n][0], acc[0]) : mfma(a[n][0], w0, acc[0]);
    SB();
    if constexpr (n + 2 < 12) wl[n + 2][1] = trd2(t1 + sl, t1 + sl + TSTEP);
    SB();
  });
}

__global__ __launch_bounds__(256, 1) void fused_fwd(Params p) {
  extern __shared__ __attribute__((aligned(16))) unsigned char smem[];
  lptr L = (lptr)smem;
  const u32 lds0 = (u32)(size_t)L;
#define IDS \
  const int tid = (int)opqv(threadIdx.x), lane = tid & 63, r = lane & 31, h = lane >> 5; \
  const int w = (int)opq(w_), b = (int)opq(blockIdx.x), g = b & 7, bi = b >> 3, pj = g >> 1; \
  const int T = 2 * g + (bi >> 4), head = bi & 15; \
  const int d3 = (bi * 43) >> 7, m3 = bi - 3 * d3; \
  const int st = 8 * pj + ((g & 1) ? d3 : (bi >> 2)), dt = (g & 1) ? 4 + m3 : (bi & 3); \
  const int ft = 7 * pj + ((g & 1) ? (bi >> 2) : d3), dt4 = (g & 1) ? (bi & 3) : 4 + m3; \
  const int wa = w >> 1, wb = w & 1; \
  const int ch16 = (lane >> 4) & 1, tq = (lane & 15) >> 2, tp = lane & 3; \
  const u32 ringw = L_RING + (u32)w * RING_W; \
  const u32 dmarow = lane >> 3, dmach = ((lane & 7) ^ (((lane >> 4) & 1) << 2)) * 16; \
  const u32 sw = (u32)((tq >> 1) & 1) << 2, cpart = 2 * ch16 + (tp >> 1); \
  (void)r; (void)h; (void)T; (void)head; (void)st; (void)dt; (void)ft; (void)dt4; (void)wa; (void)wb; (void)ch16; (void)tq; (void)tp; (void)ringw; (void)dmarow; (void)dmach; (void)sw; (void)cpart;
  const u32 w_ = __builtin_amdgcn_readfirstlane(threadIdx.x >> 6);
  const bool p35 = (blockIdx.x & 1) == 0 || (blockIdx.x >> 3) < 24;
  const bool p4 = (blockIdx.x >> 3) < ((blockIdx.x & 1) ? 28u : 21u);

  const rsrc_t rws = mkrs(p.ws, WS_END);
  v2u e0;
  asm volatile("global_load_dwordx2 %0, %1, %2 sc1" : "=v"(e0) : "v"(WS_EPOCH), "s"(p.ws));
  u64 tag = 0;
  u32 xres[8];
  v4u wf[8][2];
#pragma unroll
  for (int i = 0; i < 8; ++i) { xres[i] = 0; asm volatile("" : "=a"(wf[i][0])); asm volatile("" : "=a"(wf[i][1])); }

#pragma unroll 1
  for (int l = 0; l < 2; ++l) {
    STAMP(0);
    const rsrc_t rw1 = mkrs(p.w1, p4 ? S_ * F_ * 2 : 0);
    const rsrc_t rw2 = mkrs(p.w2, p35 ? F_ * S_ * 2 : 0);
#define W2_ARGS rw2, dmarow * 4096u + dmach + (u32)st * 128u, (u32)(448 * w + 224 * ROT5) * 4096u, (u32)(448 * w + 224 * (ROT5 ^ 1)) * 4096u, lds0 + ringw
#define W1_ARGS rw1, dmarow * 3584u + dmach + (u32)ft * 128u, (u32)(512 * w + 256 * ROT4) * 3584u, (u32)(512 * w + 256 * (ROT4 ^ 1)) * 3584u, lds0 + ringw
    {
    IDS
    const int tb = 4 * T + w;
    const u32 shift = (head & 1) * 8;
    const u32 half = (((tq >> 1) & 1) ^ 1) * 64, col = shift + ch16 * 32 + tp * 8;
    lptr wtk = L + (32 * h + tq) * 128 + half + col, wtv = L + (32 * h + tq) * 128 + (half ^ 64) + col, wtq = L + L_WQ + (32 * h + tq) * 64 + col;
    v4u X[28];
    f32x16 kt, vv;
#pragma unroll
    for (int i = 0; i < 16; ++i) { kt[i] = 0.f; vv[i] = 0.f; }
    float ssq;
    u32 tc2 = 0, tc3 = 0, tc4 = 0;
    v4u wk[28], wv[28], wq[28];
    constexpr int PD = 3;
#define WROW(k) (((k) >> 2) * 64 + ((k) & 3) * 8)
#define WFK(k) trd2(wtk + WROW(k) * 128, wtk + WROW(k) * 128 + 512)
#define WFV(k) trd2(wtv + WROW(k) * 128, wtv + WROW(k) * 128 + 512)
#define WFQ(k) trd2(wtq + WROW(k) * 64, wtq + WROW(k) * 64 + 256)
    auto kv_head = [&]() {
#pragma unroll
      for (int k = 0; k < PD; ++k) { wk[k] = WFK(k); wv[k] = WFV(k); }
      SB();
    };
    auto kv_step = [&](auto I, auto&& mem) {
      constexpr int k = I;
      kt = mfma(wk[k], X[k], kt);
      SB();
      if constexpr (k + PD < 28) wk[k + PD] = WFK(k + PD);
      mem();
      SB();
      vv = mfma(X[k], wv[k], vv);
      SB();
      if constexpr (k + PD < 28) wv[k + PD] = WFV(k + PD);
      SB();
    };
    const v4u ones = {0x3F803F80u, 0x3F803F80u, 0x3F803F80u, 0x3F803F80u};
    if (l == 0) {
      const rsrc_t rx = mkrs(p.x, S_ * D_ * 2), rwq = mkrs(p.wqkv, D_ * 3 * D_ * 2);
      const u32 hoff = (u32)(head * 56) - shift;
      const u32 xv = dmarow * 896u + (lane & 7) * 16u, xs = (u32)(tb * 32) * 896u;
      lptr xb = L + L_WO + w * 15360;
      lptr xw = xb + dmarow * 128, xr = xb + r * 128;
      const u32 xsw = (r >> 1) & 7, xc0 = ((lane & 7) ^ (lane >> 4)) * 16;
      const u32 kvv = dmarow * 2688u + 896u + ((((lane >> 2) ^ (lane >> 4) ^ 1) & 1) * 896u) + (lane & 3) * 16u + hoff;
      const u32 kvs = (u32)(112 * w) * 2688u, kvl0 = lds0 + (u32)(14 * w) * 1024u;
      const rsrc_t rwo = mkrs(p.wo, D_ * D_ * 2);
      const u32 wov = dmarow * 896u + dmach + (u32)dt * 128u, wos = (u32)(112 * w) * 896u, wol = lds0 + L_WO + (u32)w * 15360u;
      const u32 qv = (u32)(lane >> 2) * 2688u + (lane & 3) * 16u + hoff, ql = lds0 + L_WQ + (u32)(7 * w) * 1024u;
      sfor<0, 28>([&](auto I) { constexpr int k = I; X[k] = ldp<(k >> 2) * 128>(rx, xv, xs + (k & 3) * 7168u); });
      sfor<0, 7>([&](auto I) { constexpr int i = I; dma16<i * 8 * 2688, i * 1024>(rwq, kvv, kvs, kvl0); });
      STAMP(24);
      float sq4[4] = {0.f, 0.f, 0.f, 0.f};
      sfor<0, 7>([&](auto I) {
        constexpr int G = I;
        vmw<24 - 4 * G + 7 + 2 * G>(X[4 * G], X[4 * G + 1], X[4 * G + 2], X[4 * G + 3]);
#pragma unroll
        for (int i = 0; i < 4; ++i) *(LAS v4u*)(xw + i * 1024 + (xc0 ^ ((i & 1) * 64))) = X[4 * G + i];
#pragma unroll
        for (int c = 0; c < 4; ++c) X[4 * G + c] = *(LAS v4u*)(xr + (((4 * h + c) ^ xsw) * 16));
        if constexpr (2 * G < 7) dma16<(7 + 2 * G) * 8 * 2688, (7 + 2 * G) * 1024>(rwq, kvv, kvs, kvl0); else dma16<(2 * G - 7) * 16 * 2688, (2 * G - 7) * 1024>(rwq, qv, kvs, ql);
        if constexpr (2 * G + 1 < 7) dma16<(8 + 2 * G) * 8 * 2688, (8 + 2 * G) * 1024>(rwq, kvv, kvs, kvl0); else dma16<(2 * G - 6) * 16 * 2688, (2 * G - 6) * 1024>(rwq, qv, kvs, ql);
#pragma unroll
        for (int i = 0; i < 16; ++i) { const bf16x2 xx = asbf2(X[4 * G + (i >> 2)][i & 3]); sq4[i & 3] = __builtin_amdgcn_fdot2_f32_bf16(xx, xx, sq4[i & 3], false); }
        pin4(sq4);
        SB();
      });
      ssq = (sq4[0] + sq4[1]) + (sq4[2] + sq4[3]);
      STAMP(25);
      f32x16 ck, cv;
#pragma unroll
      for (int i = 0; i < 16; ++i) { ck[i] = 0.f; cv[i] = 0.f; }
      lptr ck0 = L + (112 * w + 8 * h + tq) * 128 + half + col, cv0 = L + (112 * w + 8 * h + tq) * 128 + (half ^ 64) + col;
      vmw0<7>();
      STAMP(26);
      {
        v4u f[14];
#pragma unroll
        for (int i = 0; i < 7; ++i) { f[2 * i] = trd2(ck0 + i * 2048, ck0 + i * 2048 + 512); f[2 * i + 1] = trd2(cv0 + i * 2048, cv0 + i * 2048 + 512); }
        SB();
        sfor<0, 7>([&](auto I) { constexpr int i = I;
          ck = mfma(ones, f[2 * i], ck); SB(); w_issue<2 * i, 2 * i + 1, 896u>(rwo, wov, wos, wol); SB();
          cv = mfma(ones, f[2 * i + 1], cv); SB(); w_issue<2 * i + 1, 2 * i + 2, 896u>(rwo, wov, wos, wol); SB(); });
        w_issue<14, 15, 896u>(rwo, wov, wos, wol);
      }
      {
        const u32 gw = (u32)(b * 4 + w);
        const u32 l2 = gw * 24 + (lane < 24 ? lane : 0), r2 = l2 >> 5, l1 = gw * 21 + (lane < 21 ? lane : 0), r1 = l1 / 28;
        tc2 = touch(mkrs(p.w2, TOUCH ? S_ * F_ * 2 : 0), ((r2 / 192) * 448 + r2 % 192) * 4096u + (l2 & 31) * 128u, 0);
        tc3 = touch(mkrs(p.w1, TOUCH ? S_ * F_ * 2 : 0), ((r1 / 192) * 512 + r1 % 192) * 3584u + (l1 % 28) * 128u, 0);
      }
      STAMP(27);
      asm volatile("" : "+v"(e0));
      tag = (((u64)(u32)__builtin_amdgcn_readfirstlane(e0.y) << 32) | (u64)(u32)__builtin_amdgcn_readfirstlane(e0.x)) + 1ull;
      *(LAS float*)(L + L_CSP + w * 384 + 128 + lane * 4) = h ? cv[0] : ck[0];
      bar();
      {
        lptr c28 = L + (112 * w + lane) * 128 + (((lane >> 1) & 1) ^ 1) * 64 + shift + 56;
        *(LAS unsigned short*)c28 = 0x3F80;
        if (lane < 48) *(LAS unsigned short*)(c28 + 64 * 128) = 0x3F80;
        float a0 = 0.f;
#pragma unroll
        for (int v = 0; v < 4; ++v) a0 += *(LAS float*)(L + L_CSP + v * 384 + 128 + lane * 4);
        *(LAS float*)(L + L_CSF + w * 512 + 128 + lane * 4) = a0;
      }
      bar();
      kv_head();
      STAMP(1);
      SB();
      sfor<0, 28>([&](auto I) { kv_step(I, []() {}); });
      asm volatile("" : "+a"(kt), "+a"(vv));
    } else {
      kv_head();
      const Poll pq = wait_flags(p.ws, flag_off(0, 4, (2 * T + (w >> 1)) * 8 + (lane < 7 ? lane : 0)), tag);
      STAMP(1);
      const u32 xb = WS_XF + (u32)(tb * 28) * 1024u;
      const u32 sqv = (u32)(7 * h) * 128u + r * 4, sqs = WS_SQ + (u32)tb * 1792u;
      u32 sp[7];
      sfor<0, LA1>([&](auto I) { constexpr int k = I; X[k] = lda<k>(rws, lane * 16, xb); });
      sfor<0, 28>([&](auto I) {
        constexpr int k = I;
        vmw<(k + LA1 <= 35) ? LA1 - 1 : 34 - k>(X[k]);
        if constexpr (k == 0) poll_done(pq);
        kv_step(I, [&]() {
          if constexpr (k + LA1 < 28) X[k + LA1] = lda<k + LA1>(rws, lane * 16, xb);
          else if constexpr (k + LA1 < 35) sp[k + LA1 - 28] = lda1<(k + LA1 - 28) * 128>(rws, sqv, sqs);
        });
      });
      asm volatile("s_waitcnt vmcnt(0)" : "+v"(sp[0]), "+v"(sp[1]), "+v"(sp[2]), "+v"(sp[3]), "+v"(sp[4]), "+v"(sp[5]), "+v"(sp[6]));
      asm volatile("" : "+a"(kt), "+a"(vv));
      ssq = ((asf(sp[0]) + asf(sp[1])) + (asf(sp[2]) + asf(sp[3]))) + ((asf(sp[4]) + asf(sp[5])) + asf(sp[6]));
    }
    STAMP(18);
    float mean, rstd;
    {
      const float sum = half_add(h ? kt[12] : 0.f);
      const float sq = half_add(ssq);
      mean = sum * (1.f / 448.f);
      const float var = fmaxf(sq * (1.f / 448.f) - mean * mean, 0.f);
      rstd = __builtin_amdgcn_rsqf(var + EPS);
    }
    const float mr = mean * rstd;
    *(LAS float*)(L + L_MR + w * 256 + h * 128 + r * 4) = h ? mr : rstd;
    const u32 kvbase = WS_KV + l * KV_L + (u32)head * (64u * 4096u);
    lptr kvl = L + L_INBOX + lane * 16;
    v4u K0, K1, V0, V1;
    {
      u32 o[8];
#pragma unroll
      for (int i = 0; i < 4; ++i) {
        const f32x4 cs = *(LAS f32x4*)(L + L_CSF + w * 512 + 128 + (8 * i + 4 * h) * 4);
        o[2 * i] = pk(kt[4 * i] * rstd - mr * cs[0], kt[4 * i + 1] * rstd - mr * cs[1]);
        o[2 * i + 1] = pk(kt[4 * i + 2] * rstd - mr * cs[2], kt[4 * i + 3] * rstd - mr * cs[3]);
      }
      if (h) { o[6] = 0u; o[7] = 0u; }
      K0 = v4u{o[0], o[1], o[2], o[3]}; K1 = v4u{o[4], o[5], o[6], o[7]};
      st_sc1(K0, rws, lane * 16, kvbase + tb * 4096);
      st_sc1(K1, rws, lane * 16 + 1024, kvbase + tb * 4096);
      const float csv = *(LAS float*)(L + L_CSF + w * 512 + 256 + r * 4);
#pragma unroll
      for (int i = 0; i < 4; ++i) {
        const f32x4 rr = *(LAS f32x4*)(L + L_MR + w * 256 + (8 * i + 4 * h) * 4);
        const f32x4 mm = *(LAS f32x4*)(L + L_MR + w * 256 + 128 + (8 * i + 4 * h) * 4);
        o[2 * i] = pk(vv[4 * i] * rr[0] - mm[0] * csv, vv[4 * i + 1] * rr[1] - mm[1] * csv);
        o[2 * i + 1] = pk(vv[4 * i + 2] * rr[2] - mm[2] * csv, vv[4 * i + 3] * rr[3] - mm[3] * csv);
      }
      if (r >= 28) {
#pragma unroll
        for (int i = 0; i < 8; ++i) o[i] = 0x3F803F80u;
      }
      V0 = v4u{o[0], o[1], o[2], o[3]}; V1 = v4u{o[4], o[5], o[6], o[7]};
      st_sc1(V0, rws, lane * 16 + 2048, kvbase + tb * 4096);
      st_sc1(V1, rws, lane * 16 + 3072, kvbase + tb * 4096);
    }
    STAMP(19);
    if (l == 0) {
      vmw0<6>();
      lptr t0 = L + L_WO + w * 15360 + (4 * h + tq) * 128 + (((4 * wa + cpart) ^ sw) * 16) + (tp & 1) * 8;
      lptr t1 = L + L_WO + w * 15360 + (4 * h + tq) * 128 + (((4 * (wa ^ 1) + cpart) ^ sw) * 16) + (tp & 1) * 8;
      lptr cq0 = L + L_WQ + (112 * w + 8 * h + tq) * 64 + col;
      v4u f[7];
#pragma unroll
      for (int i = 0; i < 7; ++i) f[i] = trd2(cq0 + i * 1024, cq0 + i * 1024 + 256);
#pragma unroll
      for (int k = 0; k < 8; ++k) {
        const int ro = (28 * (k >> 1) + 16 * (k & 1)) * 128;
        wf[k][0] = trd2(t0 + ro, t0 + ro + 1024); wf[k][1] = trd2(t1 + ro, t1 + ro + 1024);
      }
      SB();
      f32x16 cq;
#pragma unroll
      for (int i = 0; i < 16; ++i) cq[i] = 0.f;
#pragma unroll
      for (int i = 0; i < 7; ++i) cq = mfma(ones, f[i], cq);
      *(LAS float*)(L + L_CSP + w * 384 + r * 4) = cq[0];
      st_sc1(v4u{0u, 0u, 0u, 0u}, rws, head < 14 ? WS_STATS + h * STATS_L + (u32)((tb * 14 + head) * 512 + r * 16) : 0x40000000u, 0);
    }
    publish(p.ws, flag_off(l, 0, T * 16 + head), tag);
    touch_done(tc2); touch_done(tc3);
    STAMP(2);
#pragma unroll
    for (int k = 0; k < 2 * PD; ++k) wq[k] = WFQ(k);
    {
      lptr own = kvl + (8 + w) * 4096;
      *(LAS v4u*)own = K0; *(LAS v4u*)(own + 1024) = K1; *(LAS v4u*)(own + 2048) = V0; *(LAS v4u*)(own + 3072) = V1;
    }
    if (l == 0) {
      float a0 = 0.f;
#pragma unroll
      for (int v = 0; v < 4; ++v) a0 += *(LAS float*)(L + L_CSP + v * 384 + r * 4);
      *(LAS float*)(L + L_CSF + w * 512 + r * 4) = a0;
      const u32 wib = WS_WI + (u32)head * 86016u + (u32)tb * 1024u;
      st_sc1(*(LAS v4u*)(L + tb * 1024 + lane * 16), rws, lane * 16, wib);
      st_sc1(*(LAS v4u*)(L + (tb < 20 ? tb + 64 : tb) * 1024 + lane * 16), rws, lane * 16, tb < 20 ? wib + 65536u : 0x40000000u);
      const u32 xo = (u32)((2 * st + wb) * 32 + r) * 896u + (u32)(dt * 64 + wa * 32 + 4 * h) * 2u;
      const rsrc_t rx = mkrs(p.x, S_ * D_ * 2);
#pragma unroll
      for (int i = 0; i < 4; ++i) {
        const v2u v = __builtin_amdgcn_raw_buffer_load_b64(rx, xo + i * 16, 0, 0);
        xres[2 * i] = v.x; xres[2 * i + 1] = v.y;
      }
    }
    {
      const int tsrc = T - 2 + (w >> 1);
      const u32 f1 = opqv(flag_off(l, 0, (tsrc < 0 ? T : tsrc) * 16 + head));
      Poll pq;
      f32x16 qt;
#pragma unroll
      for (int i = 0; i < 16; ++i) qt[i] = 0.f;
      SB();
      sfor<0, 28>([&](auto I) {
        constexpr int k = I;
        qt = mfma(wq[k], X[k], qt);
        SB();
        if constexpr (k + 2 * PD < 28) wq[k + 2 * PD] = WFQ(k + 2 * PD);
        if constexpr (k == QP0) pq.a = poll_one(p.ws, f1);
        if constexpr (k == QP0 + 6) pq.b = poll_one(p.ws, f1);
        if constexpr (k == QP0 + 12) pq.c = poll_one(p.ws, f1);
        SB();
      });
      asm volatile("" : "+a"(qt));
      poll_finish(pq, p.ws, f1, tag);
      STAMP(3);
      const u32 so = tsrc >= 0 ? kvbase + (u32)(4 * T - 8 + 2 * w) * 4096u : 0x40000000u;
      v4u t[8];
      t[0] = lda<0>(rws, lane * 16, so); t[1] = lda<1>(rws, lane * 16, so); t[2] = lda<4>(rws, lane * 16, so); t[3] = lda<5>(rws, lane * 16, so);
      t[4] = lda<2>(rws, lane * 16, so); t[5] = lda<3>(rws, lane * 16, so); t[6] = lda<6>(rws, lane * 16, so); t[7] = lda<7>(rws, lane * 16, so);
      SB();
      v4u Q0, Q1;
      {
        const float sc = 0.18898223650461362f * 1.4426950408889634f;
        const float rs = rstd * sc, ms = mr * sc;
        u32 o[8];
#pragma unroll
        for (int i = 0; i < 4; ++i) {
          const f32x4 cs = *(LAS f32x4*)(L + L_CSF + w * 512 + (8 * i + 4 * h) * 4);
          o[2 * i] = pk(qt[4 * i] * rs - ms * cs[0], qt[4 * i + 1] * rs - ms * cs[1]);
          o[2 * i + 1] = pk(qt[4 * i + 2] * rs - ms * cs[2], qt[4 * i + 3] * rs - ms * cs[3]);
        }
        if (h) { o[6] = 0x00003F80u; o[7] = 0u; }
        Q0 = v4u{o[0], o[1], o[2], o[3]}; Q1 = v4u{o[4], o[5], o[6], o[7]};
      }
      f32x16 s8 = qk_own(K0, K1, Q0, Q1);
      u32 em[16], pm[8];
      f32x16 c0;
#pragma unroll
      for (int i = 0; i < 16; ++i) { const int key = (i & 3) + 8 * (i >> 2) + 4 * h; em[i] = key > r ? 0xffffffffu : 0u; c0[i] = asf(~em[i] & 0xF149F2CAu); }
#pragma unroll
      for (int i = 0; i < 8; ++i) pm[i] = (em[2 * i] & 0x0000ffffu) | (em[2 * i + 1] & 0xffff0000u);
#pragma unroll
      for (int i = 0; i < 16; ++i) asm volatile("" : "+v"(em[i]));
#pragma unroll
      for (int i = 0; i < 8; ++i) asm volatile("" : "+v"(pm[i]));
      tie_far(s8);
      float m4[4];
#pragma unroll
      for (int i = 0; i < 16; ++i) s8[i] = asf((em[i] & 0xF149F2CAu) | (~em[i] & asu(s8[i])));
#pragma unroll
      for (int i = 0; i < 4; ++i) m4[i] = fmaxf(fmaxf(s8[4 * i], s8[4 * i + 1]), fmaxf(s8[4 * i + 2], s8[4 * i + 3]));
      asm volatile("" : "+v"(c0), "+v"(m4[0]), "+v"(m4[1]), "+v"(m4[2]), "+v"(m4[3]));
      SB();
      {
        lptr dst = kvl + w * 8192;
        vmw<4>(t[0], t[1], t[2], t[3]);
        poll_done(pq);
        if (tsrc < 0 && h) { t[1][2] = 0xF149u; t[3][2] = 0xF149u; }
#pragma unroll
        for (int i = 0; i < 4; ++i) *(LAS v4u*)(dst + (i & 1) * 1024 + (i >> 1) * 4096) = t[i];
        vmw<0>(t[4], t[5], t[6], t[7]);
#pragma unroll
        for (int i = 0; i < 4; ++i) *(LAS v4u*)(dst + 2048 + (i & 1) * 1024 + (i >> 1) * 4096) = t[4 + i];
      }
      bar();
      {
        const u32 l1 = (u32)(b * 4 + w) * 35 + (lane < 35 ? lane : 0), r1 = l1 / 28;
        tc4 = touch(mkrs(p.w1, (TOUCH && l == 0) ? S_ * F_ * 2 : 0), ((r1 / 320) * 512 + 192 + r1 % 320) * 3584u + (l1 % 28) * 128u, 0);
      }
      STAMP(17);
      lptr kq = kvl + w * 4096;
      v4u Kr[8][2], Vr[8][2];
#pragma unroll
      for (int j = 0; j < 4; ++j) { Kr[j][0] = *(LAS v4u*)(kq + j * 4096); Kr[j][1] = *(LAS v4u*)(kq + j * 4096 + 1024); }
      SB();
      {
        f32x16 s[8];
        auto mx = [&](const f32x16& t, int half) {
#pragma unroll
          for (int i = 0; i < 4; ++i) m4[i] = fmaxf(fmaxf(m4[i], t[8 * half + 2 * i]), t[8 * half + 2 * i + 1]);
        };
        sfor<0, 8>([&](auto I) {
          constexpr int n = I;
          if constexpr (n == 0) s[0] = qk1(Kr[0][0], Q0, c0); else s[n] = qk1z(Kr[n][0], Q0);
          SB();
          if constexpr (n < 4) Kr[n + 4][0] = *(LAS v4u*)(kq + (n + 4) * 4096);
          Vr[n][0] = *(LAS v4u*)(kq + n * 4096 + 2048);
          if constexpr (n >= 2) mx(s[n - 2], 1);
          SB();
          qk2(s[n], Kr[n][1], Q1);
          SB();
          if constexpr (n < 4) Kr[n + 4][1] = *(LAS v4u*)(kq + (n + 4) * 4096 + 1024);
          Vr[n][1] = *(LAS v4u*)(kq + n * 4096 + 3072);
          if constexpr (n >= 1) { tie(s[n - 1]); mx(s[n - 1], 0); }
          SB();
        });
        mx(s[6], 1);
        SB();
        asm volatile("s_nop 7" : "+v"(s[7]));
        mx(s[7], 0); mx(s[7], 1);
      }
      const float nm = -half_max(fmaxf(fmaxf(m4[0], m4[1]), fmaxf(m4[2], m4[3])));
      f32x16 cm, o;
#pragma unroll
      for (int i = 0; i < 16; ++i) { cm[i] = nm; o[i] = 0.f; }
      {
        f32x16 s[9];
        u32 pp[9][8];
        float e[16];
        asm volatile("" : "+v"(cm));
        SB(); nop2();
        s[1] = qk1(Kr[1][0], Q0, cm); qk2(s[1], Kr[1][1], Q1);
        s8 = qk_own_c(K0, K1, Q0, Q1, cm);
        SB();
        sfor<1, 9>([&](auto I) {
          constexpr int c = I, nx = (c + 1) & 7;
          tie(s[c]);
          if constexpr (c == 8) {
            tie(s8);
#pragma unroll
            for (int i = 0; i < 16; ++i) s[8][i] = asf((asu(s[8][i]) & em[i]) | (asu(s8[i]) & ~em[i]));
          }
          auto ex = [&](int q) {
#pragma unroll
            for (int i = 0; i < 4; ++i) e[4 * q + i] = __builtin_amdgcn_exp2f(s[c][4 * q + i]);
          };
          auto cv = [&](int q) { pp[c][2 * q] = pk(e[4 * q], e[4 * q + 1]); pp[c][2 * q + 1] = pk(e[4 * q + 2], e[4 * q + 3]); };
          if constexpr (c < 8) s[c + 1] = qk1(Kr[nx][0], Q0, cm);
          SB(); if constexpr (c < 7) w_issue2<4 * c - 4, 4 * c - 3, 4096u, 28>(W2_ARGS); ex(0); SB();
          if constexpr (c < 8) qk2(s[c + 1], Kr[nx][1], Q1);
          SB(); if constexpr (c < 7) w_issue2<4 * c - 3, 4 * c - 2, 4096u, 28>(W2_ARGS); ex(1); cv(0); SB();
          if constexpr (c > 1) o = mfma(Vr[c - 1][1], v4u{pp[c - 1][4], pp[c - 1][5], pp[c - 1][6], pp[c - 1][7]}, o);
          SB(); if constexpr (c < 7) w_issue2<4 * c - 2, 4 * c - 1, 4096u, 28>(W2_ARGS); ex(2); cv(1); SB();
          if constexpr (c < 8) o = mfma(Vr[c][0], v4u{pp[c][0], pp[c][1], pp[c][2], pp[c][3]}, o);
          else {
            o = mfma(Vr[0][0], v4u{pp[8][0] & pm[0], pp[8][1] & pm[1], pp[8][2] & pm[2], pp[8][3] & pm[3]}, o);
            o = mfma(V0, v4u{pp[8][0] & ~pm[0], pp[8][1] & ~pm[1], pp[8][2] & ~pm[2], pp[8][3] & ~pm[3]}, o);
          }
          SB(); if constexpr (c < 7) w_issue2<4 * c - 1, 4 * c, 4096u, 28>(W2_ARGS); ex(3); cv(2); cv(3); SB();
        });
        o = mfma(Vr[0][1], v4u{pp[8][4] & pm[4], pp[8][5] & pm[5], pp[8][6] & pm[6], pp[8][7] & pm[7]}, o);
        o = mfma(V1, v4u{pp[8][4] & ~pm[4], pp[8][5] & ~pm[5], pp[8][6] & ~pm[6], pp[8][7] & ~pm[7]}, o);
      }
      const float inv = __builtin_amdgcn_rcpf(half_add(h ? o[12] : 0.f));
      u32 oo[8];
#pragma unroll
      for (int i = 0; i < 8; ++i) oo[i] = pk(o[2 * i] * inv, o[2 * i + 1] * inv);
      if (h) { oo[6] = 0u; oo[7] = 0u; }
      const u32 ob = WS_OF + l * OF_L + (u32)(tb * 16 + head) * 2048u;
      st_sc1(v4u{oo[0], oo[1], oo[2], oo[3]}, rws, lane * 16, ob);
      st_sc1(v4u{oo[4], oo[5], oo[6], oo[7]}, rws, lane * 16 + 1024, ob);
      publish(p.ws, flag_off(l, 1, T * 16 + head), tag);
      touch_done(tc4);
      STAMP(4);
    }
    }
    v4u bw4[20][2], bw5[16][2];
#define T5_PTRS \
    lptr t0 = L + ringw + (4 * h + tq) * 128 + (((4 * wb + cpart) ^ sw) * 16) + (tp & 1) * 8; \
    lptr t1 = L + ringw + (4 * h + tq) * 128 + (((4 * (wb ^ 1) + cpart) ^ sw) * 16) + (tp & 1) * 8;
#define T4_PTRS \
    lptr n0 = L + ringw + (8 * h + tq) * 128 + (((4 * wa + cpart) ^ sw) * 16) + (tp & 1) * 8; \
    lptr n1 = L + ringw + (8 * h + tq) * 128 + (((4 * (wa ^ 1) + cpart) ^ sw) * 16) + (tp & 1) * 8;
    {
      IDS
      T5_PTRS
      w_take<0, 12, 1024>(t0, t1, bw5);
      lgk0();
      w_issue2<0, 24, 3584u, 32>(W1_ARGS);
    }
    if (p35) {
      IDS
      const Poll pq = wait_flags(p.ws, flag_off(l, 1, (st >> 1) * 16 + 4 * w + (lane & 3)), tag);
      STAMP(5);
      const u32 o0 = WS_OF + l * OF_L + (u32)((2 * st + wb) * 16 + 4 * w) * 2048u;
      const u32 o1 = WS_OF + l * OF_L + (u32)((2 * st + (wb ^ 1)) * 16 + 4 * w) * 2048u;
      v4u of[8][2];
      sfor<0, LA3>([&](auto I) { constexpr int k = I; of[k][0] = lda<k>(rws, lane * 16, o0); of[k][1] = lda<k>(rws, lane * 16, o1); });
      f32x16 acc[4];
#pragma unroll
      for (int q = 0; q < 4; ++q)
#pragma unroll
        for (int i = 0; i < 16; ++i) acc[q][i] = 0.f;
      sfor<0, 8>([&](auto I) {
        constexpr int k = I;
        vmw<(k + LA3 <= 8) ? 2 * LA3 - 2 : 14 - 2 * k>(of[k][0], of[k][1]);
        if constexpr (k == 0) poll_done(pq);
        acc[1] = mfma(wf[k][0], of[k][1], acc[1]);
        SB();
        if constexpr (k + LA3 < 8) of[k + LA3][0] = lda<k + LA3>(rws, lane * 16, o0);
        SB();
        acc[2] = mfma(wf[k][1], of[k][0], acc[2]);
        acc[3] = mfma(wf[k][1], of[k][1], acc[3]);
        SB();
        if constexpr (k + LA3 < 8) of[k + LA3][1] = lda<k + LA3>(rws, lane * 16, o1);
        SB();
        acc[0] = mfma(wf[k][0], of[k][0], acc[0]);
        if constexpr (k < 7) SB();
      });
      const f32x16 y = reduce4(L, acc, w, lane);
      float xv[16], s1 = 0.f, s2 = 0.f;
#pragma unroll
      for (int i = 0; i < 8; ++i) {
        xres[i] = pk(blo(xres[i]) + y[2 * i], bhi(xres[i]) + y[2 * i + 1]);
        xv[2 * i] = blo(xres[i]); xv[2 * i + 1] = bhi(xres[i]);
        s1 += xv[2 * i] + xv[2 * i + 1];
        s2 += xv[2 * i] * xv[2 * i] + xv[2 * i + 1] * xv[2 * i + 1];
      }
      s1 = half_add(s1); s2 = half_add(s2);
      const u32 tg = (u32)tag | 1u;
      const u32 sb = WS_STATS + l * STATS_L + (u32)((2 * st + wb) * 14) * 512u;
      if (h == 0) st_sc1(v4u{asu(s1), tg, asu(s2), tg}, rws, r * 16, sb + (u32)(2 * dt + wa) * 512u);
      STAMP(6);
      {
        T4_PTRS
        w_take<0, NB, 512>(n0, n1, bw4);
        lgk0();
        w_issue2<24, 24 + 2 * NB, 3584u, 32>(W1_ARGS);
      }
      float t1s, t2s;
      for (u32 spins = 0;;) {
        v4u gr[7];
#pragma unroll
        for (int i = 0; i < 7; ++i) gr[i] = ld_sc1(rws, (u32)(7 * h) * 512u + r * 16 + i * 512, sb);
        bool ok = true; t1s = 0.f; t2s = 0.f;
#pragma unroll
        for (int i = 0; i < 7; ++i) { ok &= (gr[i].y == tg) & (gr[i].w == tg); t1s += asf(gr[i].x); t2s += asf(gr[i].z); }
        if (__all(ok)) break;
        asm volatile("" ::: "memory");
        if (spin_out(p.ws, spins, (u32)tag)) break;
      }
      STAMP(7);
      t1s = half_add(t1s); t2s = half_add(t2s);
      const float m1 = t1s * (1.f / 448.f);
      const float var = fmaxf(t2s * (1.f / 448.f) - m1 * m1, 0.f);
      const float r1 = __builtin_amdgcn_rsqf(var + EPS);
      const float r2 = __builtin_amdgcn_rsqf(var * r1 * r1 + EPS);
      const float rr = r1 * r2, mm = m1 * rr;
      lptr tw = L + L_TR + w * 2304;
#pragma unroll
      for (int i = 0; i < 4; ++i)
        *(LAS v2u*)(tw + r * 72 + i * 16 + h * 8) = v2u{pk(xv[4 * i] * rr - mm, xv[4 * i + 1] * rr - mm), pk(xv[4 * i + 2] * rr - mm, xv[4 * i + 3] * rr - mm)};
      lgk0();
      lptr tr = tw + (8 * h + tq) * 72 + ch16 * 32 + tp * 8;
      const u32 hb = WS_HF + l * HF_L + (u32)((2 * dt + wa) * 128 + (2 * st + wb) * 2) * 1024u;
      st_sc1(trd2(tr, tr + 4 * 72), rws, lane * 16, hb);
      st_sc1(trd2(tr + 16 * 72, tr + 20 * 72), rws, lane * 16 + 1024, hb);
      publish(p.ws, flag_off(l, 2, dt * 32 + st), tag);
      STAMP(8);
    } else {
      IDS
      T4_PTRS
      drain();
      w_take<0, NB, 512>(n0, n1, bw4);
      lgk0();
      w_issue2<24, 24 + 2 * NB, 3584u, 32>(W1_ARGS);
      drain();
    }
    if (p4) {
      IDS
      T4_PTRS
      v2u bias[4];
      {
        const rsrc_t rb1 = mkrs(p.b1, F_ * 2);
        const u32 bo = (u32)(ft * 64 + wa * 32 + 4 * h) * 2u;
        sfor<0, 4>([&](auto I) { constexpr int i = I; bias[i] = ldp2<16 * i>(rb1, bo); });
      }
      w_take<NB, 20, 512>(n0, n1, bw4);
      lgk0();
      w_issue2<24 + 2 * NB, 64, 3584u, 32>(W1_ARGS);
      STAMP(9);
      const Poll pq = wait_flags(p.ws, flag_off(l, 2, dt4 * 32 + 8 * w + (lane & 7)), tag);
      asm volatile("" : "+v"(bias[0]), "+v"(bias[1]), "+v"(bias[2]), "+v"(bias[3]));
      STAMP(10);
      f32x16 acc[4];
      const u32 hb = WS_HF + l * HF_L + (u32)(32 * w) * 1024u;
      gemm_main<32, 20, 512, true, LA4>(rws, lane * 16, hb + (u32)(2 * dt4 + wb) * 131072u, hb + (u32)(2 * dt4 + (wb ^ 1)) * 131072u, ROT4, n0, n1, bw4, acc,
                                        [&](auto I) { constexpr int i = I; w_issue2<24 + i, 25 + i, 4096u, 28>(W2_ARGS); });
      poll_done(pq);
      STAMP(11);
      const f32x16 y = reduce4(L, acc, w, lane);
      u32 o[8];
#pragma unroll
      for (int i = 0; i < 8; ++i) {
        const u32 bb = (i & 1) ? bias[i >> 1].y : bias[i >> 1].x;
        float gv[2];
#pragma unroll
        for (int e = 0; e < 2; ++e) {
          const float u = y[2 * i + e] + (e ? bhi(bb) : blo(bb));
          const float z = u * (-2.302208198f - 0.1029432397f * u * u);
          gv[e] = u * __builtin_amdgcn_rcpf(1.f + __builtin_amdgcn_exp2f(z));
        }
        o[i] = pk(gv[0], gv[1]);
      }
      const u32 ub = WS_UF + l * UF_L + (u32)((2 * dt4 + wb) * 112 + 4 * ft + 2 * wa) * 1024u;
      st_sc1(v4u{o[0], o[1], o[2], o[3]}, rws, lane * 16, ub);
      st_sc1(v4u{o[4], o[5], o[6], o[7]}, rws, lane * 16 + 1024, ub);
      publish(p.ws, flag_off(l, 3, dt4 * 32 + ft), tag);
      STAMP(12);
    } else {
      IDS
      {
        const rsrc_t rwf = mkrs(p.w2, F_ * S_ * 2);
        const int nb = (g & 1) ? 4 : 11, ib = bi - ((g & 1) ? 28 : 21);
        for (int row = ib * 4 + w; row < F_; row += nb * 4 * 8) {
          sfor<0, 8>([&](auto I) { constexpr int i = I; dma16<0, i * 1024>(rwf, lane * 16 + pj * 1024, (u32)(row + i * nb * 4) * 4096u, lds0 + ringw); });
          asm volatile("s_waitcnt vmcnt(16)" ::: "memory");
        }
        drain();
      }
      w_issue2<24, 24 + 2 * LA4, 4096u, 28>(W2_ARGS);
      drain();
    }
    if (p35) {
      IDS
      T5_PTRS
      u32 bias;
      asm volatile("s_nop 4\n\tbuffer_load_ushort %0, %1, %2, 0 offen" : "=v"(bias) : "v"((u32)((2 * st + wb) * 32 + r) * 2u), "s"(mkrs(p.b2, S_ * 2)));
      w_issue2<24 + 2 * LA4, 48, 4096u, 28>(W2_ARGS);
      w_take<12, 16, 1024>(t0, t1, bw5);
      lgk0();
      w_issue2<48, 56, 4096u, 28>(W2_ARGS);
      STAMP(13);
      const Poll pq = wait_flags(p.ws, flag_off(l, 3, dt * 32 + 7 * w + (lane < 7 ? lane : 0)), tag);
      asm volatile("" : "+v"(bias));
      STAMP(14);
      const u32 wis = l == 0 ? WS_WI + (u32)head * 86016u + (u32)w * 21504u : 0x40000000u;
      v4u wi[21];
      f32x16 acc[4];
      const u32 ub = WS_UF + l * UF_L + (u32)(28 * w) * 1024u;
      gemm_main<28, 16, 1024, false, LA5>(rws, lane * 16, ub + (u32)(2 * dt + wa) * 114688u, ub + (u32)(2 * dt + (wa ^ 1)) * 114688u, ROT5, t0, t1, bw5, acc,
                                          [&](auto I) { constexpr int i = I; wi[i] = lda<i>(rws, lane * 16, wis); });
      poll_done(pq);
      STAMP(15);
      const f32x16 y = reduce4(L, acc, w, lane);
      const float bv = blo(bias);
#pragma unroll
      for (int i = 0; i < 8; ++i) xres[i] = pk(blo(xres[i]) + (y[2 * i] + bv), bhi(xres[i]) + (y[2 * i + 1] + bv));
      v4u xo[2];
#pragma unroll
      for (int s = 0; s < 2; ++s) {
        auto a = __builtin_amdgcn_permlane32_swap(xres[4 * s], xres[4 * s + 2], false, false);
        auto c = __builtin_amdgcn_permlane32_swap(xres[4 * s + 1], xres[4 * s + 3], false, false);
        xo[s] = v4u{a[0], c[0], a[1], c[1]};
      }
      if (l == 0) {
        const u32 xb = WS_XF + (u32)((2 * st + wb) * 28 + 4 * dt) * 1024u + wa * 512;
        st_sc1(xo[0], rws, h * 1024 + r * 16, xb);
        st_sc1(xo[1], rws, h * 1024 + r * 16 + 2048, xb);
        float q0 = 0.f, q1 = 0.f;
#pragma unroll
        for (int i = 0; i < 8; i += 2) { q0 = __builtin_amdgcn_fdot2_f32_bf16(asbf2(xres[i]), asbf2(xres[i]), q0, false); q1 = __builtin_amdgcn_fdot2_f32_bf16(asbf2(xres[i + 1]), asbf2(xres[i + 1]), q1, false); }
        const float q = half_add(q0 + q1);
        if (h == 0) __builtin_amdgcn_raw_buffer_store_b32(asu(q), rws, r * 4, WS_SQ + (u32)((2 * st + wb) * 14 + 2 * dt + wa) * 128u, 16);
        publish(p.ws, flag_off(0, 4, st * 8 + dt), tag);
        sfor<2 * LA5, 21>([&](auto I) { constexpr int i = I; wi[i] = lda<i>(rws, lane * 16, wis); });
        sfor<0, 21>([&](auto I) { constexpr int i = I; vmw<(i < 2 * LA5) ? 21 - 2 * LA5 : 20 - i>(wi[i]); *(LAS v4u*)(L + w * 21504 + i * 1024 + lane * 16) = wi[i]; });
        bar();
      } else {
        sfor<0, 2 * LA5>([&](auto I) { constexpr int i = I; vmw<0>(wi[i]); });
        const rsrc_t rout = mkrs(p.out, S_ * D_ * 2);
        const u32 oo = (u32)((2 * st + wb) * 32 + r) * 896u + (u32)(dt * 64 + wa * 32 + 8 * h) * 2u;
        __builtin_amdgcn_raw_buffer_store_b128(xo[0], rout, oo, 0, 0);
        __builtin_amdgcn_raw_buffer_store_b128(xo[1], rout, oo + 32, 0, 0);
        if (b == 0 && tid == 0) __hip_atomic_store((GAS u64*)(p.ws + WS_EPOCH), tag, RLX_AGENT);
      }
      STAMP(16);
    } else if (l == 0) {
      IDS
      const Poll pq = wait_flags(p.ws, flag_off(0, 1, (lane & 15) * 16 + head), tag);
      const u32 wis = WS_WI + (u32)head * 86016u + (u32)w * 21504u;
      v4u wi[21];
      bar();
      sfor<0, 21>([&](auto I) { constexpr int i = I; wi[i] = lda<i>(rws, lane * 16, wis); });
      sfor<0, 21>([&](auto I) { constexpr int i = I; vmw<20 - i>(wi[i]); *(LAS v4u*)(L + w * 21504 + i * 1024 + lane * 16) = wi[i]; });
      poll_done(pq);
      bar();
    }
  }
}

extern "C" void kernel_launch(void* const* d_in, const int* in_sizes, int n_in, void* d_out, int out_size, void* d_ws, size_t ws_size,
                              hipStream_t stream) {
  Params p;
  p.x = d_in[0]; p.wqkv = d_in[1]; p.wo = d_in[2]; p.w1 = d_in[4]; p.b1 = d_in[5]; p.w2 = d_in[6]; p.b2 = d_in[7];
  p.out = d_out; p.ws = (unsigned char*)d_ws;
  (void)hipFuncSetAttribute((const void*)fused_fwd, hipFuncAttributeMaxDynamicSharedMemorySize, L_END);
  hipLaunchKernelGGL(fused_fwd, dim3(256), dim3(256), L_END, stream, p);
}
```

```cpp
#include <hip/hip_runtime.h>
#include <hip/hip_bf16.h>
#include <type_traits>

typedef unsigned u32;
typedef unsigned long long u64;
typedef u32 v4u __attribute__((ext_vector_type(4)));
typedef u32 v2u __attribute__((ext_vector_type(2)));
typedef float f32x16 __attribute__((ext_vector_type(16)));
typedef float f32x4 __attribute__((ext_vector_type(4)));
typedef float f32x2 __attribute__((ext_vector_type(2)));
typedef __bf16 bf16x8 __attribute__((ext_vector_type(8)));
typedef __bf16 bf16x2 __attribute__((ext_vector_type(2)));
typedef short s16x4 __attribute__((ext_vector_type(4)));
#define LAS __attribute__((address_space(3)))
#define GAS __attribute__((address_space(1)))
#define DI __device__ __forceinline__
#define RLX_AGENT __ATOMIC_RELAXED, __HIP_MEMORY_SCOPE_AGENT
#define SB() __builtin_amdgcn_sched_barrier(0)
typedef __amdgpu_buffer_rsrc_t rsrc_t;
typedef LAS unsigned char* lptr;

#define DBG_STAMP 0
#define DBG_WAVE 0
#define POLL_GAP "3"
#define TOUCH 1
#define QP0 12
#define NB 10
#define CODE_TOUCH 1
#define LA4 6
#define LA5 6
#define LA1 12
#define LA3 6
#define ROT4 0
#define ROT5 0
#define ROT1 (head & 1)
#define WARM2 1

constexpr int S_ = 2048, D_ = 448, F_ = 1792;
constexpr u32 WS_EPOCH = 0, WS_TMO = 128;
constexpr u32 WS_FLAGS = 4096;
constexpr u32 WS_STATS = WS_FLAGS + 2 * 5 * 256 * 128;
constexpr u32 STATS_L = 64 * 14 * 512;
constexpr u32 WS_KV = 2u << 20, KV_L = 4u << 20;
constexpr u32 WS_OF = WS_KV + 2 * KV_L, OF_L = 2u << 20;
constexpr u32 WS_HF = WS_OF + 2 * OF_L, HF_L = 2u << 20;
constexpr u32 WS_UF = WS_HF + 2 * HF_L, UF_L = 2u << 20;
constexpr u32 WS_XF = WS_UF + 2 * UF_L;
constexpr u32 WS_SQ = WS_XF + 64 * 28 * 1024;
constexpr u32 WS_WI = WS_XF + (2u << 20);
constexpr u32 WS_END = WS_WI + (2u << 20);
constexpr u32 WS_DBG = WS_END;
static_assert(WS_STATS + 2 * STATS_L <= WS_KV, "ws map");
constexpr u32 L_RING = 0, RING_W = 24576;
constexpr u32 L_INBOX = 98304, INBOX_W = 12288;
constexpr u32 L_WQ = 57344;
constexpr u32 L_WO = 86016;
constexpr u32 L_CSP = 147456;
constexpr u32 L_CSF = L_CSP + 1536;
constexpr u32 L_MR = L_CSF + 2048;
constexpr u32 L_TR = L_MR + 1024;
constexpr u32 L_END = L_TR + 4 * 2304;
constexpr u32 kSpinCap = 1u << 16;
constexpr float EPS = 1e-5f;

struct Params {
  const void* x; const void* wqkv; const void* wo; const void* w1; const void* b1; const void* w2; const void* b2;
  void* out; unsigned char* ws;
};

DI f32x16 mfma(v4u a, v4u b, f32x16 c) {
  return __builtin_amdgcn_mfma_f32_32x32x16_bf16(__builtin_bit_cast(bf16x8, a), __builtin_bit_cast(bf16x8, b), c, 0, 0, 0);
}
DI f32x16 qk1(v4u k0, v4u q0, f32x16 ci) {
  f32x16 c;
  asm volatile("v_mfma_f32_32x32x16_bf16 %0, %1, %2, %3" : "=&v"(c) : "a"(k0), "v"(q0), "v"(ci));
  return c;
}
DI f32x16 qk1z(v4u k0, v4u q0) {
  f32x16 c;
  asm volatile("v_mfma_f32_32x32x16_bf16 %0, %1, %2, 0" : "=&v"(c) : "a"(k0), "v"(q0));
  return c;
}
DI void qk2(f32x16& c, v4u k1, v4u q1) { asm volatile("v_mfma_f32_32x32x16_bf16 %0, %1, %2, %0" : "+v"(c) : "a"(k1), "v"(q1)); }
DI f32x16 qk_own(v4u k0, v4u k1, v4u q0, v4u q1) {
  f32x16 c;
  asm volatile("s_nop 1\n\tv_mfma_f32_32x32x16_bf16 %0, %1, %3, 0\n\tv_mfma_f32_32x32x16_bf16 %0, %2, %4, %0" : "=&v"(c) : "v"(k0), "v"(k1), "v"(q0), "v"(q1));
  return c;
}
DI f32x16 qk_own_c(v4u k0, v4u k1, v4u q0, v4u q1, f32x16 ci) {
  f32x16 c;
  asm volatile("s_nop 1\n\tv_mfma_f32_32x32x16_bf16 %0, %1, %3, %5\n\tv_mfma_f32_32x32x16_bf16 %0, %2, %4, %0" : "=&v"(c) : "v"(k0), "v"(k1), "v"(q0), "v"(q1), "v"(ci));
  return c;
}
DI void tie(f32x16& c) { asm volatile("" : "+v"(c)); }
DI void tie_far(f32x16& c) { asm volatile("s_nop 7\n\ts_nop 4" : "+v"(c)); }
DI void nop2() { asm volatile("s_nop 1"); }
DI u32 pk(float lo, float hi) { f32x2 v = {lo, hi}; return __builtin_bit_cast(u32, __builtin_convertvector(v, bf16x2)); }
DI float asf(u32 v) { return __builtin_bit_cast(float, v); }
DI u32 asu(float v) { return __builtin_bit_cast(u32, v); }
DI bf16x2 asbf2(u32 v) { return __builtin_bit_cast(bf16x2, v); }
DI float blo(u32 v) { return __builtin_bit_cast(float, v << 16); }
DI float bhi(u32 v) { return __builtin_bit_cast(float, v & 0xffff0000u); }
DI void bar() { asm volatile("s_waitcnt lgkmcnt(0)\n\ts_barrier" ::: "memory"); }
DI void drain() { asm volatile("s_waitcnt vmcnt(0)" ::: "memory"); }
DI void lgk0() { asm volatile("s_waitcnt lgkmcnt(0)" ::: "memory"); }
template <u32 SI, u32 LI> DI void dma16(rsrc_t rs, u32 voff, u32 soff, u32 lds) {
  u32 t;
  asm volatile("s_add_u32 m0, %1, %5\n\ts_add_u32 %0, %4, %6\n\tbuffer_load_dwordx4 %2, %3, %0 offen lds"
               : "=&s"(t) : "s"(lds), "v"(voff), "s"(rs), "s"(soff), "i"(LI), "i"(SI) : "memory", "scc");
}
template <int K> DI v4u lda(rsrc_t rs, u32 voff, u32 soff) {
  v4u d; u32 t;
  asm volatile("s_add_u32 %1, %4, %5\n\tbuffer_load_dwordx4 %0, %2, %3, %1 offen offset:%6 sc1"
               : "=v"(d), "=&s"(t) : "v"(voff), "s"(rs), "s"(soff), "i"((K >> 2) * 4096), "i"((K & 3) * 1024) : "scc");
  return d;
}
template <int IMM> DI v4u ldp(rsrc_t rs, u32 voff, u32 soff) {
  v4u d;
  asm volatile("buffer_load_dwordx4 %0, %1, %2, %3 offen offset:%4" : "=v"(d) : "v"(voff), "s"(rs), "s"(soff), "i"(IMM));
  return d;
}
template <int IMM> DI v2u ldp2(rsrc_t rs, u32 voff) {
  v2u d;
  asm volatile("buffer_load_dwordx2 %0, %1, %2, 0 offen offset:%3" : "=v"(d) : "v"(voff), "s"(rs), "i"(IMM));
  return d;
}
DI void pin4(float (&v)[4]) { asm volatile("" : "+v"(v[0]), "+v"(v[1]), "+v"(v[2]), "+v"(v[3])); }
template <int IMM> DI u32 lda1(rsrc_t rs, u32 voff, u32 soff) {
  u32 d;
  asm volatile("buffer_load_dword %0, %1, %2, %3 offen offset:%4 sc1" : "=v"(d) : "v"(voff), "s"(rs), "s"(soff), "i"(IMM));
  return d;
}
template <int N> DI void vmw0() { asm volatile("s_waitcnt vmcnt(%0)" ::"i"(N) : "memory"); }
template <int N> DI void vmw(v4u& a) { asm volatile("s_waitcnt vmcnt(%1)" : "+v"(a) : "i"(N)); }
template <int N> DI void vmw(v4u& a, v4u& b, v4u& c, v4u& d) { asm volatile("s_waitcnt vmcnt(%4)" : "+v"(a), "+v"(b), "+v"(c), "+v"(d) : "i"(N)); }
template <int N> DI void vmw(v4u& a, v4u& b) { asm volatile("s_waitcnt vmcnt(%2)" : "+v"(a), "+v"(b) : "i"(N)); }
template <int I, int N, class Fn> DI void sfor(Fn&& f) { if constexpr (I < N) { f(std::integral_constant<int, I>{}); sfor<I + 1, N>(f); } }
DI u32 opq(u32 v) { asm volatile("" : "+s"(v)); return v; }
DI u32 opqv(u32 v) { asm volatile("" : "+v"(v)); return v; }
DI v2u trd(lptr p) { return __builtin_bit_cast(v2u, __builtin_amdgcn_ds_read_tr16_b64_v4i16((LAS s16x4*)p)); }
DI v4u trd2(lptr p0, lptr p1) { v2u a = trd(p0), b = trd(p1); return v4u{a.x, a.y, b.x, b.y}; }
DI v4u ld_sc1(rsrc_t rs, u32 voff, u32 soff) { return __builtin_amdgcn_raw_buffer_load_b128(rs, voff, soff, 16); }
DI void st_sc1(v4u v, rsrc_t rs, u32 voff, u32 soff) { __builtin_amdgcn_raw_buffer_store_b128(v, rs, voff, soff, 16); }
DI rsrc_t mkrs(const void* p, u32 bytes) {
  u32 fl = 0x00020000;
  asm volatile("" : "+s"(p), "+s"(bytes), "+s"(fl));
  return __builtin_amdgcn_make_buffer_rsrc((void*)p, 0, bytes, fl);
}
DI float half_add(float v) {
  auto r = __builtin_amdgcn_permlane32_swap(asu(v), asu(v), false, false);
  const u32 a = r[0], b = r[1];
  return asf(a) + asf(b);
}
DI float half_max(float v) {
  auto r = __builtin_amdgcn_permlane32_swap(asu(v), asu(v), false, false);
  const u32 a = r[0], b = r[1];
  return fmaxf(asf(a), asf(b));
}
DI bool spin_out(unsigned char* ws, u32& spins, u32 tl) {
  ++spins;
  if ((spins & 63u) == 0u) {
    GAS u32* tmo = (GAS u32*)(ws + WS_TMO);
    if (__hip_atomic_load(tmo, RLX_AGENT) == tl) return true;
    if (spins > kSpinCap) { __hip_atomic_store(tmo, tl, RLX_AGENT); return true; }
  }
  return false;
}
struct Poll { v2u a, b, c; };
#define PL(R, O, B) "global_load_dwordx2 " R ", " O ", " B " sc1\n\t"
DI v2u poll_one(unsigned char* ws, u32 off) {
  v2u r;
  asm volatile(PL("%0", "%1", "%2") : "=v"(r) : "v"(off), "s"(ws) : "memory");
  return r;
}
DI void poll_finish(Poll& q, unsigned char* ws, u32 off, u64 tag) {
  u32 left;
#define PC(R) "s_waitcnt vmcnt(2)\n\tv_cmp_ne_u64 vcc, %6, " R "\n\ts_cbranch_vccz 2f\n\t" PL(R, "%4", "%5")
  asm volatile("s_mov_b32 %3, 0x8000\n1:\n\t" PC("%0") PC("%1") PC("%2") "s_sub_u32 %3, %3, 1\n\ts_cbranch_scc0 1b\n2:"
               : "+v"(q.a), "+v"(q.b), "+v"(q.c), "=&s"(left) : "v"(off), "s"(ws), "s"(tag) : "vcc", "scc", "memory");
#undef PC
  if (left == 0xffffffffu) { drain(); __hip_atomic_store((GAS u32*)(ws + WS_TMO), (u32)tag, RLX_AGENT); }
}
DI Poll wait_flags(unsigned char* ws, u32 off, u64 tag) {
  Poll q;
  q.a = poll_one(ws, off); asm volatile("s_sleep " POLL_GAP);
  q.b = poll_one(ws, off); asm volatile("s_sleep " POLL_GAP);
  q.c = poll_one(ws, off);
  poll_finish(q, ws, off, tag);
  return q;
}
DI void poll_done(const Poll& q) { asm volatile("" ::"v"(q.a), "v"(q.b), "v"(q.c)); }
DI u32 touch(rsrc_t rs, u32 voff, u32 soff) {
  u32 d;
  asm volatile("s_nop 4\n\tbuffer_load_dword %0, %1, %2, %3 offen" : "=v"(d) : "v"(voff), "s"(rs), "s"(soff));
  return d;
}
DI void touch_done(u32 d) { asm volatile("" ::"v"(d)); }
DI void publish(unsigned char* ws, u32 off, u64 tag) {
  asm volatile("s_waitcnt vmcnt(0) lgkmcnt(0)\n\ts_barrier" ::: "memory");
  if (threadIdx.x == 0) __hip_atomic_store((GAS u64*)(ws + off), tag, RLX_AGENT);
  asm volatile("" ::: "memory");
}
DI u32 flag_off(int l, int type, int idx) { return WS_FLAGS + (u32)(((l * 5 + type) << 8) + idx) * 128u; }

#define STAMP(i) do {} while (0)

DI f32x16 reduce4(lptr L, const f32x16 (&acc)[4], int w, int lane) {
#pragma unroll
  for (int q = 1; q < 4; ++q) {
    lptr dst = L + L_INBOX + (u32)(w ^ q) * INBOX_W + (q - 1) * 4096 + lane * 16;
#pragma unroll
    for (int i = 0; i < 4; ++i)
      *(LAS f32x4*)(dst + i * 1024) = f32x4{acc[q][4 * i], acc[q][4 * i + 1], acc[q][4 * i + 2], acc[q][4 * i + 3]};
  }
  bar();
  f32x16 r = acc[0];
  lptr src = L + L_INBOX + (u32)w * INBOX_W + lane * 16;
#pragma unroll
  for (int q = 0; q < 3; ++q)
#pragma unroll
    for (int i = 0; i < 4; ++i) {
      const f32x4 v = *(LAS f32x4*)(src + q * 4096 + i * 1024);
      r[4 * i] += v[0]; r[4 * i + 1] += v[1]; r[4 * i + 2] += v[2]; r[4 * i + 3] += v[3];
    }
  return r;
}

template <int P0, int P1, u32 RS> DI void w_issue(rsrc_t rs, u32 voff, u32 soff, u32 ring) {
  sfor<P0, P1>([&](auto I) { constexpr int i = I; dma16<i * 8 * RS, (i % 24) * 1024>(rs, voff, soff, ring); });
}
template <int P0, int P1, u32 RS, int HP> DI void w_issue2(rsrc_t rs, u32 voff, u32 s0, u32 s1, u32 ring) {
  sfor<P0, P1>([&](auto I) { constexpr int i = I; dma16<(i % HP) * 8 * RS, (i % 24) * 1024>(rs, voff, i < HP ? s0 : s1, ring); });
}
template <int K0, int K1, int TSTEP, int NR> DI void w_take(lptr t0, lptr t1, v4u (&bw)[NR][2]) {
#pragma unroll
  for (int kk = K0; kk < K1; ++kk) {
    const int sl = (kk % 12) * 2048;
    bw[kk][0] = trd2(t0 + sl, t0 + sl + TSTEP);
    bw[kk][1] = trd2(t1 + sl, t1 + sl + TSTEP);
  }
}
template <int NR> DI void w_undef(v4u (&bw)[NR][2], int k0, int k1) {
#pragma unroll
  for (int kk = 0; kk < NR; ++kk) if (kk >= k0 && kk < k1) { asm volatile("" : "=v"(bw[kk][0])); asm volatile("" : "=v"(bw[kk][1])); }
}

template <int NK, int NR, int TSTEP, bool WA, int LA, class Tail>
DI void gemm_main(rsrc_t ws, u32 voff, u32 a0, u32 a1, u32 rot, lptr t0, lptr t1, const v4u (&bw)[NR][2], f32x16 (&acc)[4], Tail&& tail) {
  static_assert(NK - NR == 12 && NK - LA >= 13, "ring is 12 slots and must be consumed before the tail");
  v4u a[NK][2];
  constexpr int HK = NK / 2;
  const u32 b0[2] = {a0 + rot * (HK * 1024), a0 + (rot ^ 1u) * (HK * 1024)}, b1[2] = {a1 + rot * (HK * 1024), a1 + (rot ^ 1u) * (HK * 1024)};
  sfor<0, LA>([&](auto I) { constexpr int n = I, k = (n + NR) % NK; a[n][0] = lda<k % HK>(ws, voff, b0[k / HK]); a[n][1] = lda<k % HK>(ws, voff, b1[k / HK]); });
  v4u wl[12][2];
  wl[0][0] = trd2(t0 + (NR % 12) * 2048, t0 + (NR % 12) * 2048 + TSTEP); wl[0][1] = trd2(t1 + (NR % 12) * 2048, t1 + (NR % 12) * 2048 + TSTEP);
#pragma unroll
  for (int q = 0; q < 4; ++q)
#pragma unroll
    for (int i = 0; i < 16; ++i) acc[q][i] = 0.f;
  wl[1][0] = trd2(t0 + ((NR + 1) % 12) * 2048, t0 + ((NR + 1) % 12) * 2048 + TSTEP); wl[1][1] = trd2(t1 + ((NR + 1) % 12) * 2048, t1 + ((NR + 1) % 12) * 2048 + TSTEP);
  SB();
  sfor<0, NK>([&](auto I) {
    constexpr int n = I, kn = (n + LA + NR) % NK, sl = ((NR + n + 2) % 12) * 2048;
    vmw<2 * LA - 2>(a[n][0], a[n][1]);
    v4u w0, w1;
    if constexpr (n < 12) { w0 = wl[n][0]; w1 = wl[n][1]; } else { w0 = bw[n - 12][0]; w1 = bw[n - 12][1]; }
    acc[1] = WA ? mfma(w0, a[n][1], acc[1]) : mfma(a[n][0], w1, acc[1]);
    SB();
    if constexpr (n + LA < NK) a[n + LA][0] = lda<kn % HK>(ws, voff, b0[kn / HK]); else tail(std::integral_constant<int, 2 * (n + LA - NK)>{});
    SB();
    acc[2] = WA ? mfma(w1, a[n][0], acc[2]) : mfma(a[n][1], w0, acc[2]);
    SB();
    if constexpr (n + 2 < 12) wl[n + 2][0] = trd2(t0 + sl, t0 + sl + TSTEP);
    SB();
    acc[3] = WA ? mfma(w1, a[n][1], acc[3]) : mfma(a[n][1], w1, acc[3]);
    SB();
    if constexpr (n + LA < NK) a[n + LA][1] = lda<kn % HK>(ws, voff, b1[kn / HK]); else tail(std::integral_constant<int, 2 * (n + LA - NK) + 1>{});
    SB();
    acc[0] = WA ? mfma(w0, a[n][0], acc[0]) : mfma(a[n][0], w0, acc[0]);
    SB();
    if constexpr (n + 2 < 12) wl[n + 2][1] = trd2(t1 + sl, t1 + sl + TSTEP);
    SB();
  });
}

__global__ __launch_bounds__(256, 1) void fused_fwd(Params p) {
  extern __shared__ __attribute__((aligned(16))) unsigned char smem[];
  lptr L = (lptr)smem;
  const u32 lds0 = (u32)(size_t)L;
#define IDS \
  const int tid = (int)opqv(threadIdx.x), lane = tid & 63, r = lane & 31, h = lane >> 5; \
  const int w = (int)opq(w_), b = (int)opq(blockIdx.x), g = b & 7, bi = b >> 3, pj = g >> 1; \
  const int T = 2 * g + (bi >> 4), head = bi & 15; \
  const int d3 = (bi * 43) >> 7, m3 = bi - 3 * d3; \
  const int st = 8 * pj + ((g & 1) ? d3 : (bi >> 2)), dt = (g & 1) ? 4 + m3 : (bi & 3); \
  const int ft = 7 * pj + ((g & 1) ? (bi >> 2) : d3), dt4 = (g & 1) ? (bi & 3) : 4 + m3; \
  const int wa = w >> 1, wb = w & 1; \
  const int ch16 = (lane >> 4) & 1, tq = (lane & 15) >> 2, tp = lane & 3; \
  const u32 ringw = L_RING + (u32)w * RING_W; \
  const u32 dmarow = lane >> 3, dmach = ((lane & 7) ^ (((lane >> 4) & 1) << 2)) * 16; \
  const u32 sw = (u32)((tq >> 1) & 1) << 2, cpart = 2 * ch16 + (tp >> 1); \
  (void)r; (void)h; (void)T; (void)head; (void)st; (void)dt; (void)ft; (void)dt4; (void)wa; (void)wb; (void)ch16; (void)tq; (void)tp; (void)ringw; (void)dmarow; (void)dmach; (void)sw; (void)cpart;
  const u32 w_ = __builtin_amdgcn_readfirstlane(threadIdx.x >> 6);
  const bool p35 = (blockIdx.x & 1) == 0 || (blockIdx.x >> 3) < 24;
  const bool p4 = (blockIdx.x >> 3) < ((blockIdx.x & 1) ? 28u : 21u);

  const rsrc_t rws = mkrs(p.ws, WS_END);
  u64 tag = 0;
  u32 xres[8];
  v4u wf[8][2];
#pragma unroll
  for (int i = 0; i < 8; ++i) { xres[i] = 0; asm volatile("" : "=a"(wf[i][0])); asm volatile("" : "=a"(wf[i][1])); }

#pragma unroll 1
  for (int l = 0; l < 2; ++l) {
    STAMP(0);
    const rsrc_t rw1 = mkrs(p.w1, p4 ? S_ * F_ * 2 : 0);
    const rsrc_t rw2 = mkrs(p.w2, p35 ? F_ * S_ * 2 : 0);
#define W2_ARGS rw2, dmarow * 4096u + dmach + (u32)st * 128u, (u32)(448 * w + 224 * ROT5) * 4096u, (u32)(448 * w + 224 * (ROT5 ^ 1)) * 4096u, lds0 + ringw
#define W1_ARGS rw1, dmarow * 3584u + dmach + (u32)ft * 128u, (u32)(512 * w + 256 * ROT4) * 3584u, (u32)(512 * w + 256 * (ROT4 ^ 1)) * 3584u, lds0 + ringw
    {
    IDS
    const int tb = 4 * T + w;
    const u32 shift = (head & 1) * 8;
    const u32 half = (((tq >> 1) & 1) ^ 1) * 64, col = shift + ch16 * 32 + tp * 8;
    lptr wtk = L + (32 * h + tq) * 128 + half + col, wtv = L + (32 * h + tq) * 128 + (half ^ 64) + col, wtq = L + L_WQ + (32 * h + tq) * 64 + col;
    v4u X[28];
    f32x16 kt, vv;
#pragma unroll
    for (int i = 0; i < 16; ++i) { kt[i] = 0.f; vv[i] = 0.f; }
    float ssq;
    u32 tc2 = 0, tc3 = 0, tc4 = 0;
    v4u wk[28], wv[28], wq[28];
    constexpr int PD = 3;
#define WROW(k) (((k) >> 2) * 64 + ((k) & 3) * 8)
#define WFK(k) trd2(wtk + WROW(k) * 128, wtk + WROW(k) * 128 + 512)
#define WFV(k) trd2(wtv + WROW(k) * 128, wtv + WROW(k) * 128 + 512)
#define WFQ(k) trd2(wtq + WROW(k) * 64, wtq + WROW(k) * 64 + 256)
    auto kv_head = [&]() {
#pragma unroll
      for (int k = 0; k < PD; ++k) { wk[k] = WFK(k); wv[k] = WFV(k); }
      SB();
    };
    auto kv_step = [&](auto I, auto&& mem) {
      constexpr int k = I;
      kt = mfma(wk[k], X[k], kt);
      SB();
      if constexpr (k + PD < 28) wk[k + PD] = WFK(k + PD);
      mem();
      SB();
      vv = mfma(X[k], wv[k], vv);
      SB();
      if constexpr (k + PD < 28) wv[k + PD] = WFV(k + PD);
      SB();
    };
    const v4u ones = {0x3F803F80u, 0x3F803F80u, 0x3F803F80u, 0x3F803F80u};
    if (l == 0) {
      v2u e0;
      asm volatile("global_load_dwordx2 %0, %1, %2 sc1" : "=v"(e0) : "v"(WS_EPOCH), "s"(p.ws));
      u32 tcc;
      {
        u64 pc; u32 back, len;
        asm volatile("s_getpc_b64 %0\n.Lfwd_here:\n\ts_mov_b32 %1, .Lfwd_here-_Z9fused_fwd6Params\n\ts_mov_b32 %2, .Lfwd_last-_Z9fused_fwd6Params" : "=s"(pc), "=s"(back), "=s"(len));
        tcc = touch(mkrs((const void*)(pc - back), CODE_TOUCH ? len : 0), lane * 128, (u32)(bi * 4 + w) * 8192u);
      }
      const rsrc_t rx = mkrs(p.x, S_ * D_ * 2), rwq = mkrs(p.wqkv, D_ * 3 * D_ * 2);
      const u32 hoff = (u32)(head * 56) - shift;
      const u32 xv = dmarow * 896u + (lane & 7) * 16u, xs = (u32)(tb * 32) * 896u;
      lptr xb = L + L_WO + w * 15360;
      lptr xw = xb + dmarow * 128, xr = xb + r * 128;
      const u32 xsw = (r >> 1) & 7, xc0 = ((lane & 7) ^ (lane >> 4)) * 16;
      const u32 kvv = dmarow * 2688u + 896u + ((((lane >> 2) ^ (lane >> 4) ^ 1) & 1) * 896u) + (lane & 3) * 16u + hoff;
      const u32 kvs = (u32)(112 * w) * 2688u, kvl0 = lds0 + (u32)(14 * w) * 1024u;
      const rsrc_t rwo = mkrs(p.wo, D_ * D_ * 2);
      const u32 wov = dmarow * 896u + dmach + (u32)dt * 128u, wos = (u32)(112 * w) * 896u, wol = lds0 + L_WO + (u32)w * 15360u;
      const u32 qv = (u32)(lane >> 2) * 2688u + (lane & 3) * 16u + hoff, ql = lds0 + L_WQ + (u32)(7 * w) * 1024u;
      sfor<0, 28>([&](auto I) { constexpr int k = I; X[k] = ldp<(k >> 2) * 128>(rx, xv, xs + (k & 3) * 7168u); });
      sfor<0, 7>([&](auto I) { constexpr int i = I; dma16<i * 8 * 2688, i * 1024>(rwq, kvv, kvs, kvl0); });
      STAMP(24);
      float sq4[4] = {0.f, 0.f, 0.f, 0.f};
      sfor<0, 7>([&](auto I) {
        constexpr int G = I;
        vmw<24 - 4 * G + 7 + 2 * G>(X[4 * G], X[4 * G + 1], X[4 * G + 2], X[4 * G + 3]);
#pragma unroll
        for (int i = 0; i < 4; ++i) *(LAS v4u*)(xw + i * 1024 + (xc0 ^ ((i & 1) * 64))) = X[4 * G + i];
#pragma unroll
        for (int c = 0; c < 4; ++c) X[4 * G + c] = *(LAS v4u*)(xr + (((4 * h + c) ^ xsw) * 16));
        if constexpr (2 * G < 7) dma16<(7 + 2 * G) * 8 * 2688, (7 + 2 * G) * 1024>(rwq, kvv, kvs, kvl0); else dma16<(2 * G - 7) * 16 * 2688, (2 * G - 7) * 1024>(rwq, qv, kvs, ql);
        if constexpr (2 * G + 1 < 7) dma16<(8 + 2 * G) * 8 * 2688, (8 + 2 * G) * 1024>(rwq, kvv, kvs, kvl0); else dma16<(2 * G - 6) * 16 * 2688, (2 * G - 6) * 1024>(rwq, qv, kvs, ql);
#pragma unroll
        for (int i = 0; i < 16; ++i) { const bf16x2 xx = asbf2(X[4 * G + (i >> 2)][i & 3]); sq4[i & 3] = __builtin_amdgcn_fdot2_f32_bf16(xx, xx, sq4[i & 3], false); }
        pin4(sq4);
        SB();
      });
      ssq = (sq4[0] + sq4[1]) + (sq4[2] + sq4[3]);
      STAMP(25);
      f32x16 ck, cv;
#pragma unroll
      for (int i = 0; i < 16; ++i) { ck[i] = 0.f; cv[i] = 0.f; }
      lptr ck0 = L + (112 * w + 8 * h + tq) * 128 + half + col, cv0 = L + (112 * w + 8 * h + tq) * 128 + (half ^ 64) + col;
      vmw0<7>();
      STAMP(26);
      {
        v4u f[14];
#pragma unroll
        for (int i = 0; i < 7; ++i) { f[2 * i] = trd2(ck0 + i * 2048, ck0 + i * 2048 + 512); f[2 * i + 1] = trd2(cv0 + i * 2048, cv0 + i * 2048 + 512); }
        SB();
        sfor<0, 7>([&](auto I) { constexpr int i = I;
          ck = mfma(ones, f[2 * i], ck); SB(); w_issue<2 * i, 2 * i + 1, 896u>(rwo, wov, wos, wol); SB();
          cv = mfma(ones, f[2 * i + 1], cv); SB(); w_issue<2 * i + 1, 2 * i + 2, 896u>(rwo, wov, wos, wol); SB(); });
        w_issue<14, 15, 896u>(rwo, wov, wos, wol);
      }
      {
        const u32 gw = (u32)(b * 4 + w);
        const u32 l2 = gw * 24 + (lane < 24 ? lane : 0), r2 = l2 >> 5, l1 = gw * 21 + (lane < 21 ? lane : 0), r1 = l1 / 28;
        tc2 = touch(mkrs(p.w2, TOUCH ? S_ * F_ * 2 : 0), ((r2 / 192) * 448 + r2 % 192) * 4096u + (l2 & 31) * 128u, 0);
        tc3 = touch(mkrs(p.w1, TOUCH ? S_ * F_ * 2 : 0), ((r1 / 192) * 512 + r1 % 192) * 3584u + (l1 % 28) * 128u, 0);
      }
      STAMP(27);
      touch_done(tcc);
      asm volatile("" : "+v"(e0));
      tag = (((u64)(u32)__builtin_amdgcn_readfirstlane(e0.y) << 32) | (u64)(u32)__builtin_amdgcn_readfirstlane(e0.x)) + 1ull;
      *(LAS float*)(L + L_CSP + w * 384 + 128 + lane * 4) = h ? cv[0] : ck[0];
      bar();
      {
        lptr c28 = L + (112 * w + lane) * 128 + (((lane >> 1) & 1) ^ 1) * 64 + shift + 56;
        *(LAS unsigned short*)c28 = 0x3F80;
        if (lane < 48) *(LAS unsigned short*)(c28 + 64 * 128) = 0x3F80;
        float a0 = 0.f;
#pragma unroll
        for (int v = 0; v < 4; ++v) a0 += *(LAS float*)(L + L_CSP + v * 384 + 128 + lane * 4);
        *(LAS float*)(L + L_CSF + w * 512 + 128 + lane * 4) = a0;
      }
      bar();
      kv_head();
      STAMP(1);
      SB();
      sfor<0, 28>([&](auto I) { kv_step(I, []() {}); });
      asm volatile("" : "+a"(kt), "+a"(vv));
    } else {
      kv_head();
      const Poll pq = wait_flags(p.ws, flag_off(0, 4, (2 * T + (w >> 1)) * 8 + (lane < 7 ? lane : 0)), tag);
      STAMP(1);
      const u32 xb = WS_XF + (u32)(tb * 28) * 1024u;
      const u32 sqv = (u32)(7 * h) * 128u + r * 4, sqs = WS_SQ + (u32)tb * 1792u;
      u32 sp[7];
      sfor<0, LA1>([&](auto I) { constexpr int k = I; X[k] = lda<k>(rws, lane * 16, xb); });
      sfor<0, 28>([&](auto I) {
        constexpr int k = I;
        vmw<(k + LA1 <= 35) ? LA1 - 1 : 34 - k>(X[k]);
        if constexpr (k == 0) poll_done(pq);
        kv_step(I, [&]() {
          if constexpr (k + LA1 < 28) X[k + LA1] = lda<k + LA1>(rws, lane * 16, xb);
          else if constexpr (k + LA1 < 35) sp[k + LA1 - 28] = lda1<(k + LA1 - 28) * 128>(rws, sqv, sqs);
        });
      });
      asm volatile("s_waitcnt vmcnt(0)" : "+v"(sp[0]), "+v"(sp[1]), "+v"(sp[2]), "+v"(sp[3]), "+v"(sp[4]), "+v"(sp[5]), "+v"(sp[6]));
      asm volatile("" : "+a"(kt), "+a"(vv));
      ssq = ((asf(sp[0]) + asf(sp[1])) + (asf(sp[2]) + asf(sp[3]))) + ((asf(sp[4]) + asf(sp[5])) + asf(sp[6]));
    }
    STAMP(18);
    float mean, rstd;
    {
      const float sum = half_add(h ? kt[12] : 0.f);
      const float sq = half_add(ssq);
      mean = sum * (1.f / 448.f);
      const float var = fmaxf(sq * (1.f / 448.f) - mean * mean, 0.f);
      rstd = __builtin_amdgcn_rsqf(var + EPS);
    }
    const float mr = mean * rstd;
    *(LAS float*)(L + L_MR + w * 256 + h * 128 + r * 4) = h ? mr : rstd;
    const u32 kvbase = WS_KV + l * KV_L + (u32)head * (64u * 4096u);
    lptr kvl = L + L_INBOX + lane * 16;
    v4u K0, K1, V0, V1;
    {
      u32 o[8];
#pragma unroll
      for (int i = 0; i < 4; ++i) {
        const f32x4 cs = *(LAS f32x4*)(L + L_CSF + w * 512 + 128 + (8 * i + 4 * h) * 4);
        o[2 * i] = pk(kt[4 * i] * rstd - mr * cs[0], kt[4 * i + 1] * rstd - mr * cs[1]);
        o[2 * i + 1] = pk(kt[4 * i + 2] * rstd - mr * cs[2], kt[4 * i + 3] * rstd - mr * cs[3]);
      }
      if (h) { o[6] = 0u; o[7] = 0u; }
      K0 = v4u{o[0], o[1], o[2], o[3]}; K1 = v4u{o[4], o[5], o[6], o[7]};
      st_sc1(K0, rws, lane * 16, kvbase + tb * 4096);
      st_sc1(K1, rws, lane * 16 + 1024, kvbase + tb * 4096);
      const float csv = *(LAS float*)(L + L_CSF + w * 512 + 256 + r * 4);
#pragma unroll
      for (int i = 0; i < 4; ++i) {
        const f32x4 rr = *(LAS f32x4*)(L + L_MR + w * 256 + (8 * i + 4 * h) * 4);
        const f32x4 mm = *(LAS f32x4*)(L + L_MR + w * 256 + 128 + (8 * i + 4 * h) * 4);
        o[2 * i] = pk(vv[4 * i] * rr[0] - mm[0] * csv, vv[4 * i + 1] * rr[1] - mm[1] * csv);
        o[2 * i + 1] = pk(vv[4 * i + 2] * rr[2] - mm[2] * csv, vv[4 * i + 3] * rr[3] - mm[3] * csv);
      }
      if (r >= 28) {
#pragma unroll
        for (int i = 0; i < 8; ++i) o[i] = 0x3F803F80u;
      }
      V0 = v4u{o[0], o[1], o[2], o[3]}; V1 = v4u{o[4], o[5], o[6], o[7]};
      st_sc1(V0, rws, lane * 16 + 2048, kvbase + tb * 4096);
      st_sc1(V1, rws, lane * 16 + 3072, kvbase + tb * 4096);
    }
    STAMP(19);
    if (l == 0) {
      vmw0<6>();
      lptr t0 = L + L_WO + w * 15360 + (4 * h + tq) * 128 + (((4 * wa + cpart) ^ sw) * 16) + (tp & 1) * 8;
      lptr t1 = L + L_WO + w * 15360 + (4 * h + tq) * 128 + (((4 * (wa ^ 1) + cpart) ^ sw) * 16) + (tp & 1) * 8;
      lptr cq0 = L + L_WQ + (112 * w + 8 * h + tq) * 64 + col;
      v4u f[7];
#pragma unroll
      for (int i = 0; i < 7; ++i) f[i] = trd2(cq0 + i * 1024, cq0 + i * 1024 + 256);
#pragma unroll
      for (int k = 0; k < 8; ++k) {
        const int ro = (28 * (k >> 1) + 16 * (k & 1)) * 128;
        wf[k][0] = trd2(t0 + ro, t0 + ro + 1024); wf[k][1] = trd2(t1 + ro, t1 + ro + 1024);
      }
      SB();
      f32x16 cq;
#pragma unroll
      for (int i = 0; i < 16; ++i) cq[i] = 0.f;
#pragma unroll
      for (int i = 0; i < 7; ++i) cq = mfma(ones, f[i], cq);
      *(LAS float*)(L + L_CSP + w * 384 + r * 4) = cq[0];
      st_sc1(v4u{0u, 0u, 0u, 0u}, rws, head < 14 ? WS_STATS + h * STATS_L + (u32)((tb * 14 + head) * 512 + r * 16) : 0x40000000u, 0);
    }
    publish(p.ws, flag_off(l, 0, T * 16 + head), tag);
    touch_done(tc2); touch_done(tc3);
    STAMP(2);
#pragma unroll
    for (int k = 0; k < 2 * PD; ++k) wq[k] = WFQ(k);
    {
      lptr own = kvl + (8 + w) * 4096;
      *(LAS v4u*)own = K0; *(LAS v4u*)(own + 1024) = K1; *(LAS v4u*)(own + 2048) = V0; *(LAS v4u*)(own + 3072) = V1;
    }
    if (l == 0) {
      float a0 = 0.f;
#pragma unroll
      for (int v = 0; v < 4; ++v) a0 += *(LAS float*)(L + L_CSP + v * 384 + r * 4);
      *(LAS float*)(L + L_CSF + w * 512 + r * 4) = a0;
      const u32 wib = WS_WI + (u32)head * 86016u + (u32)tb * 1024u;
      st_sc1(*(LAS v4u*)(L + tb * 1024 + lane * 16), rws, lane * 16, wib);
      st_sc1(*(LAS v4u*)(L + (tb < 20 ? tb + 64 : tb) * 1024 + lane * 16), rws, lane * 16, tb < 20 ? wib + 65536u : 0x40000000u);
      const u32 xo = (u32)((2 * st + wb) * 32 + r) * 896u + (u32)(dt * 64 + wa * 32 + 4 * h) * 2u;
      const rsrc_t rx = mkrs(p.x, S_ * D_ * 2);
#pragma unroll
      for (int i = 0; i < 4; ++i) {
        const v2u v = __builtin_amdgcn_raw_buffer_load_b64(rx, xo + i * 16, 0, 0);
        xres[2 * i] = v.x; xres[2 * i + 1] = v.y;
      }
    }
    {
      const int tsrc = T - 2 + (w >> 1);
      const u32 f1 = opqv(flag_off(l, 0, (tsrc < 0 ? T : tsrc) * 16 + head));
      Poll pq;
      f32x16 qt;
#pragma unroll
      for (int i = 0; i < 16; ++i) qt[i] = 0.f;
      SB();
      sfor<0, 28>([&](auto I) {
        constexpr int k = I;
        qt = mfma(wq[k], X[k], qt);
        SB();
        if constexpr (k + 2 * PD < 28) wq[k + 2 * PD] = WFQ(k + 2 * PD);
        if constexpr (k == QP0) pq.a = poll_one(p.ws, f1);
        if constexpr (k == QP0 + 6) pq.b = poll_one(p.ws, f1);
        if constexpr (k == QP0 + 12) pq.c = poll_one(p.ws, f1);
        SB();
      });
      asm volatile("" : "+a"(qt));
      poll_finish(pq, p.ws, f1, tag);
      STAMP(3);
      const u32 so = tsrc >= 0 ? kvbase + (u32)(4 * T - 8 + 2 * w) * 4096u : 0x40000000u;
      v4u t[8];
      t[0] = lda<0>(rws, lane * 16, so); t[1] = lda<1>(rws, lane * 16, so); t[2] = lda<4>(rws, lane * 16, so); t[3] = lda<5>(rws, lane * 16, so);
      t[4] = lda<2>(rws, lane * 16, so); t[5] = lda<3>(rws, lane * 16, so); t[6] = lda<6>(rws, lane * 16, so); t[7] = lda<7>(rws, lane * 16, so);
      SB();
      v4u Q0, Q1;
      {
        const float sc = 0.18898223650461362f * 1.4426950408889634f;
        const float rs = rstd * sc, ms = mr * sc;
        u32 o[8];
#pragma unroll
        for (int i = 0; i < 4; ++i) {
          const f32x4 cs = *(LAS f32x4*)(L + L_CSF + w * 512 + (8 * i + 4 * h) * 4);
          o[2 * i] = pk(qt[4 * i] * rs - ms * cs[0], qt[4 * i + 1] * rs - ms * cs[1]);
          o[2 * i + 1] = pk(qt[4 * i + 2] * rs - ms * cs[2], qt[4 * i + 3] * rs - ms * cs[3]);
        }
        if (h) { o[6] = 0x00003F80u; o[7] = 0u; }
        Q0 = v4u{o[0], o[1], o[2], o[3]}; Q1 = v4u{o[4], o[5], o[6], o[7]};
      }
      f32x16 s8 = qk_own(K0, K1, Q0, Q1);
      u32 em[16], pm[8];
      f32x16 c0;
#pragma unroll
      for (int i = 0; i < 16; ++i) { const int key = (i & 3) + 8 * (i >> 2) + 4 * h; em[i] = key > r ? 0xffffffffu : 0u; c0[i] = asf(~em[i] & 0xF149F2CAu); }
#pragma unroll
      for (int i = 0; i < 8; ++i) pm[i] = (em[2 * i] & 0x0000ffffu) | (em[2 * i + 1] & 0xffff0000u);
#pragma unroll
      for (int i = 0; i < 16; ++i) asm volatile("" : "+v"(em[i]));
#pragma unroll
      for (int i = 0; i < 8; ++i) asm volatile("" : "+v"(pm[i]));
      tie_far(s8);
      float m4[4];
#pragma unroll
      for (int i = 0; i < 16; ++i) s8[i] = asf((em[i] & 0xF149F2CAu) | (~em[i] & asu(s8[i])));
#pragma unroll
      for (int i = 0; i < 4; ++i) m4[i] = fmaxf(fmaxf(s8[4 * i], s8[4 * i + 1]), fmaxf(s8[4 * i + 2], s8[4 * i + 3]));
      asm volatile("" : "+v"(c0), "+v"(m4[0]), "+v"(m4[1]), "+v"(m4[2]), "+v"(m4[3]));
      SB();
      {
        lptr dst = kvl + w * 8192;
        vmw<4>(t[0], t[1], t[2], t[3]);
        poll_done(pq);
        if (tsrc < 0 && h) { t[1][2] = 0xF149u; t[3][2] = 0xF149u; }
#pragma unroll
        for (int i = 0; i < 4; ++i) *(LAS v4u*)(dst + (i & 1) * 1024 + (i >> 1) * 4096) = t[i];
        vmw<0>(t[4], t[5], t[6], t[7]);
#pragma unroll
        for (int i = 0; i < 4; ++i) *(LAS v4u*)(dst + 2048 + (i & 1) * 1024 + (i >> 1) * 4096) = t[4 + i];
      }
      bar();
      {
        const u32 l1 = (u32)(b * 4 + w) * 35 + (lane < 35 ? lane : 0), r1 = l1 / 28;
        tc4 = touch(mkrs(p.w1, (TOUCH && l == 0) ? S_ * F_ * 2 : 0), ((r1 / 320) * 512 + 192 + r1 % 320) * 3584u + (l1 % 28) * 128u, 0);
      }
      STAMP(17);
      lptr kq = kvl + w * 4096;
      v4u Kr[8][2], Vr[8][2];
#pragma unroll
      for (int j = 0; j < 4; ++j) { Kr[j][0] = *(LAS v4u*)(kq + j * 4096); Kr[j][1] = *(LAS v4u*)(kq + j * 4096 + 1024); }
      SB();
      {
        f32x16 s[8];
        auto mx = [&](const f32x16& t, int half) {
#pragma unroll
          for (int i = 0; i < 4; ++i) m4[i] = fmaxf(fmaxf(m4[i], t[8 * half + 2 * i]), t[8 * half + 2 * i + 1]);
        };
        sfor<0, 8>([&](auto I) {
          constexpr int n = I;
          if constexpr (n == 0) s[0] = qk1(Kr[0][0], Q0, c0); else s[n] = qk1z(Kr[n][0], Q0);
          SB();
          if constexpr (n < 4) Kr[n + 4][0] = *(LAS v4u*)(kq + (n + 4) * 4096);
          Vr[n][0] = *(LAS v4u*)(kq + n * 4096 + 2048);
          if constexpr (n >= 2) mx(s[n - 2], 1);
          SB();
          qk2(s[n], Kr[n][1], Q1);
          SB();
          if constexpr (n < 4) Kr[n + 4][1] = *(LAS v4u*)(kq + (n + 4) * 4096 + 1024);
          Vr[n][1] = *(LAS v4u*)(kq + n * 4096 + 3072);
          if constexpr (n >= 1) { tie(s[n - 1]); mx(s[n - 1], 0); }
          SB();
        });
        mx(s[6], 1);
        SB();
        asm volatile("s_nop 7" : "+v"(s[7]));
        mx(s[7], 0); mx(s[7], 1);
      }
      const float nm = -half_max(fmaxf(fmaxf(m4[0], m4[1]), fmaxf(m4[2], m4[3])));
      f32x16 cm, o;
#pragma unroll
      for (int i = 0; i < 16; ++i) { cm[i] = nm; o[i] = 0.f; }
      {
        f32x16 s[9];
        u32 pp[9][8];
        float e[16];
        asm volatile("" : "+v"(cm));
        SB(); nop2();
        s[1] = qk1(Kr[1][0], Q0, cm); qk2(s[1], Kr[1][1], Q1);
        s8 = qk_own_c(K0, K1, Q0, Q1, cm);
        SB();
        sfor<1, 9>([&](auto I) {
          constexpr int c = I, nx = (c + 1) & 7;
          tie(s[c]);
          if constexpr (c == 8) {
            tie(s8);
#pragma unroll
            for (int i = 0; i < 16; ++i) s[8][i] = asf((asu(s[8][i]) & em[i]) | (asu(s8[i]) & ~em[i]));
          }
          auto ex = [&](int q) {
#pragma unroll
            for (int i = 0; i < 4; ++i) e[4 * q + i] = __builtin_amdgcn_exp2f(s[c][4 * q + i]);
          };
          auto cv = [&](int q) { pp[c][2 * q] = pk(e[4 * q], e[4 * q + 1]); pp[c][2 * q + 1] = pk(e[4 * q + 2], e[4 * q + 3]); };
          if constexpr (c < 8) s[c + 1] = qk1(Kr[nx][0], Q0, cm);
          SB(); if constexpr (c < 7) w_issue2<4 * c - 4, 4 * c - 3, 4096u, 28>(W2_ARGS); ex(0); SB();
          if constexpr (c < 8) qk2(s[c + 1], Kr[nx][1], Q1);
          SB(); if constexpr (c < 7) w_issue2<4 * c - 3, 4 * c - 2, 4096u, 28>(W2_ARGS); ex(1); cv(0); SB();
          if constexpr (c > 1) o = mfma(Vr[c - 1][1], v4u{pp[c - 1][4], pp[c - 1][5], pp[c - 1][6], pp[c - 1][7]}, o);
          SB(); if constexpr (c < 7) w_issue2<4 * c - 2, 4 * c - 1, 4096u, 28>(W2_ARGS); ex(2); cv(1); SB();
          if constexpr (c < 8) o = mfma(Vr[c][0], v4u{pp[c][0], pp[c][1], pp[c][2], pp[c][3]}, o);
          else {
            o = mfma(Vr[0][0], v4u{pp[8][0] & pm[0], pp[8][1] & pm[1], pp[8][2] & pm[2], pp[8][3] & pm[3]}, o);
            o = mfma(V0, v4u{pp[8][0] & ~pm[0], pp[8][1] & ~pm[1], pp[8][2] & ~pm[2], pp[8][3] & ~pm[3]}, o);
          }
          SB(); if constexpr (c < 7) w_issue2<4 * c - 1, 4 * c, 4096u, 28>(W2_ARGS); ex(3); cv(2); cv(3); SB();
        });
        o = mfma(Vr[0][1], v4u{pp[8][4] & pm[4], pp[8][5] & pm[5], pp[8][6] & pm[6], pp[8][7] & pm[7]}, o);
        o = mfma(V1, v4u{pp[8][4] & ~pm[4], pp[8][5] & ~pm[5], pp[8][6] & ~pm[6], pp[8][7] & ~pm[7]}, o);
      }
      const float inv = __builtin_amdgcn_rcpf(half_add(h ? o[12] : 0.f));
      u32 oo[8];
#pragma unroll
      for (int i = 0; i < 8; ++i) oo[i] = pk(o[2 * i] * inv, o[2 * i + 1] * inv);
      if (h) { oo[6] = 0u; oo[7] = 0u; }
      const u32 ob = WS_OF + l * OF_L + (u32)(tb * 16 + head) * 2048u;
      st_sc1(v4u{oo[0], oo[1], oo[2], oo[3]}, rws, lane * 16, ob);
      st_sc1(v4u{oo[4], oo[5], oo[6], oo[7]}, rws, lane * 16 + 1024, ob);
      publish(p.ws, flag_off(l, 1, T * 16 + head), tag);
      touch_done(tc4);
      STAMP(4);
    }
    }
    v4u bw4[20][2], bw5[16][2];
#define T5_PTRS \
    lptr t0 = L + ringw + (4 * h + tq) * 128 + (((4 * wb + cpart) ^ sw) * 16) + (tp & 1) * 8; \
    lptr t1 = L + ringw + (4 * h + tq) * 128 + (((4 * (wb ^ 1) + cpart) ^ sw) * 16) + (tp & 1) * 8;
#define T4_PTRS \
    lptr n0 = L + ringw + (8 * h + tq) * 128 + (((4 * wa + cpart) ^ sw) * 16) + (tp & 1) * 8; \
    lptr n1 = L + ringw + (8 * h + tq) * 128 + (((4 * (wa ^ 1) + cpart) ^ sw) * 16) + (tp & 1) * 8;
    {
      IDS
      T5_PTRS
      w_take<0, 12, 1024>(t0, t1, bw5);
      lgk0();
      w_issue2<0, 24, 3584u, 32>(W1_ARGS);
    }
    if (p35) {
      IDS
      const Poll pq = wait_flags(p.ws, flag_off(l, 1, (st >> 1) * 16 + 4 * w + (lane & 3)), tag);
      STAMP(5);
      const u32 o0 = WS_OF + l * OF_L + (u32)((2 * st + wb) * 16 + 4 * w) * 2048u;
      const u32 o1 = WS_OF + l * OF_L + (u32)((2 * st + (wb ^ 1)) * 16 + 4 * w) * 2048u;
      v4u of[8][2];
      sfor<0, LA3>([&](auto I) { constexpr int k = I; of[k][0] = lda<k>(rws, lane * 16, o0); of[k][1] = lda<k>(rws, lane * 16, o1); });
      f32x16 acc[4];
#pragma unroll
      for (int q = 0; q < 4; ++q)
#pragma unroll
        for (int i = 0; i < 16; ++i) acc[q][i] = 0.f;
      sfor<0, 8>([&](auto I) {
        constexpr int k = I;
        vmw<(k + LA3 <= 8) ? 2 * LA3 - 2 : 14 - 2 * k>(of[k][0], of[k][1]);
        if constexpr (k == 0) poll_done(pq);
        acc[1] = mfma(wf[k][0], of[k][1], acc[1]);
        SB();
        if constexpr (k + LA3 < 8) of[k + LA3][0] = lda<k + LA3>(rws, lane * 16, o0);
        SB();
        acc[2] = mfma(wf[k][1], of[k][0], acc[2]);
        acc[3] = mfma(wf[k][1], of[k][1], acc[3]);
        SB();
        if constexpr (k + LA3 < 8) of[k + LA3][1] = lda<k + LA3>(rws, lane * 16, o1);
        SB();
        acc[0] = mfma(wf[k][0], of[k][0], acc[0]);
        if constexpr (k < 7) SB();
      });
      const f32x16 y = reduce4(L, acc, w, lane);
      float xv[16], s1 = 0.f, s2 = 0.f;
#pragma unroll
      for (int i = 0; i < 8; ++i) {
        xres[i] = pk(blo(xres[i]) + y[2 * i], bhi(xres[i]) + y[2 * i + 1]);
        xv[2 * i] = blo(xres[i]); xv[2 * i + 1] = bhi(xres[i]);
        s1 += xv[2 * i] + xv[2 * i + 1];
        s2 += xv[2 * i] * xv[2 * i] + xv[2 * i + 1] * xv[2 * i + 1];
      }
      s1 = half_add(s1); s2 = half_add(s2);
      const u32 tg = (u32)tag | 1u;
      const u32 sb = WS_STATS + l * STATS_L + (u32)((2 * st + wb) * 14) * 512u;
      if (h == 0) st_sc1(v4u{asu(s1), tg, asu(s2), tg}, rws, r * 16, sb + (u32)(2 * dt + wa) * 512u);
      STAMP(6);
      {
        T4_PTRS
        w_take<0, NB, 512>(n0, n1, bw4);
        lgk0();
        w_issue2<24, 24 + 2 * NB, 3584u, 32>(W1_ARGS);
      }
      float t1s, t2s;
      for (u32 spins = 0;;) {
        v4u gr[7];
#pragma unroll
        for (int i = 0; i < 7; ++i) gr[i] = ld_sc1(rws, (u32)(7 * h) * 512u + r * 16 + i * 512, sb);
        bool ok = true; t1s = 0.f; t2s = 0.f;
#pragma unroll
        for (int i = 0; i < 7; ++i) { ok &= (gr[i].y == tg) & (gr[i].w == tg); t1s += asf(gr[i].x); t2s += asf(gr[i].z); }
        if (__all(ok)) break;
        asm volatile("" ::: "memory");
        if (spin_out(p.ws, spins, (u32)tag)) break;
      }
      STAMP(7);
      t1s = half_add(t1s); t2s = half_add(t2s);
      const float m1 = t1s * (1.f / 448.f);
      const float var = fmaxf(t2s * (1.f / 448.f) - m1 * m1, 0.f);
      const float r1 = __builtin_amdgcn_rsqf(var + EPS);
      const float r2 = __builtin_amdgcn_rsqf(var * r1 * r1 + EPS);
      const float rr = r1 * r2, mm = m1 * rr;
      lptr tw = L + L_TR + w * 2304;
#pragma unroll
      for (int i = 0; i < 4; ++i)
        *(LAS v2u*)(tw + r * 72 + i * 16 + h * 8) = v2u{pk(xv[4 * i] * rr - mm, xv[4 * i + 1] * rr - mm), pk(xv[4 * i + 2] * rr - mm, xv[4 * i + 3] * rr - mm)};
      lgk0();
      lptr tr = tw + (8 * h + tq) * 72 + ch16 * 32 + tp * 8;
      const u32 hb = WS_HF + l * HF_L + (u32)((2 * dt + wa) * 128 + (2 * st + wb) * 2) * 1024u;
      st_sc1(trd2(tr, tr + 4 * 72), rws, lane * 16, hb);
      st_sc1(trd2(tr + 16 * 72, tr + 20 * 72), rws, lane * 16 + 1024, hb);
      publish(p.ws, flag_off(l, 2, dt * 32 + st), tag);
      STAMP(8);
    } else {
      IDS
      T4_PTRS
      drain();
      w_take<0, NB, 512>(n0, n1, bw4);
      lgk0();
      w_issue2<24, 24 + 2 * NB, 3584u, 32>(W1_ARGS);
      drain();
    }
    if (p4) {
      IDS
      T4_PTRS
      v2u bias[4];
      {
        const rsrc_t rb1 = mkrs(p.b1, F_ * 2);
        const u32 bo = (u32)(ft * 64 + wa * 32 + 4 * h) * 2u;
        sfor<0, 4>([&](auto I) { constexpr int i = I; bias[i] = ldp2<16 * i>(rb1, bo); });
      }
      w_take<NB, 20, 512>(n0, n1, bw4);
      lgk0();
      w_issue2<24 + 2 * NB, 64, 3584u, 32>(W1_ARGS);
      STAMP(9);
      const Poll pq = wait_flags(p.ws, flag_off(l, 2, dt4 * 32 + 8 * w + (lane & 7)), tag);
      asm volatile("" : "+v"(bias[0]), "+v"(bias[1]), "+v"(bias[2]), "+v"(bias[3]));
      STAMP(10);
      f32x16 acc[4];
      const u32 hb = WS_HF + l * HF_L + (u32)(32 * w) * 1024u;
      gemm_main<32, 20, 512, true, LA4>(rws, lane * 16, hb + (u32)(2 * dt4 + wb) * 131072u, hb + (u32)(2 * dt4 + (wb ^ 1)) * 131072u, ROT4, n0, n1, bw4, acc,
                                        [&](auto I) { constexpr int i = I; w_issue2<24 + i, 25 + i, 4096u, 28>(W2_ARGS); });
      poll_done(pq);
      STAMP(11);
      const f32x16 y = reduce4(L, acc, w, lane);
      u32 o[8];
#pragma unroll
      for (int i = 0; i < 8; ++i) {
        const u32 bb = (i & 1) ? bias[i >> 1].y : bias[i >> 1].x;
        float gv[2];
#pragma unroll
        for (int e = 0; e < 2; ++e) {
          const float u = y[2 * i + e] + (e ? bhi(bb) : blo(bb));
          const float z = u * (-2.302208198f - 0.1029432397f * u * u);
          gv[e] = u * __builtin_amdgcn_rcpf(1.f + __builtin_amdgcn_exp2f(z));
        }
        o[i] = pk(gv[0], gv[1]);
      }
      const u32 ub = WS_UF + l * UF_L + (u32)((2 * dt4 + wb) * 112 + 4 * ft + 2 * wa) * 1024u;
      st_sc1(v4u{o[0], o[1], o[2], o[3]}, rws, lane * 16, ub);
      st_sc1(v4u{o[4], o[5], o[6], o[7]}, rws, lane * 16 + 1024, ub);
      publish(p.ws, flag_off(l, 3, dt4 * 32 + ft), tag);
      STAMP(12);
    } else {
      IDS
      {
        const rsrc_t rwf = mkrs(p.w2, F_ * S_ * 2);
        const int nb = (g & 1) ? 4 : 11, ib = bi - ((g & 1) ? 28 : 21);
        for (int row = ib * 4 + w; row < F_; row += nb * 4 * 8) {
          sfor<0, 8>([&](auto I) { constexpr int i = I; dma16<0, i * 1024>(rwf, lane * 16 + pj * 1024, (u32)(row + i * nb * 4) * 4096u, lds0 + ringw); });
          asm volatile("s_waitcnt vmcnt(16)" ::: "memory");
        }
        drain();
      }
      w_issue2<24, 24 + 2 * LA4, 4096u, 28>(W2_ARGS);
      drain();
    }
    if (p35) {
      IDS
      T5_PTRS
      u32 bias;
      asm volatile("s_nop 4\n\tbuffer_load_ushort %0, %1, %2, 0 offen" : "=v"(bias) : "v"((u32)((2 * st + wb) * 32 + r) * 2u), "s"(mkrs(p.b2, S_ * 2)));
      w_issue2<24 + 2 * LA4, 48, 4096u, 28>(W2_ARGS);
      w_take<12, 16, 1024>(t0, t1, bw5);
      lgk0();
      w_issue2<48, 56, 4096u, 28>(W2_ARGS);
      STAMP(13);
      const Poll pq = wait_flags(p.ws, flag_off(l, 3, dt * 32 + 7 * w + (lane < 7 ? lane : 0)), tag);
      asm volatile("" : "+v"(bias));
      STAMP(14);
      const u32 wis = l == 0 ? WS_WI + (u32)head * 86016u + (u32)w * 21504u : 0x40000000u;
      v4u wi[21];
      f32x16 acc[4];
      const u32 ub = WS_UF + l * UF_L + (u32)(28 * w) * 1024u;
      gemm_main<28, 16, 1024, false, LA5>(rws, lane * 16, ub + (u32)(2 * dt + wa) * 114688u, ub + (u32)(2 * dt + (wa ^ 1)) * 114688u, ROT5, t0, t1, bw5, acc,
                                          [&](auto I) { constexpr int i = I; wi[i] = lda<i>(rws, lane * 16, wis); });
      poll_done(pq);
      STAMP(15);
      const f32x16 y = reduce4(L, acc, w, lane);
      const float bv = blo(bias);
#pragma unroll
      for (int i = 0; i < 8; ++i) xres[i] = pk(blo(xres[i]) + (y[2 * i] + bv), bhi(xres[i]) + (y[2 * i + 1] + bv));
      v4u xo[2];
#pragma unroll
      for (int s = 0; s < 2; ++s) {
        auto a = __builtin_amdgcn_permlane32_swap(xres[4 * s], xres[4 * s + 2], false, false);
        auto c = __builtin_amdgcn_permlane32_swap(xres[4 * s + 1], xres[4 * s + 3], false, false);
        xo[s] = v4u{a[0], c[0], a[1], c[1]};
      }
      if (l == 0) {
        const u32 xb = WS_XF + (u32)((2 * st + wb) * 28 + 4 * dt) * 1024u + wa * 512;
        st_sc1(xo[0], rws, h * 1024 + r * 16, xb);
        st_sc1(xo[1], rws, h * 1024 + r * 16 + 2048, xb);
        float q0 = 0.f, q1 = 0.f;
#pragma unroll
        for (int i = 0; i < 8; i += 2) { q0 = __builtin_amdgcn_fdot2_f32_bf16(asbf2(xres[i]), asbf2(xres[i]), q0, false); q1 = __builtin_amdgcn_fdot2_f32_bf16(asbf2(xres[i + 1]), asbf2(xres[i + 1]), q1, false); }
        const float q = half_add(q0 + q1);
        if (h == 0) __builtin_amdgcn_raw_buffer_store_b32(asu(q), rws, r * 4, WS_SQ + (u32)((2 * st + wb) * 14 + 2 * dt + wa) * 128u, 16);
        publish(p.ws, flag_off(0, 4, st * 8 + dt), tag);
        sfor<2 * LA5, 21>([&](auto I) { constexpr int i = I; wi[i] = lda<i>(rws, lane * 16, wis); });
        sfor<0, 21>([&](auto I) { constexpr int i = I; vmw<(i < 2 * LA5) ? 21 - 2 * LA5 : 20 - i>(wi[i]); *(LAS v4u*)(L + w * 21504 + i * 1024 + lane * 16) = wi[i]; });
        bar();
      } else {
        sfor<0, 2 * LA5>([&](auto I) { constexpr int i = I; vmw<0>(wi[i]); });
        const rsrc_t rout = mkrs(p.out, S_ * D_ * 2);
        const u32 oo = (u32)((2 * st + wb) * 32 + r) * 896u + (u32)(dt * 64 + wa * 32 + 8 * h) * 2u;
        __builtin_amdgcn_raw_buffer_store_b128(xo[0], rout, oo, 0, 0);
        __builtin_amdgcn_raw_buffer_store_b128(xo[1], rout, oo + 32, 0, 0);
        if (b == 0 && tid == 0) __hip_atomic_store((GAS u64*)(p.ws + WS_EPOCH), tag, RLX_AGENT);
      }
      STAMP(16);
    } else if (l == 0) {
      IDS
      const Poll pq = wait_flags(p.ws, flag_off(0, 1, (lane & 15) * 16 + head), tag);
      const u32 wis = WS_WI + (u32)head * 86016u + (u32)w * 21504u;
      v4u wi[21];
      bar();
      sfor<0, 21>([&](auto I) { constexpr int i = I; wi[i] = lda<i>(rws, lane * 16, wis); });
      sfor<0, 21>([&](auto I) { constexpr int i = I; vmw<20 - i>(wi[i]); *(LAS v4u*)(L + w * 21504 + i * 1024 + lane * 16) = wi[i]; });
      poll_done(pq);
      bar();
    }
  }
  asm volatile(".Lfwd_last:");
}

extern "C" void kernel_launch(void* const* d_in, const int* in_sizes, int n_in, void* d_out, int out_size, void* d_ws, size_t ws_size,
                              hipStream_t stream) {
  Params p;
  p.x = d_in[0]; p.wqkv = d_in[1]; p.wo = d_in[2]; p.w1 = d_in[4]; p.b1 = d_in[5]; p.w2 = d_in[6]; p.b2 = d_in[7];
  p.out = d_out; p.ws = (unsigned char*)d_ws;
  (void)hipFuncSetAttribute((const void*)fused_fwd, hipFuncAttributeMaxDynamicSharedMemorySize, L_END);
  hipLaunchKernelGGL(fused_fwd, dim3(256), dim3(256), L_END, stream, p);
}
```

```cpp
#include <hip/hip_runtime.h>
#include <hip/hip_bf16.h>
#include <type_traits>

typedef unsigned u32;
typedef unsigned long long u64;
typedef u32 v4u __attribute__((ext_vector_type(4)));
typedef u32 v2u __attribute__((ext_vector_type(2)));
typedef float f32x16 __attribute__((ext_vector_type(16)));
typedef float f32x4 __attribute__((ext_vector_type(4)));
typedef float f32x2 __attribute__((ext_vector_type(2)));
typedef __bf16 bf16x8 __attribute__((ext_vector_type(8)));
typedef __bf16 bf16x2 __attribute__((ext_vector_type(2)));
typedef short s16x4 __attribute__((ext_vector_type(4)));
#define LAS __attribute__((address_space(3)))
#define GAS __attribute__((address_space(1)))
#define DI __device__ __forceinline__
#define RLX_AGENT __ATOMIC_RELAXED, __HIP_MEMORY_SCOPE_AGENT
#define SB() __builtin_amdgcn_sched_barrier(0)
typedef __amdgpu_buffer_rsrc_t rsrc_t;
typedef LAS unsigned char* lptr;

#define DBG_STAMP 0
#define DBG_WAVE 0
#define POLL_GAP "3"
#define TOUCH 1
#define QP0 12
#define NB 10
#define CODE_TOUCH 1
#define TLBT 0
#define LA4 6
#define LA5 6
#define LA1 12
#define LA3 6
#define ROT4 0
#define ROT5 0
#define ROT1 (head & 1)
#define WARM2 1

constexpr int S_ = 2048, D_ = 448, F_ = 1792;
constexpr u32 WS_EPOCH = 0, WS_TMO = 128;
constexpr u32 WS_FLAGS = 4096;
constexpr u32 WS_STATS = WS_FLAGS + 2 * 5 * 256 * 128;
constexpr u32 STATS_L = 64 * 14 * 512;
constexpr u32 WS_KV = 2u << 20, KV_L = 4u << 20;
constexpr u32 WS_OF = WS_KV + 2 * KV_L, OF_L = 2u << 20;
constexpr u32 WS_HF = WS_OF + 2 * OF_L, HF_L = 2u << 20;
constexpr u32 WS_UF = WS_HF + 2 * HF_L, UF_L = 2u << 20;
constexpr u32 WS_XF = WS_UF + 2 * UF_L;
constexpr u32 WS_SQ = WS_XF + 64 * 28 * 1024;
constexpr u32 WS_WI = WS_XF + (2u << 20);
constexpr u32 WS_END = WS_WI + (2u << 20);
constexpr u32 WS_DBG = WS_END;
static_assert(WS_STATS + 2 * STATS_L <= WS_KV, "ws map");
constexpr u32 L_RING = 0, RING_W = 24576;
constexpr u32 L_INBOX = 98304, INBOX_W = 12288;
constexpr u32 L_WQ = 57344;
constexpr u32 L_WO = 86016;
constexpr u32 L_CSP = 147456;
constexpr u32 L_CSF = L_CSP + 1536;
constexpr u32 L_MR = L_CSF + 2048;
constexpr u32 L_TR = L_MR + 1024;
constexpr u32 L_END = L_TR + 4 * 2304;
constexpr u32 kSpinCap = 1u << 16;
constexpr float EPS = 1e-5f;

struct Params {
  const void* x; const void* wqkv; const void* wo; const void* w1; const void* b1; const void* w2; const void* b2;
  void* out; unsigned char* ws;
};

DI f32x16 mfma(v4u a, v4u b, f32x16 c) {
  return __builtin_amdgcn_mfma_f32_32x32x16_bf16(__builtin_bit_cast(bf16x8, a), __builtin_bit_cast(bf16x8, b), c, 0, 0, 0);
}
DI f32x16 qk1(v4u k0, v4u q0, f32x16 ci) {
  f32x16 c;
  asm volatile("v_mfma_f32_32x32x16_bf16 %0, %1, %2, %3" : "=&v"(c) : "a"(k0), "v"(q0), "v"(ci));
  return c;
}
DI f32x16 qk1z(v4u k0, v4u q0) {
  f32x16 c;
  asm volatile("v_mfma_f32_32x32x16_bf16 %0, %1, %2, 0" : "=&v"(c) : "a"(k0), "v"(q0));
  return c;
}
DI void qk2(f32x16& c, v4u k1, v4u q1) { asm volatile("v_mfma_f32_32x32x16_bf16 %0, %1, %2, %0" : "+v"(c) : "a"(k1), "v"(q1)); }
DI f32x16 qk_own(v4u k0, v4u k1, v4u q0, v4u q1) {
  f32x16 c;
  asm volatile("s_nop 1\n\tv_mfma_f32_32x32x16_bf16 %0, %1, %3, 0\n\tv_mfma_f32_32x32x16_bf16 %0, %2, %4, %0" : "=&v"(c) : "v"(k0), "v"(k1), "v"(q0), "v"(q1));
  return c;
}
DI f32x16 qk_own_c(v4u k0, v4u k1, v4u q0, v4u q1, f32x16 ci) {
  f32x16 c;
  asm volatile("s_nop 1\n\tv_mfma_f32_32x32x16_bf16 %0, %1, %3, %5\n\tv_mfma_f32_32x32x16_bf16 %0, %2, %4, %0" : "=&v"(c) : "v"(k0), "v"(k1), "v"(q0), "v"(q1), "v"(ci));
  return c;
}
DI void tie(f32x16& c) { asm volatile("" : "+v"(c)); }
DI void tie_far(f32x16& c) { asm volatile("s_nop 7\n\ts_nop 4" : "+v"(c)); }
DI void nop2() { asm volatile("s_nop 1"); }
DI u32 pk(float lo, float hi) { f32x2 v = {lo, hi}; return __builtin_bit_cast(u32, __builtin_convertvector(v, bf16x2)); }
DI float asf(u32 v) { return __builtin_bit_cast(float, v); }
DI u32 asu(float v) { return __builtin_bit_cast(u32, v); }
DI bf16x2 asbf2(u32 v) { return __builtin_bit_cast(bf16x2, v); }
DI float blo(u32 v) { return __builtin_bit_cast(float, v << 16); }
DI float bhi(u32 v) { return __builtin_bit_cast(float, v & 0xffff0000u); }
DI void bar() { asm volatile("s_waitcnt lgkmcnt(0)\n\ts_barrier" ::: "memory"); }
DI void drain() { asm volatile("s_waitcnt vmcnt(0)" ::: "memory"); }
DI void lgk0() { asm volatile("s_waitcnt lgkmcnt(0)" ::: "memory"); }
template <u32 SI, u32 LI> DI void dma16(rsrc_t rs, u32 voff, u32 soff, u32 lds) {
  u32 t;
  asm volatile("s_add_u32 m0, %1, %5\n\ts_add_u32 %0, %4, %6\n\tbuffer_load_dwordx4 %2, %3, %0 offen lds"
               : "=&s"(t) : "s"(lds), "v"(voff), "s"(rs), "s"(soff), "i"(LI), "i"(SI) : "memory", "scc");
}
template <int K> DI v4u lda(rsrc_t rs, u32 voff, u32 soff) {
  v4u d; u32 t;
  asm volatile("s_add_u32 %1, %4, %5\n\tbuffer_load_dwordx4 %0, %2, %3, %1 offen offset:%6 sc1"
               : "=v"(d), "=&s"(t) : "v"(voff), "s"(rs), "s"(soff), "i"((K >> 2) * 4096), "i"((K & 3) * 1024) : "scc");
  return d;
}
template <int IMM> DI v4u ldp(rsrc_t rs, u32 voff, u32 soff) {
  v4u d;
  asm volatile("buffer_load_dwordx4 %0, %1, %2, %3 offen offset:%4" : "=v"(d) : "v"(voff), "s"(rs), "s"(soff), "i"(IMM));
  return d;
}
template <int IMM> DI v2u ldp2(rsrc_t rs, u32 voff) {
  v2u d;
  asm volatile("buffer_load_dwordx2 %0, %1, %2, 0 offen offset:%3" : "=v"(d) : "v"(voff), "s"(rs), "i"(IMM));
  return d;
}
DI void pin4(float (&v)[4]) { asm volatile("" : "+v"(v[0]), "+v"(v[1]), "+v"(v[2]), "+v"(v[3])); }
template <int IMM> DI u32 lda1(rsrc_t rs, u32 voff, u32 soff) {
  u32 d;
  asm volatile("buffer_load_dword %0, %1, %2, %3 offen offset:%4 sc1" : "=v"(d) : "v"(voff), "s"(rs), "s"(soff), "i"(IMM));
  return d;
}
template <int N> DI void vmw0() { asm volatile("s_waitcnt vmcnt(%0)" ::"i"(N) : "memory"); }
template <int N> DI void vmw(v4u& a) { asm volatile("s_waitcnt vmcnt(%1)" : "+v"(a) : "i"(N)); }
template <int N> DI void vmw(v4u& a, v4u& b, v4u& c, v4u& d) { asm volatile("s_waitcnt vmcnt(%4)" : "+v"(a), "+v"(b), "+v"(c), "+v"(d) : "i"(N)); }
template <int N> DI void vmw(v4u& a, v4u& b) { asm volatile("s_waitcnt vmcnt(%2)" : "+v"(a), "+v"(b) : "i"(N)); }
template <int I, int N, class Fn> DI void sfor(Fn&& f) { if constexpr (I < N) { f(std::integral_constant<int, I>{}); sfor<I + 1, N>(f); } }
DI u32 opq(u32 v) { asm volatile("" : "+s"(v)); return v; }
DI u32 opqv(u32 v) { asm volatile("" : "+v"(v)); return v; }
DI v2u trd(lptr p) { return __builtin_bit_cast(v2u, __builtin_amdgcn_ds_read_tr16_b64_v4i16((LAS s16x4*)p)); }
DI v4u trd2(lptr p0, lptr p1) { v2u a = trd(p0), b = trd(p1); return v4u{a.x, a.y, b.x, b.y}; }
DI v4u ld_sc1(rsrc_t rs, u32 voff, u32 soff) { return __builtin_amdgcn_raw_buffer_load_b128(rs, voff, soff, 16); }
DI void st_sc1(v4u v, rsrc_t rs, u32 voff, u32 soff) { __builtin_amdgcn_raw_buffer_store_b128(v, rs, voff, soff, 16); }
DI rsrc_t mkrs(const void* p, u32 bytes) {
  u32 fl = 0x00020000;
  asm volatile("" : "+s"(p), "+s"(bytes), "+s"(fl));
  return __builtin_amdgcn_make_buffer_rsrc((void*)p, 0, bytes, fl);
}
DI float half_add(float v) {
  auto r = __builtin_amdgcn_permlane32_swap(asu(v), asu(v), false, false);
  const u32 a = r[0], b = r[1];
  return asf(a) + asf(b);
}
DI float half_max(float v) {
  auto r = __builtin_amdgcn_permlane32_swap(asu(v), asu(v), false, false);
  const u32 a = r[0], b = r[1];
  return fmaxf(asf(a), asf(b));
}
DI bool spin_out(unsigned char* ws, u32& spins, u32 tl) {
  ++spins;
  if ((spins & 63u) == 0u) {
    GAS u32* tmo = (GAS u32*)(ws + WS_TMO);
    if (__hip_atomic_load(tmo, RLX_AGENT) == tl) return true;
    if (spins > kSpinCap) { __hip_atomic_store(tmo, tl, RLX_AGENT); return true; }
  }
  return false;
}
struct Poll { v2u a, b, c; };
#define PL(R, O, B) "global_load_dwordx2 " R ", " O ", " B " sc1\n\t"
DI v2u poll_one(unsigned char* ws, u32 off) {
  v2u r;
  asm volatile(PL("%0", "%1", "%2") : "=v"(r) : "v"(off), "s"(ws) : "memory");
  return r;
}
DI void poll_finish(Poll& q, unsigned char* ws, u32 off, u64 tag) {
  u32 left;
#define PC(R) "s_waitcnt vmcnt(2)\n\tv_cmp_ne_u64 vcc, %6, " R "\n\ts_cbranch_vccz 2f\n\t" PL(R, "%4", "%5")
  asm volatile("s_mov_b32 %3, 0x8000\n1:\n\t" PC("%0") PC("%1") PC("%2") "s_sub_u32 %3, %3, 1\n\ts_cbranch_scc0 1b\n2:"
               : "+v"(q.a), "+v"(q.b), "+v"(q.c), "=&s"(left) : "v"(off), "s"(ws), "s"(tag) : "vcc", "scc", "memory");
#undef PC
  if (left == 0xffffffffu) { drain(); __hip_atomic_store((GAS u32*)(ws + WS_TMO), (u32)tag, RLX_AGENT); }
}
DI Poll wait_flags(unsigned char* ws, u32 off, u64 tag) {
  Poll q;
  q.a = poll_one(ws, off); asm volatile("s_sleep " POLL_GAP);
  q.b = poll_one(ws, off); asm volatile("s_sleep " POLL_GAP);
  q.c = poll_one(ws, off);
  poll_finish(q, ws, off, tag);
  return q;
}
DI void poll_done(const Poll& q) { asm volatile("" ::"v"(q.a), "v"(q.b), "v"(q.c)); }
DI u32 touch(rsrc_t rs, u32 voff, u32 soff) {
  u32 d;
  asm volatile("s_nop 4\n\tbuffer_load_dword %0, %1, %2, %3 offen" : "=v"(d) : "v"(voff), "s"(rs), "s"(soff));
  return d;
}
DI void touch_done(u32 d) { asm volatile("" ::"v"(d)); }
DI void publish(unsigned char* ws, u32 off, u64 tag) {
  asm volatile("s_waitcnt vmcnt(0) lgkmcnt(0)\n\ts_barrier" ::: "memory");
  if (threadIdx.x == 0) __hip_atomic_store((GAS u64*)(ws + off), tag, RLX_AGENT);
  asm volatile("" ::: "memory");
}
DI u32 flag_off(int l, int type, int idx) { return WS_FLAGS + (u32)(((l * 5 + type) << 8) + idx) * 128u; }

#define STAMP(i) do {} while (0)

DI f32x16 reduce4(lptr L, const f32x16 (&acc)[4], int w, int lane) {
#pragma unroll
  for (int q = 1; q < 4; ++q) {
    lptr dst = L + L_INBOX + (u32)(w ^ q) * INBOX_W + (q - 1) * 4096 + lane * 16;
#pragma unroll
    for (int i = 0; i < 4; ++i)
      *(LAS f32x4*)(dst + i * 1024) = f32x4{acc[q][4 * i], acc[q][4 * i + 1], acc[q][4 * i + 2], acc[q][4 * i + 3]};
  }
  bar();
  f32x16 r = acc[0];
  lptr src = L + L_INBOX + (u32)w * INBOX_W + lane * 16;
#pragma unroll
  for (int q = 0; q < 3; ++q)
#pragma unroll
    for (int i = 0; i < 4; ++i) {
      const f32x4 v = *(LAS f32x4*)(src + q * 4096 + i * 1024);
      r[4 * i] += v[0]; r[4 * i + 1] += v[1]; r[4 * i + 2] += v[2]; r[4 * i + 3] += v[3];
    }
  return r;
}

template <int P0, int P1, u32 RS> DI void w_issue(rsrc_t rs, u32 voff, u32 soff, u32 ring) {
  sfor<P0, P1>([&](auto I) { constexpr int i = I; dma16<i * 8 * RS, (i % 24) * 1024>(rs, voff, soff, ring); });
}
template <int P0, int P1, u32 RS, int HP> DI void w_issue2(rsrc_t rs, u32 voff, u32 s0, u32 s1, u32 ring) {
  sfor<P0, P1>([&](auto I) { constexpr int i = I; dma16<(i % HP) * 8 * RS, (i % 24) * 1024>(rs, voff, i < HP ? s0 : s1, ring); });
}
template <int K0, int K1, int TSTEP, int NR> DI void w_take(lptr t0, lptr t1, v4u (&bw)[NR][2]) {
#pragma unroll
  for (int kk = K0; kk < K1; ++kk) {
    const int sl = (kk % 12) * 2048;
    bw[kk][0] = trd2(t0 + sl, t0 + sl + TSTEP);
    bw[kk][1] = trd2(t1 + sl, t1 + sl + TSTEP);
  }
}
template <int NR> DI void w_undef(v4u (&bw)[NR][2], int k0, int k1) {
#pragma unroll
  for (int kk = 0; kk < NR; ++kk) if (kk >= k0 && kk < k1) { asm volatile("" : "=v"(bw[kk][0])); asm volatile("" : "=v"(bw[kk][1])); }
}

template <int NK, int NR, int TSTEP, bool WA, int LA, class Tail>
DI void gemm_main(rsrc_t ws, u32 voff, u32 a0, u32 a1, u32 rot, lptr t0, lptr t1, const v4u (&bw)[NR][2], f32x16 (&acc)[4], Tail&& tail) {
  static_assert(NK - NR == 12 && NK - LA >= 13, "ring is 12 slots and must be consumed before the tail");
  v4u a[NK][2];
  constexpr int HK = NK / 2;
  const u32 b0[2] = {a0 + rot * (HK * 1024), a0 + (rot ^ 1u) * (HK * 1024)}, b1[2] = {a1 + rot * (HK * 1024), a1 + (rot ^ 1u) * (HK * 1024)};
  sfor<0, LA>([&](auto I) { constexpr int n = I, k = (n + NR) % NK; a[n][0] = lda<k % HK>(ws, voff, b0[k / HK]); a[n][1] = lda<k % HK>(ws, voff, b1[k / HK]); });
  v4u wl[12][2];
  wl[0][0] = trd2(t0 + (NR % 12) * 2048, t0 + (NR % 12) * 2048 + TSTEP); wl[0][1] = trd2(t1 + (NR % 12) * 2048, t1 + (NR % 12) * 2048 + TSTEP);
#pragma unroll
  for (int q = 0; q < 4; ++q)
#pragma unroll
    for (int i = 0; i < 16; ++i) acc[q][i] = 0.f;
  wl[1][0] = trd2(t0 + ((NR + 1) % 12) * 2048, t0 + ((NR + 1) % 12) * 2048 + TSTEP); wl[1][1] = trd2(t1 + ((NR + 1) % 12) * 2048, t1 + ((NR + 1) % 12) * 2048 + TSTEP);
  SB();
  sfor<0, NK>([&](auto I) {
    constexpr int n = I, kn = (n + LA + NR) % NK, sl = ((NR + n + 2) % 12) * 2048;
    vmw<2 * LA - 2>(a[n][0], a[n][1]);
    v4u w0, w1;
    if constexpr (n < 12) { w0 = wl[n][0]; w1 = wl[n][1]; } else { w0 = bw[n - 12][0]; w1 = bw[n - 12][1]; }
    acc[1] = WA ? mfma(w0, a[n][1], acc[1]) : mfma(a[n][0], w1, acc[1]);
    SB();
    if constexpr (n + LA < NK) a[n + LA][0] = lda<kn % HK>(ws, voff, b0[kn / HK]); else tail(std::integral_constant<int, 2 * (n + LA - NK)>{});
    SB();
    acc[2] = WA ? mfma(w1, a[n][0], acc[2]) : mfma(a[n][1], w0, acc[2]);
    SB();
    if constexpr (n + 2 < 12) wl[n + 2][0] = trd2(t0 + sl, t0 + sl + TSTEP);
    SB();
    acc[3] = WA ? mfma(w1, a[n][1], acc[3]) : mfma(a[n][1], w1, acc[3]);
    SB();
    if constexpr (n + LA < NK) a[n + LA][1] = lda<kn % HK>(ws, voff, b1[kn / HK]); else tail(std::integral_constant<int, 2 * (n + LA - NK) + 1>{});
    SB();
    acc[0] = WA ? mfma(w0, a[n][0], acc[0]) : mfma(a[n][0], w0, acc[0]);
    SB();
    if constexpr (n + 2 < 12) wl[n + 2][1] = trd2(t1 + sl, t1 + sl + TSTEP);
    SB();
  });
}

__global__ __launch_bounds__(256, 1) void fused_fwd(Params p) {
  extern __shared__ __attribute__((aligned(16))) unsigned char smem[];
  lptr L = (lptr)smem;
  const u32 lds0 = (u32)(size_t)L;
#define IDS \
  const int tid = (int)opqv(threadIdx.x), lane = tid & 63, r = lane & 31, h = lane >> 5; \
  const int w = (int)opq(w_), b = (int)opq(blockIdx.x), g = b & 7, bi = b >> 3, pj = g >> 1; \
  const int T = 2 * g + (bi >> 4), head = bi & 15; \
  const int d3 = (bi * 43) >> 7, m3 = bi - 3 * d3; \
  const int st = 8 * pj + ((g & 1) ? d3 : (bi >> 2)), dt = (g & 1) ? 4 + m3 : (bi & 3); \
  const int ft = 7 * pj + ((g & 1) ? (bi >> 2) : d3), dt4 = (g & 1) ? (bi & 3) : 4 + m3; \
  const int wa = w >> 1, wb = w & 1; \
  const int ch16 = (lane >> 4) & 1, tq = (lane & 15) >> 2, tp = lane & 3; \
  const u32 ringw = L_RING + (u32)w * RING_W; \
  const u32 dmarow = lane >> 3, dmach = ((lane & 7) ^ (((lane >> 4) & 1) << 2)) * 16; \
  const u32 sw = (u32)((tq >> 1) & 1) << 2, cpart = 2 * ch16 + (tp >> 1); \
  (void)r; (void)h; (void)T; (void)head; (void)st; (void)dt; (void)ft; (void)dt4; (void)wa; (void)wb; (void)ch16; (void)tq; (void)tp; (void)ringw; (void)dmarow; (void)dmach; (void)sw; (void)cpart;
  const u32 w_ = __builtin_amdgcn_readfirstlane(threadIdx.x >> 6);
  const bool p35 = (blockIdx.x & 1) == 0 || (blockIdx.x >> 3) < 24;
  const bool p4 = (blockIdx.x >> 3) < ((blockIdx.x & 1) ? 28u : 21u);

  const rsrc_t rws = mkrs(p.ws, WS_END);
  u64 tag = 0;
  u32 xres[8];
  v4u wf[8][2];
#pragma unroll
  for (int i = 0; i < 8; ++i) { xres[i] = 0; asm volatile("" : "=a"(wf[i][0])); asm volatile("" : "=a"(wf[i][1])); }

#pragma unroll 1
  for (int l = 0; l < 2; ++l) {
    STAMP(0);
    const rsrc_t rw1 = mkrs(p.w1, p4 ? S_ * F_ * 2 : 0);
    const rsrc_t rw2 = mkrs(p.w2, p35 ? F_ * S_ * 2 : 0);
#define W2_ARGS rw2, dmarow * 4096u + dmach + (u32)st * 128u, (u32)(448 * w + 224 * ROT5) * 4096u, (u32)(448 * w + 224 * (ROT5 ^ 1)) * 4096u, lds0 + ringw
#define W1_ARGS rw1, dmarow * 3584u + dmach + (u32)ft * 128u, (u32)(512 * w + 256 * ROT4) * 3584u, (u32)(512 * w + 256 * (ROT4 ^ 1)) * 3584u, lds0 + ringw
    {
    IDS
    const int tb = 4 * T + w;
    const u32 shift = (head & 1) * 8;
    const u32 half = (((tq >> 1) & 1) ^ 1) * 64, col = shift + ch16 * 32 + tp * 8;
    lptr wtk = L + (32 * h + tq) * 128 + half + col, wtv = L + (32 * h + tq) * 128 + (half ^ 64) + col, wtq = L + L_WQ + (32 * h + tq) * 64 + col;
    v4u X[28];
    f32x16 kt, vv;
#pragma unroll
    for (int i = 0; i < 16; ++i) { kt[i] = 0.f; vv[i] = 0.f; }
    float ssq;
    u32 tc2 = 0, tc3 = 0, tc4 = 0, tc5 = 0, tc6 = 0;
    v4u wk[28], wv[28], wq[28];
    constexpr int PD = 3;
#define WROW(k) (((k) >> 2) * 64 + ((k) & 3) * 8)
#define WFK(k) trd2(wtk + WROW(k) * 128, wtk + WROW(k) * 128 + 512)
#define WFV(k) trd2(wtv + WROW(k) * 128, wtv + WROW(k) * 128 + 512)
#define WFQ(k) trd2(wtq + WROW(k) * 64, wtq + WROW(k) * 64 + 256)
    auto kv_head = [&]() {
#pragma unroll
      for (int k = 0; k < PD; ++k) { wk[k] = WFK(k); wv[k] = WFV(k); }
      SB();
    };
    auto kv_step = [&](auto I, auto&& mem) {
      constexpr int k = I;
      kt = mfma(wk[k], X[k], kt);
      SB();
      if constexpr (k + PD < 28) wk[k + PD] = WFK(k + PD);
      mem();
      SB();
      vv = mfma(X[k], wv[k], vv);
      SB();
      if constexpr (k + PD < 28) wv[k + PD] = WFV(k + PD);
      SB();
    };
    const v4u ones = {0x3F803F80u, 0x3F803F80u, 0x3F803F80u, 0x3F803F80u};
    if (l == 0) {
      v2u e0;
      asm volatile("global_load_dwordx2 %0, %1, %2 sc1" : "=v"(e0) : "v"(WS_EPOCH), "s"(p.ws));
      u32 tcc;
      {
        u64 pc; u32 back, len;
        asm volatile("s_getpc_b64 %0\n.Lfwd_here:\n\ts_mov_b32 %1, .Lfwd_here-_Z9fused_fwd6Params\n\ts_mov_b32 %2, .Lfwd_last-_Z9fused_fwd6Params" : "=s"(pc), "=s"(back), "=s"(len));
        tcc = touch(mkrs((const void*)(pc - back), CODE_TOUCH ? len : 0), lane * 128, (u32)(bi * 4 + w) * 8192u);
      }
      const rsrc_t rx = mkrs(p.x, S_ * D_ * 2), rwq = mkrs(p.wqkv, D_ * 3 * D_ * 2);
      const u32 hoff = (u32)(head * 56) - shift;
      const u32 xv = dmarow * 896u + (lane & 7) * 16u, xs = (u32)(tb * 32) * 896u;
      lptr xb = L + L_WO + w * 15360;
      lptr xw = xb + dmarow * 128, xr = xb + r * 128;
      const u32 xsw = (r >> 1) & 7, xc0 = ((lane & 7) ^ (lane >> 4)) * 16;
      const u32 kvv = dmarow * 2688u + 896u + ((((lane >> 2) ^ (lane >> 4) ^ 1) & 1) * 896u) + (lane & 3) * 16u + hoff;
      const u32 kvs = (u32)(112 * w) * 2688u, kvl0 = lds0 + (u32)(14 * w) * 1024u;
      const rsrc_t rwo = mkrs(p.wo, D_ * D_ * 2);
      const u32 wov = dmarow * 896u + dmach + (u32)dt * 128u, wos = (u32)(112 * w) * 896u, wol = lds0 + L_WO + (u32)w * 15360u;
      const u32 qv = (u32)(lane >> 2) * 2688u + (lane & 3) * 16u + hoff, ql = lds0 + L_WQ + (u32)(7 * w) * 1024u;
      sfor<0, 28>([&](auto I) { constexpr int k = I; X[k] = ldp<(k >> 2) * 128>(rx, xv, xs + (k & 3) * 7168u); });
      sfor<0, 7>([&](auto I) { constexpr int i = I; dma16<i * 8 * 2688, i * 1024>(rwq, kvv, kvs, kvl0); });
      STAMP(24);
      float sq4[4] = {0.f, 0.f, 0.f, 0.f};
      sfor<0, 7>([&](auto I) {
        constexpr int G = I;
        vmw<24 - 4 * G + 7 + 2 * G>(X[4 * G], X[4 * G + 1], X[4 * G + 2], X[4 * G + 3]);
#pragma unroll
        for (int i = 0; i < 4; ++i) *(LAS v4u*)(xw + i * 1024 + (xc0 ^ ((i & 1) * 64))) = X[4 * G + i];
#pragma unroll
        for (int c = 0; c < 4; ++c) X[4 * G + c] = *(LAS v4u*)(xr + (((4 * h + c) ^ xsw) * 16));
        if constexpr (2 * G < 7) dma16<(7 + 2 * G) * 8 * 2688, (7 + 2 * G) * 1024>(rwq, kvv, kvs, kvl0); else dma16<(2 * G - 7) * 16 * 2688, (2 * G - 7) * 1024>(rwq, qv, kvs, ql);
        if constexpr (2 * G + 1 < 7) dma16<(8 + 2 * G) * 8 * 2688, (8 + 2 * G) * 1024>(rwq, kvv, kvs, kvl0); else dma16<(2 * G - 6) * 16 * 2688, (2 * G - 6) * 1024>(rwq, qv, kvs, ql);
#pragma unroll
        for (int i = 0; i < 16; ++i) { const bf16x2 xx = asbf2(X[4 * G + (i >> 2)][i & 3]); sq4[i & 3] = __builtin_amdgcn_fdot2_f32_bf16(xx, xx, sq4[i & 3], false); }
        pin4(sq4);
        SB();
      });
      ssq = (sq4[0] + sq4[1]) + (sq4[2] + sq4[3]);
      STAMP(25);
      f32x16 ck, cv;
#pragma unroll
      for (int i = 0; i < 16; ++i) { ck[i] = 0.f; cv[i] = 0.f; }
      lptr ck0 = L + (112 * w + 8 * h + tq) * 128 + half + col, cv0 = L + (112 * w + 8 * h + tq) * 128 + (half ^ 64) + col;
      vmw0<7>();
      STAMP(26);
      {
        v4u f[14];
#pragma unroll
        for (int i = 0; i < 7; ++i) { f[2 * i] = trd2(ck0 + i * 2048, ck0 + i * 2048 + 512); f[2 * i + 1] = trd2(cv0 + i * 2048, cv0 + i * 2048 + 512); }
        SB();
        sfor<0, 7>([&](auto I) { constexpr int i = I;
          ck = mfma(ones, f[2 * i], ck); SB(); w_issue<2 * i, 2 * i + 1, 896u>(rwo, wov, wos, wol); SB();
          cv = mfma(ones, f[2 * i + 1], cv); SB(); w_issue<2 * i + 1, 2 * i + 2, 896u>(rwo, wov, wos, wol); SB(); });
        w_issue<14, 15, 896u>(rwo, wov, wos, wol);
      }
      {
        const u32 gw = (u32)(b * 4 + w);
        const u32 l2 = gw * 24 + (lane < 24 ? lane : 0), r2 = l2 >> 5, l1 = gw * 21 + (lane < 21 ? lane : 0), r1 = l1 / 28;
        tc2 = touch(mkrs(p.w2, TOUCH ? S_ * F_ * 2 : 0), ((r2 / 192) * 448 + r2 % 192) * 4096u + (l2 & 31) * 128u, 0);
        tc3 = touch(mkrs(p.w1, TOUCH ? S_ * F_ * 2 : 0), ((r1 / 192) * 512 + r1 % 192) * 3584u + (l1 % 28) * 128u, 0);
      }
      STAMP(27);
      touch_done(tcc);
      asm volatile("" : "+v"(e0));
      tag = (((u64)(u32)__builtin_amdgcn_readfirstlane(e0.y) << 32) | (u64)(u32)__builtin_amdgcn_readfirstlane(e0.x)) + 1ull;
      *(LAS float*)(L + L_CSP + w * 384 + 128 + lane * 4) = h ? cv[0] : ck[0];
      bar();
      {
        lptr c28 = L + (112 * w + lane) * 128 + (((lane >> 1) & 1) ^ 1) * 64 + shift + 56;
        *(LAS unsigned short*)c28 = 0x3F80;
        if (lane < 48) *(LAS unsigned short*)(c28 + 64 * 128) = 0x3F80;
        float a0 = 0.f;
#pragma unroll
        for (int v = 0; v < 4; ++v) a0 += *(LAS float*)(L + L_CSP + v * 384 + 128 + lane * 4);
        *(LAS float*)(L + L_CSF + w * 512 + 128 + lane * 4) = a0;
      }
      bar();
      kv_head();
      STAMP(1);
      SB();
      sfor<0, 28>([&](auto I) { kv_step(I, []() {}); });
      asm volatile("" : "+a"(kt), "+a"(vv));
    } else {
      kv_head();
      const Poll pq = wait_flags(p.ws, flag_off(0, 4, (2 * T + (w >> 1)) * 8 + (lane < 7 ? lane : 0)), tag);
      STAMP(1);
      const u32 xb = WS_XF + (u32)(tb * 28) * 1024u;
      const u32 sqv = (u32)(7 * h) * 128u + r * 4, sqs = WS_SQ + (u32)tb * 1792u;
      u32 sp[7];
      sfor<0, LA1>([&](auto I) { constexpr int k = I; X[k] = lda<k>(rws, lane * 16, xb); });
      sfor<0, 28>([&](auto I) {
        constexpr int k = I;
        vmw<(k + LA1 <= 35) ? LA1 - 1 : 34 - k>(X[k]);
        if constexpr (k == 0) poll_done(pq);
        kv_step(I, [&]() {
          if constexpr (k + LA1 < 28) X[k + LA1] = lda<k + LA1>(rws, lane * 16, xb);
          else if constexpr (k + LA1 < 35) sp[k + LA1 - 28] = lda1<(k + LA1 - 28) * 128>(rws, sqv, sqs);
        });
      });
      asm volatile("s_waitcnt vmcnt(0)" : "+v"(sp[0]), "+v"(sp[1]), "+v"(sp[2]), "+v"(sp[3]), "+v"(sp[4]), "+v"(sp[5]), "+v"(sp[6]));
      asm volatile("" : "+a"(kt), "+a"(vv));
      ssq = ((asf(sp[0]) + asf(sp[1])) + (asf(sp[2]) + asf(sp[3]))) + ((asf(sp[4]) + asf(sp[5])) + asf(sp[6]));
    }
    STAMP(18);
    float mean, rstd;
    {
      const float sum = half_add(h ? kt[12] : 0.f);
      const float sq = half_add(ssq);
      mean = sum * (1.f / 448.f);
      const float var = fmaxf(sq * (1.f / 448.f) - mean * mean, 0.f);
      rstd = __builtin_amdgcn_rsqf(var + EPS);
    }
    const float mr = mean * rstd;
    *(LAS float*)(L + L_MR + w * 256 + h * 128 + r * 4) = h ? mr : rstd;
    const u32 kvbase = WS_KV + l * KV_L + (u32)head * (64u * 4096u);
    lptr kvl = L + L_INBOX + lane * 16;
    v4u K0, K1, V0, V1;
    {
      u32 o[8];
#pragma unroll
      for (int i = 0; i < 4; ++i) {
        const f32x4 cs = *(LAS f32x4*)(L + L_CSF + w * 512 + 128 + (8 * i + 4 * h) * 4);
        o[2 * i] = pk(kt[4 * i] * rstd - mr * cs[0], kt[4 * i + 1] * rstd - mr * cs[1]);
        o[2 * i + 1] = pk(kt[4 * i + 2] * rstd - mr * cs[2], kt[4 * i + 3] * rstd - mr * cs[3]);
      }
      if (h) { o[6] = 0u; o[7] = 0u; }
      K0 = v4u{o[0], o[1], o[2], o[3]}; K1 = v4u{o[4], o[5], o[6], o[7]};
      st_sc1(K0, rws, lane * 16, kvbase + tb * 4096);
      st_sc1(K1, rws, lane * 16 + 1024, kvbase + tb * 4096);
      const float csv = *(LAS float*)(L + L_CSF + w * 512 + 256 + r * 4);
#pragma unroll
      for (int i = 0; i < 4; ++i) {
        const f32x4 rr = *(LAS f32x4*)(L + L_MR + w * 256 + (8 * i + 4 * h) * 4);
        const f32x4 mm = *(LAS f32x4*)(L + L_MR + w * 256 + 128 + (8 * i + 4 * h) * 4);
        o[2 * i] = pk(vv[4 * i] * rr[0] - mm[0] * csv, vv[4 * i + 1] * rr[1] - mm[1] * csv);
        o[2 * i + 1] = pk(vv[4 * i + 2] * rr[2] - mm[2] * csv, vv[4 * i + 3] * rr[3] - mm[3] * csv);
      }
      if (r >= 28) {
#pragma unroll
        for (int i = 0; i < 8; ++i) o[i] = 0x3F803F80u;
      }
      V0 = v4u{o[0], o[1], o[2], o[3]}; V1 = v4u{o[4], o[5], o[6], o[7]};
      st_sc1(V0, rws, lane * 16 + 2048, kvbase + tb * 4096);
      st_sc1(V1, rws, lane * 16 + 3072, kvbase + tb * 4096);
    }
    STAMP(19);
    if (l == 0) {
      vmw0<TLBT ? 8 : 6>();
      lptr t0 = L + L_WO + w * 15360 + (4 * h + tq) * 128 + (((4 * wa + cpart) ^ sw) * 16) + (tp & 1) * 8;
      lptr t1 = L + L_WO + w * 15360 + (4 * h + tq) * 128 + (((4 * (wa ^ 1) + cpart) ^ sw) * 16) + (tp & 1) * 8;
      lptr cq0 = L + L_WQ + (112 * w + 8 * h + tq) * 64 + col;
      v4u f[7];
#pragma unroll
      for (int i = 0; i < 7; ++i) f[i] = trd2(cq0 + i * 1024, cq0 + i * 1024 + 256);
#pragma unroll
      for (int k = 0; k < 8; ++k) {
        const int ro = (28 * (k >> 1) + 16 * (k & 1)) * 128;
        wf[k][0] = trd2(t0 + ro, t0 + ro + 1024); wf[k][1] = trd2(t1 + ro, t1 + ro + 1024);
      }
      SB();
      f32x16 cq;
#pragma unroll
      for (int i = 0; i < 16; ++i) cq[i] = 0.f;
#pragma unroll
      for (int i = 0; i < 7; ++i) cq = mfma(ones, f[i], cq);
      *(LAS float*)(L + L_CSP + w * 384 + r * 4) = cq[0];
      st_sc1(v4u{0u, 0u, 0u, 0u}, rws, head < 14 ? WS_STATS + h * STATS_L + (u32)((tb * 14 + head) * 512 + r * 16) : 0x40000000u, 0);
    }
    publish(p.ws, flag_off(l, 0, T * 16 + head), tag);
    touch_done(tc2); touch_done(tc3); touch_done(tc5); touch_done(tc6);
    STAMP(2);
#pragma unroll
    for (int k = 0; k < 2 * PD; ++k) wq[k] = WFQ(k);
    {
      lptr own = kvl + (8 + w) * 4096;
      *(LAS v4u*)own = K0; *(LAS v4u*)(own + 1024) = K1; *(LAS v4u*)(own + 2048) = V0; *(LAS v4u*)(own + 3072) = V1;
    }
    if (l == 0) {
      float a0 = 0.f;
#pragma unroll
      for (int v = 0; v < 4; ++v) a0 += *(LAS float*)(L + L_CSP + v * 384 + r * 4);
      *(LAS float*)(L + L_CSF + w * 512 + r * 4) = a0;
      const u32 wib = WS_WI + (u32)head * 86016u + (u32)tb * 1024u;
      st_sc1(*(LAS v4u*)(L + tb * 1024 + lane * 16), rws, lane * 16, wib);
      st_sc1(*(LAS v4u*)(L + (tb < 20 ? tb + 64 : tb) * 1024 + lane * 16), rws, lane * 16, tb < 20 ? wib + 65536u : 0x40000000u);
      const u32 xo = (u32)((2 * st + wb) * 32 + r) * 896u + (u32)(dt * 64 + wa * 32 + 4 * h) * 2u;
      const rsrc_t rx = mkrs(p.x, S_ * D_ * 2);
#pragma unroll
      for (int i = 0; i < 4; ++i) {
        const v2u v = __builtin_amdgcn_raw_buffer_load_b64(rx, xo + i * 16, 0, 0);
        xres[2 * i] = v.x; xres[2 * i + 1] = v.y;
      }
    }
    {
      const int tsrc = T - 2 + (w >> 1);
      const u32 f1 = opqv(flag_off(l, 0, (tsrc < 0 ? T : tsrc) * 16 + head));
      Poll pq;
      f32x16 qt;
#pragma unroll
      for (int i = 0; i < 16; ++i) qt[i] = 0.f;
      SB();
      sfor<0, 28>([&](auto I) {
        constexpr int k = I;
        qt = mfma(wq[k], X[k], qt);
        SB();
        if constexpr (k + 2 * PD < 28) wq[k + 2 * PD] = WFQ(k + 2 * PD);
        if constexpr (k == QP0) pq.a = poll_one(p.ws, f1);
        if constexpr (k == QP0 + 6) pq.b = poll_one(p.ws, f1);
        if constexpr (k == QP0 + 12) pq.c = poll_one(p.ws, f1);
        SB();
      });
      asm volatile("" : "+a"(qt));
      poll_finish(pq, p.ws, f1, tag);
      STAMP(3);
      const u32 so = tsrc >= 0 ? kvbase + (u32)(4 * T - 8 + 2 * w) * 4096u : 0x40000000u;
      v4u t[8];
      t[0] = lda<0>(rws, lane * 16, so); t[1] = lda<1>(rws, lane * 16, so); t[2] = lda<4>(rws, lane * 16, so); t[3] = lda<5>(rws, lane * 16, so);
      t[4] = lda<2>(rws, lane * 16, so); t[5] = lda<3>(rws, lane * 16, so); t[6] = lda<6>(rws, lane * 16, so); t[7] = lda<7>(rws, lane * 16, so);
      SB();
      v4u Q0, Q1;
      {
        const float sc = 0.18898223650461362f * 1.4426950408889634f;
        const float rs = rstd * sc, ms = mr * sc;
        u32 o[8];
#pragma unroll
        for (int i = 0; i < 4; ++i) {
          const f32x4 cs = *(LAS f32x4*)(L + L_CSF + w * 512 + (8 * i + 4 * h) * 4);
          o[2 * i] = pk(qt[4 * i] * rs - ms * cs[0], qt[4 * i + 1] * rs - ms * cs[1]);
          o[2 * i + 1] = pk(qt[4 * i + 2] * rs - ms * cs[2], qt[4 * i + 3] * rs - ms * cs[3]);
        }
        if (h) { o[6] = 0x00003F80u; o[7] = 0u; }
        Q0 = v4u{o[0], o[1], o[2], o[3]}; Q1 = v4u{o[4], o[5], o[6], o[7]};
      }
      f32x16 s8 = qk_own(K0, K1, Q0, Q1);
      u32 em[16], pm[8];
      f32x16 c0;
#pragma unroll
      for (int i = 0; i < 16; ++i) { const int key = (i & 3) + 8 * (i >> 2) + 4 * h; em[i] = key > r ? 0xffffffffu : 0u; c0[i] = asf(~em[i] & 0xF149F2CAu); }
#pragma unroll
      for (int i = 0; i < 8; ++i) pm[i] = (em[2 * i] & 0x0000ffffu) | (em[2 * i + 1] & 0xffff0000u);
#pragma unroll
      for (int i = 0; i < 16; ++i) asm volatile("" : "+v"(em[i]));
#pragma unroll
      for (int i = 0; i < 8; ++i) asm volatile("" : "+v"(pm[i]));
      tie_far(s8);
      float m4[4];
#pragma unroll
      for (int i = 0; i < 16; ++i) s8[i] = asf((em[i] & 0xF149F2CAu) | (~em[i] & asu(s8[i])));
#pragma unroll
      for (int i = 0; i < 4; ++i) m4[i] = fmaxf(fmaxf(s8[4 * i], s8[4 * i + 1]), fmaxf(s8[4 * i + 2], s8[4 * i + 3]));
      asm volatile("" : "+v"(c0), "+v"(m4[0]), "+v"(m4[1]), "+v"(m4[2]), "+v"(m4[3]));
      SB();
      {
        lptr dst = kvl + w * 8192;
        vmw<4>(t[0], t[1], t[2], t[3]);
        poll_done(pq);
        if (tsrc < 0 && h) { t[1][2] = 0xF149u; t[3][2] = 0xF149u; }
#pragma unroll
        for (int i = 0; i < 4; ++i) *(LAS v4u*)(dst + (i & 1) * 1024 + (i >> 1) * 4096) = t[i];
        vmw<0>(t[4], t[5], t[6], t[7]);
#pragma unroll
        for (int i = 0; i < 4; ++i) *(LAS v4u*)(dst + 2048 + (i & 1) * 1024 + (i >> 1) * 4096) = t[4 + i];
      }
      bar();
      {
        const u32 l1 = (u32)(b * 4 + w) * 35 + (lane < 35 ? lane : 0), r1 = l1 / 28;
        tc4 = touch(mkrs(p.w1, (TOUCH && l == 0) ? S_ * F_ * 2 : 0), ((r1 / 320) * 512 + 192 + r1 % 320) * 3584u + (l1 % 28) * 128u, 0);
      }
      STAMP(17);
      lptr kq = kvl + w * 4096;
      v4u Kr[8][2], Vr[8][2];
#pragma unroll
      for (int j = 0; j < 4; ++j) { Kr[j][0] = *(LAS v4u*)(kq + j * 4096); Kr[j][1] = *(LAS v4u*)(kq + j * 4096 + 1024); }
      SB();
      {
        f32x16 s[8];
        auto mx = [&](const f32x16& t, int half) {
#pragma unroll
          for (int i = 0; i < 4; ++i) m4[i] = fmaxf(fmaxf(m4[i], t[8 * half + 2 * i]), t[8 * half + 2 * i + 1]);
        };
        sfor<0, 8>([&](auto I) {
          constexpr int n = I;
          if constexpr (n == 0) s[0] = qk1(Kr[0][0], Q0, c0); else s[n] = qk1z(Kr[n][0], Q0);
          SB();
          if constexpr (n < 4) Kr[n + 4][0] = *(LAS v4u*)(kq + (n + 4) * 4096);
          Vr[n][0] = *(LAS v4u*)(kq + n * 4096 + 2048);
          if constexpr (n >= 2) mx(s[n - 2], 1);
          SB();
          qk2(s[n], Kr[n][1], Q1);
          SB();
          if constexpr (n < 4) Kr[n + 4][1] = *(LAS v4u*)(kq + (n + 4) * 4096 + 1024);
          Vr[n][1] = *(LAS v4u*)(kq + n * 4096 + 3072);
          if constexpr (n >= 1) { tie(s[n - 1]); mx(s[n - 1], 0); }
          SB();
        });
        mx(s[6], 1);
        SB();
        asm volatile("s_nop 7" : "+v"(s[7]));
        mx(s[7], 0); mx(s[7], 1);
      }
      const float nm = -half_max(fmaxf(fmaxf(m4[0], m4[1]), fmaxf(m4[2], m4[3])));
      f32x16 cm, o;
#pragma unroll
      for (int i = 0; i < 16; ++i) { cm[i] = nm; o[i] = 0.f; }
      {
        f32x16 s[9];
        u32 pp[9][8];
        float e[16];
        asm volatile("" : "+v"(cm));
        SB(); nop2();
        s[1] = qk1(Kr[1][0], Q0, cm); qk2(s[1], Kr[1][1], Q1);
        s8 = qk_own_c(K0, K1, Q0, Q1, cm);
        SB();
        sfor<1, 9>([&](auto I) {
          constexpr int c = I, nx = (c + 1) & 7;
          tie(s[c]);
          if constexpr (c == 8) {
            tie(s8);
#pragma unroll
            for (int i = 0; i < 16; ++i) s[8][i] = asf((asu(s[8][i]) & em[i]) | (asu(s8[i]) & ~em[i]));
          }
          auto ex = [&](int q) {
#pragma unroll
            for (int i = 0; i < 4; ++i) e[4 * q + i] = __builtin_amdgcn_exp2f(s[c][4 * q + i]);
          };
          auto cv = [&](int q) { pp[c][2 * q] = pk(e[4 * q], e[4 * q + 1]); pp[c][2 * q + 1] = pk(e[4 * q + 2], e[4 * q + 3]); };
          if constexpr (c < 8) s[c + 1] = qk1(Kr[nx][0], Q0, cm);
          SB(); if constexpr (c < 7) w_issue2<4 * c - 4, 4 * c - 3, 4096u, 28>(W2_ARGS); ex(0); SB();
          if constexpr (c < 8) qk2(s[c + 1], Kr[nx][1], Q1);
          SB(); if constexpr (c < 7) w_issue2<4 * c - 3, 4 * c - 2, 4096u, 28>(W2_ARGS); ex(1); cv(0); SB();
          if constexpr (c > 1) o = mfma(Vr[c - 1][1], v4u{pp[c - 1][4], pp[c - 1][5], pp[c - 1][6], pp[c - 1][7]}, o);
          SB(); if constexpr (c < 7) w_issue2<4 * c - 2, 4 * c - 1, 4096u, 28>(W2_ARGS); ex(2); cv(1); SB();
          if constexpr (c < 8) o = mfma(Vr[c][0], v4u{pp[c][0], pp[c][1], pp[c][2], pp[c][3]}, o);
          else {
            o = mfma(Vr[0][0], v4u{pp[8][0] & pm[0], pp[8][1] & pm[1], pp[8][2] & pm[2], pp[8][3] & pm[3]}, o);
            o = mfma(V0, v4u{pp[8][0] & ~pm[0], pp[8][1] & ~pm[1], pp[8][2] & ~pm[2], pp[8][3] & ~pm[3]}, o);
          }
          SB(); if constexpr (c < 7) w_issue2<4 * c - 1, 4 * c, 4096u, 28>(W2_ARGS); ex(3); cv(2); cv(3); SB();
        });
        o = mfma(Vr[0][1], v4u{pp[8][4] & pm[4], pp[8][5] & pm[5], pp[8][6] & pm[6], pp[8][7] & pm[7]}, o);
        o = mfma(V1, v4u{pp[8][4] & ~pm[4], pp[8][5] & ~pm[5], pp[8][6] & ~pm[6], pp[8][7] & ~pm[7]}, o);
      }
      const float inv = __builtin_amdgcn_rcpf(half_add(h ? o[12] : 0.f));
      u32 oo[8];
#pragma unroll
      for (int i = 0; i < 8; ++i) oo[i] = pk(o[2 * i] * inv, o[2 * i + 1] * inv);
      if (h) { oo[6] = 0u; oo[7] = 0u; }
      const u32 ob = WS_OF + l * OF_L + (u32)(tb * 16 + head) * 2048u;
      st_sc1(v4u{oo[0], oo[1], oo[2], oo[3]}, rws, lane * 16, ob);
      st_sc1(v4u{oo[4], oo[5], oo[6], oo[7]}, rws, lane * 16 + 1024, ob);
      publish(p.ws, flag_off(l, 1, T * 16 + head), tag);
      touch_done(tc4);
      STAMP(4);
    }
    }
    v4u bw4[20][2], bw5[16][2];
#define T5_PTRS \
    lptr t0 = L + ringw + (4 * h + tq) * 128 + (((4 * wb + cpart) ^ sw) * 16) + (tp & 1) * 8; \
    lptr t1 = L + ringw + (4 * h + tq) * 128 + (((4 * (wb ^ 1) + cpart) ^ sw) * 16) + (tp & 1) * 8;
#define T4_PTRS \
    lptr n0 = L + ringw + (8 * h + tq) * 128 + (((4 * wa + cpart) ^ sw) * 16) + (tp & 1) * 8; \
    lptr n1 = L + ringw + (8 * h + tq) * 128 + (((4 * (wa ^ 1) + cpart) ^ sw) * 16) + (tp & 1) * 8;
    {
      IDS
      T5_PTRS
      w_take<0, 12, 1024>(t0, t1, bw5);
      lgk0();
      w_issue2<0, 24, 3584u, 32>(W1_ARGS);
    }
    if (p35) {
      IDS
      const Poll pq = wait_flags(p.ws, flag_off(l, 1, (st >> 1) * 16 + 4 * w + (lane & 3)), tag);
      STAMP(5);
      const u32 o0 = WS_OF + l * OF_L + (u32)((2 * st + wb) * 16 + 4 * w) * 2048u;
      const u32 o1 = WS_OF + l * OF_L + (u32)((2 * st + (wb ^ 1)) * 16 + 4 * w) * 2048u;
      v4u of[8][2];
      sfor<0, LA3>([&](auto I) { constexpr int k = I; of[k][0] = lda<k>(rws, lane * 16, o0); of[k][1] = lda<k>(rws, lane * 16, o1); });
      f32x16 acc[4];
#pragma unroll
      for (int q = 0; q < 4; ++q)
#pragma unroll
        for (int i = 0; i < 16; ++i) acc[q][i] = 0.f;
      sfor<0, 8>([&](auto I) {
        constexpr int k = I;
        vmw<(k + LA3 <= 8) ? 2 * LA3 - 2 : 14 - 2 * k>(of[k][0], of[k][1]);
        if constexpr (k == 0) poll_done(pq);
        acc[1] = mfma(wf[k][0], of[k][1], acc[1]);
        SB();
        if constexpr (k + LA3 < 8) of[k + LA3][0] = lda<k + LA3>(rws, lane * 16, o0);
        SB();
        acc[2] = mfma(wf[k][1], of[k][0], acc[2]);
        acc[3] = mfma(wf[k][1], of[k][1], acc[3]);
        SB();
        if constexpr (k + LA3 < 8) of[k + LA3][1] = lda<k + LA3>(rws, lane * 16, o1);
        SB();
        acc[0] = mfma(wf[k][0], of[k][0], acc[0]);
        if constexpr (k < 7) SB();
      });
      const f32x16 y = reduce4(L, acc, w, lane);
      float xv[16], s1 = 0.f, s2 = 0.f;
#pragma unroll
      for (int i = 0; i < 8; ++i) {
        xres[i] = pk(blo(xres[i]) + y[2 * i], bhi(xres[i]) + y[2 * i + 1]);
        xv[2 * i] = blo(xres[i]); xv[2 * i + 1] = bhi(xres[i]);
        s1 += xv[2 * i] + xv[2 * i + 1];
        s2 += xv[2 * i] * xv[2 * i] + xv[2 * i + 1] * xv[2 * i + 1];
      }
      s1 = half_add(s1); s2 = half_add(s2);
      const u32 tg = (u32)tag | 1u;
      const u32 sb = WS_STATS + l * STATS_L + (u32)((2 * st + wb) * 14) * 512u;
      if (h == 0) st_sc1(v4u{asu(s1), tg, asu(s2), tg}, rws, r * 16, sb + (u32)(2 * dt + wa) * 512u);
      STAMP(6);
      {
        T4_PTRS
        w_take<0, NB, 512>(n0, n1, bw4);
        lgk0();
        w_issue2<24, 24 + 2 * NB, 3584u, 32>(W1_ARGS);
      }
      float t1s, t2s;
      for (u32 spins = 0;;) {
        v4u gr[7];
#pragma unroll
        for (int i = 0; i < 7; ++i) gr[i] = ld_sc1(rws, (u32)(7 * h) * 512u + r * 16 + i * 512, sb);
        bool ok = true; t1s = 0.f; t2s = 0.f;
#pragma unroll
        for (int i = 0; i < 7; ++i) { ok &= (gr[i].y == tg) & (gr[i].w == tg); t1s += asf(gr[i].x); t2s += asf(gr[i].z); }
        if (__all(ok)) break;
        asm volatile("" ::: "memory");
        if (spin_out(p.ws, spins, (u32)tag)) break;
      }
      STAMP(7);
      t1s = half_add(t1s); t2s = half_add(t2s);
      const float m1 = t1s * (1.f / 448.f);
      const float var = fmaxf(t2s * (1.f / 448.f) - m1 * m1, 0.f);
      const float r1 = __builtin_amdgcn_rsqf(var + EPS);
      const float r2 = __builtin_amdgcn_rsqf(var * r1 * r1 + EPS);
      const float rr = r1 * r2, mm = m1 * rr;
      lptr tw = L + L_TR + w * 2304;
#pragma unroll
      for (int i = 0; i < 4; ++i)
        *(LAS v2u*)(tw + r * 72 + i * 16 + h * 8) = v2u{pk(xv[4 * i] * rr - mm, xv[4 * i + 1] * rr - mm), pk(xv[4 * i + 2] * rr - mm, xv[4 * i + 3] * rr - mm)};
      lgk0();
      lptr tr = tw + (8 * h + tq) * 72 + ch16 * 32 + tp * 8;
      const u32 hb = WS_HF + l * HF_L + (u32)((2 * dt + wa) * 128 + (2 * st + wb) * 2) * 1024u;
      st_sc1(trd2(tr, tr + 4 * 72), rws, lane * 16, hb);
      st_sc1(trd2(tr + 16 * 72, tr + 20 * 72), rws, lane * 16 + 1024, hb);
      publish(p.ws, flag_off(l, 2, dt * 32 + st), tag);
      STAMP(8);
    } else {
      IDS
      T4_PTRS
      drain();
      w_take<0, NB, 512>(n0, n1, bw4);
      lgk0();
      w_issue2<24, 24 + 2 * NB, 3584u, 32>(W1_ARGS);
      drain();
    }
    if (p4) {
      IDS
      T4_PTRS
      v2u bias[4];
      {
        const rsrc_t rb1 = mkrs(p.b1, F_ * 2);
        const u32 bo = (u32)(ft * 64 + wa * 32 + 4 * h) * 2u;
        sfor<0, 4>([&](auto I) { constexpr int i = I; bias[i] = ldp2<16 * i>(rb1, bo); });
      }
      w_take<NB, 20, 512>(n0, n1, bw4);
      lgk0();
      w_issue2<24 + 2 * NB, 64, 3584u, 32>(W1_ARGS);
      STAMP(9);
      const Poll pq = wait_flags(p.ws, flag_off(l, 2, dt4 * 32 + 8 * w + (lane & 7)), tag);
      asm volatile("" : "+v"(bias[0]), "+v"(bias[1]), "+v"(bias[2]), "+v"(bias[3]));
      STAMP(10);
      f32x16 acc[4];
      const u32 hb = WS_HF + l * HF_L + (u32)(32 * w) * 1024u;
      gemm_main<32, 20, 512, true, LA4>(rws, lane * 16, hb + (u32)(2 * dt4 + wb) * 131072u, hb + (u32)(2 * dt4 + (wb ^ 1)) * 131072u, ROT4, n0, n1, bw4, acc,
                                        [&](auto I) { constexpr int i = I; w_issue2<24 + i, 25 + i, 4096u, 28>(W2_ARGS); });
      poll_done(pq);
      STAMP(11);
      const f32x16 y = reduce4(L, acc, w, lane);
      u32 o[8];
#pragma unroll
      for (int i = 0; i < 8; ++i) {
        const u32 bb = (i & 1) ? bias[i >> 1].y : bias[i >> 1].x;
        float gv[2];
#pragma unroll
        for (int e = 0; e < 2; ++e) {
          const float u = y[2 * i + e] + (e ? bhi(bb) : blo(bb));
          const float z = u * (-2.302208198f - 0.1029432397f * u * u);
          gv[e] = u * __builtin_amdgcn_rcpf(1.f + __builtin_amdgcn_exp2f(z));
        }
        o[i] = pk(gv[0], gv[1]);
      }
      const u32 ub = WS_UF + l * UF_L + (u32)((2 * dt4 + wb) * 112 + 4 * ft + 2 * wa) * 1024u;
      st_sc1(v4u{o[0], o[1], o[2], o[3]}, rws, lane * 16, ub);
      st_sc1(v4u{o[4], o[5], o[6], o[7]}, rws, lane * 16 + 1024, ub);
      publish(p.ws, flag_off(l, 3, dt4 * 32 + ft), tag);
      STAMP(12);
    } else {
      IDS
      {
        const rsrc_t rwf = mkrs(p.w2, F_ * S_ * 2);
        const int nb = (g & 1) ? 4 : 11, ib = bi - ((g & 1) ? 28 : 21);
        for (int row = ib * 4 + w; row < F_; row += nb * 4 * 8) {
          sfor<0, 8>([&](auto I) { constexpr int i = I; dma16<0, i * 1024>(rwf, lane * 16 + pj * 1024, (u32)(row + i * nb * 4) * 4096u, lds0 + ringw); });
          asm volatile("s_waitcnt vmcnt(16)" ::: "memory");
        }
        drain();
      }
      w_issue2<24, 24 + 2 * LA4, 4096u, 28>(W2_ARGS);
      drain();
    }
    if (p35) {
      IDS
      T5_PTRS
      u32 bias;
      asm volatile("s_nop 4\n\tbuffer_load_ushort %0, %1, %2, 0 offen" : "=v"(bias) : "v"((u32)((2 * st + wb) * 32 + r) * 2u), "s"(mkrs(p.b2, S_ * 2)));
      w_issue2<24 + 2 * LA4, 48, 4096u, 28>(W2_ARGS);
      w_take<12, 16, 1024>(t0, t1, bw5);
      lgk0();
      w_issue2<48, 56, 4096u, 28>(W2_ARGS);
      STAMP(13);
      const Poll pq = wait_flags(p.ws, flag_off(l, 3, dt * 32 + 7 * w + (lane < 7 ? lane : 0)), tag);
      asm volatile("" : "+v"(bias));
      STAMP(14);
      const u32 wis = l == 0 ? WS_WI + (u32)head * 86016u + (u32)w * 21504u : 0x40000000u;
      v4u wi[21];
      f32x16 acc[4];
      const u32 ub = WS_UF + l * UF_L + (u32)(28 * w) * 1024u;
      gemm_main<28, 16, 1024, false, LA5>(rws, lane * 16, ub + (u32)(2 * dt + wa) * 114688u, ub + (u32)(2 * dt + (wa ^ 1)) * 114688u, ROT5, t0, t1, bw5, acc,
                                          [&](auto I) { constexpr int i = I; wi[i] = lda<i>(rws, lane * 16, wis); });
      poll_done(pq);
      STAMP(15);
      const f32x16 y = reduce4(L, acc, w, lane);
      const float bv = blo(bias);
#pragma unroll
      for (int i = 0; i < 8; ++i) xres[i] = pk(blo(xres[i]) + (y[2 * i] + bv), bhi(xres[i]) + (y[2 * i + 1] + bv));
      v4u xo[2];
#pragma unroll
      for (int s = 0; s < 2; ++s) {
        auto a = __builtin_amdgcn_permlane32_swap(xres[4 * s], xres[4 * s + 2], false, false);
        auto c = __builtin_amdgcn_permlane32_swap(xres[4 * s + 1], xres[4 * s + 3], false, false);
        xo[s] = v4u{a[0], c[0], a[1], c[1]};
      }
      if (l == 0) {
        const u32 xb = WS_XF + (u32)((2 * st + wb) * 28 + 4 * dt) * 1024u + wa * 512;
        st_sc1(xo[0], rws, h * 1024 + r * 16, xb);
        st_sc1(xo[1], rws, h * 1024 + r * 16 + 2048, xb);
        float q0 = 0.f, q1 = 0.f;
#pragma unroll
        for (int i = 0; i < 8; i += 2) { q0 = __builtin_amdgcn_fdot2_f32_bf16(asbf2(xres[i]), asbf2(xres[i]), q0, false); q1 = __builtin_amdgcn_fdot2_f32_bf16(asbf2(xres[i + 1]), asbf2(xres[i + 1]), q1, false); }
        const float q = half_add(q0 + q1);
        if (h == 0) __builtin_amdgcn_raw_buffer_store_b32(asu(q), rws, r * 4, WS_SQ + (u32)((2 * st + wb) * 14 + 2 * dt + wa) * 128u, 16);
        publish(p.ws, flag_off(0, 4, st * 8 + dt), tag);
        sfor<2 * LA5, 21>([&](auto I) { constexpr int i = I; wi[i] = lda<i>(rws, lane * 16, wis); });
        sfor<0, 21>([&](auto I) { constexpr int i = I; vmw<(i < 2 * LA5) ? 21 - 2 * LA5 : 20 - i>(wi[i]); *(LAS v4u*)(L + w * 21504 + i * 1024 + lane * 16) = wi[i]; });
        bar();
      } else {
        sfor<0, 2 * LA5>([&](auto I) { constexpr int i = I; vmw<0>(wi[i]); });
        const rsrc_t rout = mkrs(p.out, S_ * D_ * 2);
        const u32 oo = (u32)((2 * st + wb) * 32 + r) * 896u + (u32)(dt * 64 + wa * 32 + 8 * h) * 2u;
        __builtin_amdgcn_raw_buffer_store_b128(xo[0], rout, oo, 0, 0);
        __builtin_amdgcn_raw_buffer_store_b128(xo[1], rout, oo + 32, 0, 0);
        if (b == 0 && tid == 0) __hip_atomic_store((GAS u64*)(p.ws + WS_EPOCH), tag, RLX_AGENT);
      }
      STAMP(16);
    } else if (l == 0) {
      IDS
      const Poll pq = wait_flags(p.ws, flag_off(0, 1, (lane & 15) * 16 + head), tag);
      const u32 wis = WS_WI + (u32)head * 86016u + (u32)w * 21504u;
      v4u wi[21];
      bar();
      sfor<0, 21>([&](auto I) { constexpr int i = I; wi[i] = lda<i>(rws, lane * 16, wis); });
      sfor<0, 21>([&](auto I) { constexpr int i = I; vmw<20 - i>(wi[i]); *(LAS v4u*)(L + w * 21504 + i * 1024 + lane * 16) = wi[i]; });
      poll_done(pq);
      bar();
    }
  }
  asm volatile(".Lfwd_last:");
}

extern "C" void kernel_launch(void* const* d_in, const int* in_sizes, int n_in, void* d_out, int out_size, void* d_ws, size_t ws_size,
                              hipStream_t stream) {
  Params p;
  p.x = d_in[0]; p.wqkv = d_in[1]; p.wo = d_in[2]; p.w1 = d_in[4]; p.b1 = d_in[5]; p.w2 = d_in[6]; p.b2 = d_in[7];
  p.out = d_out; p.ws = (unsigned char*)d_ws;
  (void)hipFuncSetAttribute((const void*)fused_fwd, hipFuncAttributeMaxDynamicSharedMemorySize, L_END);
  hipLaunchKernelGGL(fused_fwd, dim3(256), dim3(256), L_END, stream, p);
}
```

```cpp
#define DBG_STAMP 1
#define DIAG_A 0
#define DIAG_B 0
#define DIAG_SRC dbgR
#define DIAG_MUL 10
#define DIAG_DIV 1
#define DIAG_BLK 16
#include <hip/hip_runtime.h>
#include <hip/hip_bf16.h>
#include <type_traits>

typedef unsigned u32;
typedef unsigned long long u64;
typedef u32 v4u __attribute__((ext_vector_type(4)));
typedef u32 v2u __attribute__((ext_vector_type(2)));
typedef float f32x16 __attribute__((ext_vector_type(16)));
typedef float f32x4 __attribute__((ext_vector_type(4)));
typedef float f32x2 __attribute__((ext_vector_type(2)));
typedef __bf16 bf16x8 __attribute__((ext_vector_type(8)));
typedef __bf16 bf16x2 __attribute__((ext_vector_type(2)));
typedef short s16x4 __attribute__((ext_vector_type(4)));
#define LAS __attribute__((address_space(3)))
#define GAS __attribute__((address_space(1)))
#define DI __device__ __forceinline__
#define RLX_AGENT __ATOMIC_RELAXED, __HIP_MEMORY_SCOPE_AGENT
#define SB() __builtin_amdgcn_sched_barrier(0)
typedef __amdgpu_buffer_rsrc_t rsrc_t;
typedef LAS unsigned char* lptr;

#define DBG_WAVE 0
#define POLL_GAP "3"
#define TOUCH 1
#define QP0 12
#define NB 10
#define CODE_TOUCH 1
#define TLBT 0
#define LA4 6
#define LA5 6
#define LA1 12
#define LA3 6
#define ROT4 0
#define ROT5 0
#define ROT1 (head & 1)
#define WARM2 1

constexpr int S_ = 2048, D_ = 448, F_ = 1792;
constexpr u32 WS_EPOCH = 0, WS_TMO = 128;
constexpr u32 WS_FLAGS = 4096;
constexpr u32 WS_STATS = WS_FLAGS + 2 * 5 * 256 * 128;
constexpr u32 STATS_L = 64 * 14 * 512;
constexpr u32 WS_KV = 2u << 20, KV_L = 4u << 20;
constexpr u32 WS_OF = WS_KV + 2 * KV_L, OF_L = 2u << 20;
constexpr u32 WS_HF = WS_OF + 2 * OF_L, HF_L = 2u << 20;
constexpr u32 WS_UF = WS_HF + 2 * HF_L, UF_L = 2u << 20;
constexpr u32 WS_XF = WS_UF + 2 * UF_L;
constexpr u32 WS_SQ = WS_XF + 64 * 28 * 1024;
constexpr u32 WS_WI = WS_XF + (2u << 20);
constexpr u32 WS_END = WS_WI + (2u << 20);
constexpr u32 WS_DBG = WS_END;
static_assert(WS_STATS + 2 * STATS_L <= WS_KV, "ws map");
constexpr u32 L_RING = 0, RING_W = 24576;
constexpr u32 L_INBOX = 98304, INBOX_W = 12288;
constexpr u32 L_WQ = 57344;
constexpr u32 L_WO = 86016;
constexpr u32 L_CSP = 147456;
constexpr u32 L_CSF = L_CSP + 1536;
constexpr u32 L_MR = L_CSF + 2048;
constexpr u32 L_TR = L_MR + 1024;
constexpr u32 L_END = L_TR + 4 * 2304;
constexpr u32 kSpinCap = 1u << 16;
constexpr float EPS = 1e-5f;

struct Params {
  const void* x; const void* wqkv; const void* wo; const void* w1; const void* b1; const void* w2; const void* b2;
  void* out; unsigned char* ws;
};

DI f32x16 mfma(v4u a, v4u b, f32x16 c) {
  return __builtin_amdgcn_mfma_f32_32x32x16_bf16(__builtin_bit_cast(bf16x8, a), __builtin_bit_cast(bf16x8, b), c, 0, 0, 0);
}
DI f32x16 qk1(v4u k0, v4u q0, f32x16 ci) {
  f32x16 c;
  asm volatile("v_mfma_f32_32x32x16_bf16 %0, %1, %2, %3" : "=&v"(c) : "a"(k0), "v"(q0), "v"(ci));
  return c;
}
DI f32x16 qk1z(v4u k0, v4u q0) {
  f32x16 c;
  asm volatile("v_mfma_f32_32x32x16_bf16 %0, %1, %2, 0" : "=&v"(c) : "a"(k0), "v"(q0));
  return c;
}
DI void qk2(f32x16& c, v4u k1, v4u q1) { asm volatile("v_mfma_f32_32x32x16_bf16 %0, %1, %2, %0" : "+v"(c) : "a"(k1), "v"(q1)); }
DI f32x16 qk_own(v4u k0, v4u k1, v4u q0, v4u q1) {
  f32x16 c;
  asm volatile("s_nop 1\n\tv_mfma_f32_32x32x16_bf16 %0, %1, %3, 0\n\tv_mfma_f32_32x32x16_bf16 %0, %2, %4, %0" : "=&v"(c) : "v"(k0), "v"(k1), "v"(q0), "v"(q1));
  return c;
}
DI f32x16 qk_own_c(v4u k0, v4u k1, v4u q0, v4u q1, f32x16 ci) {
  f32x16 c;
  asm volatile("s_nop 1\n\tv_mfma_f32_32x32x16_bf16 %0, %1, %3, %5\n\tv_mfma_f32_32x32x16_bf16 %0, %2, %4, %0" : "=&v"(c) : "v"(k0), "v"(k1), "v"(q0), "v"(q1), "v"(ci));
  return c;
}
DI void tie(f32x16& c) { asm volatile("" : "+v"(c)); }
DI void tie_far(f32x16& c) { asm volatile("s_nop 7\n\ts_nop 4" : "+v"(c)); }
DI void nop2() { asm volatile("s_nop 1"); }
DI u32 pk(float lo, float hi) { f32x2 v = {lo, hi}; return __builtin_bit_cast(u32, __builtin_convertvector(v, bf16x2)); }
DI float asf(u32 v) { return __builtin_bit_cast(float, v); }
DI u32 asu(float v) { return __builtin_bit_cast(u32, v); }
DI bf16x2 asbf2(u32 v) { return __builtin_bit_cast(bf16x2, v); }
DI float blo(u32 v) { return __builtin_bit_cast(float, v << 16); }
DI float bhi(u32 v) { return __builtin_bit_cast(float, v & 0xffff0000u); }
DI void bar() { asm volatile("s_waitcnt lgkmcnt(0)\n\ts_barrier" ::: "memory"); }
DI void drain() { asm volatile("s_waitcnt vmcnt(0)" ::: "memory"); }
DI void lgk0() { asm volatile("s_waitcnt lgkmcnt(0)" ::: "memory"); }
template <u32 SI, u32 LI> DI void dma16(rsrc_t rs, u32 voff, u32 soff, u32 lds) {
  u32 t;
  asm volatile("s_add_u32 m0, %1, %5\n\ts_add_u32 %0, %4, %6\n\tbuffer_load_dwordx4 %2, %3, %0 offen lds"
               : "=&s"(t) : "s"(lds), "v"(voff), "s"(rs), "s"(soff), "i"(LI), "i"(SI) : "memory", "scc");
}
template <int K> DI v4u lda(rsrc_t rs, u32 voff, u32 soff) {
  v4u d; u32 t;
  asm volatile("s_add_u32 %1, %4, %5\n\tbuffer_load_dwordx4 %0, %2, %3, %1 offen offset:%6 sc1"
               : "=v"(d), "=&s"(t) : "v"(voff), "s"(rs), "s"(soff), "i"((K >> 2) * 4096), "i"((K & 3) * 1024) : "scc");
  return d;
}
template <int IMM> DI v4u ldp(rsrc_t rs, u32 voff, u32 soff) {
  v4u d;
  asm volatile("buffer_load_dwordx4 %0, %1, %2, %3 offen offset:%4" : "=v"(d) : "v"(voff), "s"(rs), "s"(soff), "i"(IMM));
  return d;
}
template <int IMM> DI v2u ldp2(rsrc_t rs, u32 voff) {
  v2u d;
  asm volatile("buffer_load_dwordx2 %0, %1, %2, 0 offen offset:%3" : "=v"(d) : "v"(voff), "s"(rs), "i"(IMM));
  return d;
}
DI void pin4(float (&v)[4]) { asm volatile("" : "+v"(v[0]), "+v"(v[1]), "+v"(v[2]), "+v"(v[3])); }
template <int IMM> DI u32 lda1(rsrc_t rs, u32 voff, u32 soff) {
  u32 d;
  asm volatile("buffer_load_dword %0, %1, %2, %3 offen offset:%4 sc1" : "=v"(d) : "v"(voff), "s"(rs), "s"(soff), "i"(IMM));
  return d;
}
template <int N> DI void vmw0() { asm volatile("s_waitcnt vmcnt(%0)" ::"i"(N) : "memory"); }
template <int N> DI void vmw(v4u& a) { asm volatile("s_waitcnt vmcnt(%1)" : "+v"(a) : "i"(N)); }
template <int N> DI void vmw(v4u& a, v4u& b, v4u& c, v4u& d) { asm volatile("s_waitcnt vmcnt(%4)" : "+v"(a), "+v"(b), "+v"(c), "+v"(d) : "i"(N)); }
template <int N> DI void vmw(v4u& a, v4u& b) { asm volatile("s_waitcnt vmcnt(%2)" : "+v"(a), "+v"(b) : "i"(N)); }
template <int I, int N, class Fn> DI void sfor(Fn&& f) { if constexpr (I < N) { f(std::integral_constant<int, I>{}); sfor<I + 1, N>(f); } }
DI u32 opq(u32 v) { asm volatile("" : "+s"(v)); return v; }
DI u32 opqv(u32 v) { asm volatile("" : "+v"(v)); return v; }
DI v2u trd(lptr p) { return __builtin_bit_cast(v2u, __builtin_amdgcn_ds_read_tr16_b64_v4i16((LAS s16x4*)p)); }
DI v4u trd2(lptr p0, lptr p1) { v2u a = trd(p0), b = trd(p1); return v4u{a.x, a.y, b.x, b.y}; }
DI v4u ld_sc1(rsrc_t rs, u32 voff, u32 soff) { return __builtin_amdgcn_raw_buffer_load_b128(rs, voff, soff, 16); }
DI void st_sc1(v4u v, rsrc_t rs, u32 voff, u32 soff) { __builtin_amdgcn_raw_buffer_store_b128(v, rs, voff, soff, 16); }
DI rsrc_t mkrs(const void* p, u32 bytes) {
  u32 fl = 0x00020000;
  asm volatile("" : "+s"(p), "+s"(bytes), "+s"(fl));
  return __builtin_amdgcn_make_buffer_rsrc((void*)p, 0, bytes, fl);
}
DI float half_add(float v) {
  auto r = __builtin_amdgcn_permlane32_swap(asu(v), asu(v), false, false);
  const u32 a = r[0], b = r[1];
  return asf(a) + asf(b);
}
DI float half_max(float v) {
  auto r = __builtin_amdgcn_permlane32_swap(asu(v), asu(v), false, false);
  const u32 a = r[0], b = r[1];
  return fmaxf(asf(a), asf(b));
}
DI bool spin_out(unsigned char* ws, u32& spins, u32 tl) {
  ++spins;
  if ((spins & 63u) == 0u) {
    GAS u32* tmo = (GAS u32*)(ws + WS_TMO);
    if (__hip_atomic_load(tmo, RLX_AGENT) == tl) return true;
    if (spins > kSpinCap) { __hip_atomic_store(tmo, tl, RLX_AGENT); return true; }
  }
  return false;
}
struct Poll { v2u a, b, c; };
#define PL(R, O, B) "global_load_dwordx2 " R ", " O ", " B " sc1\n\t"
DI v2u poll_one(unsigned char* ws, u32 off) {
  v2u r;
  asm volatile(PL("%0", "%1", "%2") : "=v"(r) : "v"(off), "s"(ws) : "memory");
  return r;
}
DI void poll_finish(Poll& q, unsigned char* ws, u32 off, u64 tag) {
  u32 left;
#define PC(R) "s_waitcnt vmcnt(2)\n\tv_cmp_ne_u64 vcc, %6, " R "\n\ts_cbranch_vccz 2f\n\t" PL(R, "%4", "%5")
  asm volatile("s_mov_b32 %3, 0x8000\n1:\n\t" PC("%0") PC("%1") PC("%2") "s_sub_u32 %3, %3, 1\n\ts_cbranch_scc0 1b\n2:"
               : "+v"(q.a), "+v"(q.b), "+v"(q.c), "=&s"(left) : "v"(off), "s"(ws), "s"(tag) : "vcc", "scc", "memory");
#undef PC
  if (left == 0xffffffffu) { drain(); __hip_atomic_store((GAS u32*)(ws + WS_TMO), (u32)tag, RLX_AGENT); }
}
DI Poll wait_flags(unsigned char* ws, u32 off, u64 tag) {
  Poll q;
  q.a = poll_one(ws, off); asm volatile("s_sleep " POLL_GAP);
  q.b = poll_one(ws, off); asm volatile("s_sleep " POLL_GAP);
  q.c = poll_one(ws, off);
  poll_finish(q, ws, off, tag);
  return q;
}
DI void poll_done(const Poll& q) { asm volatile("" ::"v"(q.a), "v"(q.b), "v"(q.c)); }
DI u32 touch(rsrc_t rs, u32 voff, u32 soff) {
  u32 d;
  asm volatile("s_nop 4\n\tbuffer_load_dword %0, %1, %2, %3 offen" : "=v"(d) : "v"(voff), "s"(rs), "s"(soff));
  return d;
}
DI void touch_done(u32 d) { asm volatile("" ::"v"(d)); }
DI void publish(unsigned char* ws, u32 off, u64 tag) {
  asm volatile("s_waitcnt vmcnt(0) lgkmcnt(0)\n\ts_barrier" ::: "memory");
  if (threadIdx.x == 0) __hip_atomic_store((GAS u64*)(ws + off), tag, RLX_AGENT);
  asm volatile("" ::: "memory");
}
DI u32 flag_off(int l, int type, int idx) { return WS_FLAGS + (u32)(((l * 5 + type) << 8) + idx) * 128u; }

#define STAMP(i) do { u64 t0_, t1_; asm volatile("s_memrealtime %0\n\ts_memtime %1\n\ts_waitcnt lgkmcnt(0)" : "=s"(t0_), "=s"(t1_)); \
  const u32 sl_ = l * 32 + (i); asm volatile("s_mov_b32 m0, %4\n\tv_writelane_b32 %0, %2, m0\n\tv_writelane_b32 %1, %3, m0" : "+v"(dbgR), "+v"(dbgC) : "s"((u32)t0_), "s"((u32)t1_), "s"(sl_)); } while (0)

DI f32x16 reduce4(lptr L, const f32x16 (&acc)[4], int w, int lane) {
#pragma unroll
  for (int q = 1; q < 4; ++q) {
    lptr dst = L + L_INBOX + (u32)(w ^ q) * INBOX_W + (q - 1) * 4096 + lane * 16;
#pragma unroll
    for (int i = 0; i < 4; ++i)
      *(LAS f32x4*)(dst + i * 1024) = f32x4{acc[q][4 * i], acc[q][4 * i + 1], acc[q][4 * i + 2], acc[q][4 * i + 3]};
  }
  bar();
  f32x16 r = acc[0];
  lptr src = L + L_INBOX + (u32)w * INBOX_W + lane * 16;
#pragma unroll
  for (int q = 0; q < 3; ++q)
#pragma unroll
    for (int i = 0; i < 4; ++i) {
      const f32x4 v = *(LAS f32x4*)(src + q * 4096 + i * 1024);
      r[4 * i] += v[0]; r[4 * i + 1] += v[1]; r[4 * i + 2] += v[2]; r[4 * i + 3] += v[3];
    }
  return r;
}

template <int P0, int P1, u32 RS> DI void w_issue(rsrc_t rs, u32 voff, u32 soff, u32 ring) {
  sfor<P0, P1>([&](auto I) { constexpr int i = I; dma16<i * 8 * RS, (i % 24) * 1024>(rs, voff, soff, ring); });
}
template <int P0, int P1, u32 RS, int HP> DI void w_issue2(rsrc_t rs, u32 voff, u32 s0, u32 s1, u32 ring) {
  sfor<P0, P1>([&](auto I) { constexpr int i = I; dma16<(i % HP) * 8 * RS, (i % 24) * 1024>(rs, voff, i < HP ? s0 : s1, ring); });
}
template <int K0, int K1, int TSTEP, int NR> DI void w_take(lptr t0, lptr t1, v4u (&bw)[NR][2]) {
#pragma unroll
  for (int kk = K0; kk < K1; ++kk) {
    const int sl = (kk % 12) * 2048;
    bw[kk][0] = trd2(t0 + sl, t0 + sl + TSTEP);
    bw[kk][1] = trd2(t1 + sl, t1 + sl + TSTEP);
  }
}
template <int NR> DI void w_undef(v4u (&bw)[NR][2], int k0, int k1) {
#pragma unroll
  for (int kk = 0; kk < NR; ++kk) if (kk >= k0 && kk < k1) { asm volatile("" : "=v"(bw[kk][0])); asm volatile("" : "=v"(bw[kk][1])); }
}

template <int NK, int NR, int TSTEP, bool WA, int LA, class Tail>
DI void gemm_main(rsrc_t ws, u32 voff, u32 a0, u32 a1, u32 rot, lptr t0, lptr t1, const v4u (&bw)[NR][2], f32x16 (&acc)[4], Tail&& tail) {
  static_assert(NK - NR == 12 && NK - LA >= 13, "ring is 12 slots and must be consumed before the tail");
  v4u a[NK][2];
  constexpr int HK = NK / 2;
  const u32 b0[2] = {a0 + rot * (HK * 1024), a0 + (rot ^ 1u) * (HK * 1024)}, b1[2] = {a1 + rot * (HK * 1024), a1 + (rot ^ 1u) * (HK * 1024)};
  sfor<0, LA>([&](auto I) { constexpr int n = I, k = (n + NR) % NK; a[n][0] = lda<k % HK>(ws, voff, b0[k / HK]); a[n][1] = lda<k % HK>(ws, voff, b1[k / HK]); });
  v4u wl[12][2];
  wl[0][0] = trd2(t0 + (NR % 12) * 2048, t0 + (NR % 12) * 2048 + TSTEP); wl[0][1] = trd2(t1 + (NR % 12) * 2048, t1 + (NR % 12) * 2048 + TSTEP);
#pragma unroll
  for (int q = 0; q < 4; ++q)
#pragma unroll
    for (int i = 0; i < 16; ++i) acc[q][i] = 0.f;
  wl[1][0] = trd2(t0 + ((NR + 1) % 12) * 2048, t0 + ((NR + 1) % 12) * 2048 + TSTEP); wl[1][1] = trd2(t1 + ((NR + 1) % 12) * 2048, t1 + ((NR + 1) % 12) * 2048 + TSTEP);
  SB();
  sfor<0, NK>([&](auto I) {
    constexpr int n = I, kn = (n + LA + NR) % NK, sl = ((NR + n + 2) % 12) * 2048;
    vmw<2 * LA - 2>(a[n][0], a[n][1]);
    v4u w0, w1;
    if constexpr (n < 12) { w0 = wl[n][0]; w1 = wl[n][1]; } else { w0 = bw[n - 12][0]; w1 = bw[n - 12][1]; }
    acc[1] = WA ? mfma(w0, a[n][1], acc[1]) : mfma(a[n][0], w1, acc[1]);
    SB();
    if constexpr (n + LA < NK) a[n + LA][0] = lda<kn % HK>(ws, voff, b0[kn / HK]); else tail(std::integral_constant<int, 2 * (n + LA - NK)>{});
    SB();
    acc[2] = WA ? mfma(w1, a[n][0], acc[2]) : mfma(a[n][1], w0, acc[2]);
    SB();
    if constexpr (n + 2 < 12) wl[n + 2][0] = trd2(t0 + sl, t0 + sl + TSTEP);
    SB();
    acc[3] = WA ? mfma(w1, a[n][1], acc[3]) : mfma(a[n][1], w1, acc[3]);
    SB();
    if constexpr (n + LA < NK) a[n + LA][1] = lda<kn % HK>(ws, voff, b1[kn / HK]); else tail(std::integral_constant<int, 2 * (n + LA - NK) + 1>{});
    SB();
    acc[0] = WA ? mfma(w0, a[n][0], acc[0]) : mfma(a[n][0], w0, acc[0]);
    SB();
    if constexpr (n + 2 < 12) wl[n + 2][1] = trd2(t1 + sl, t1 + sl + TSTEP);
    SB();
  });
}

__global__ __launch_bounds__(256, 1) void fused_fwd(Params p) {
  extern __shared__ __attribute__((aligned(16))) unsigned char smem[];
  lptr L = (lptr)smem;
  const u32 lds0 = (u32)(size_t)L;
#define IDS \
  const int tid = (int)opqv(threadIdx.x), lane = tid & 63, r = lane & 31, h = lane >> 5; \
  const int w = (int)opq(w_), b = (int)opq(blockIdx.x), g = b & 7, bi = b >> 3, pj = g >> 1; \
  const int T = 2 * g + (bi >> 4), head = bi & 15; \
  const int d3 = (bi * 43) >> 7, m3 = bi - 3 * d3; \
  const int st = 8 * pj + ((g & 1) ? d3 : (bi >> 2)), dt = (g & 1) ? 4 + m3 : (bi & 3); \
  const int ft = 7 * pj + ((g & 1) ? (bi >> 2) : d3), dt4 = (g & 1) ? (bi & 3) : 4 + m3; \
  const int wa = w >> 1, wb = w & 1; \
  const int ch16 = (lane >> 4) & 1, tq = (lane & 15) >> 2, tp = lane & 3; \
  const u32 ringw = L_RING + (u32)w * RING_W; \
  const u32 dmarow = lane >> 3, dmach = ((lane & 7) ^ (((lane >> 4) & 1) << 2)) * 16; \
  const u32 sw = (u32)((tq >> 1) & 1) << 2, cpart = 2 * ch16 + (tp >> 1); \
  (void)r; (void)h; (void)T; (void)head; (void)st; (void)dt; (void)ft; (void)dt4; (void)wa; (void)wb; (void)ch16; (void)tq; (void)tp; (void)ringw; (void)dmarow; (void)dmach; (void)sw; (void)cpart;
  const u32 w_ = __builtin_amdgcn_readfirstlane(threadIdx.x >> 6);
  const bool p35 = (blockIdx.x & 1) == 0 || (blockIdx.x >> 3) < 24;
  const bool p4 = (blockIdx.x >> 3) < ((blockIdx.x & 1) ? 28u : 21u);

  const rsrc_t rws = mkrs(p.ws, WS_END);
  u64 tag = 0;
  u32 dbgR = 0, dbgC = 0;
  u32 xres[8];
  v4u wf[8][2];
#pragma unroll
  for (int i = 0; i < 8; ++i) { xres[i] = 0; asm volatile("" : "=a"(wf[i][0])); asm volatile("" : "=a"(wf[i][1])); }

#pragma unroll 1
  for (int l = 0; l < 2; ++l) {
    STAMP(0);
    const rsrc_t rw1 = mkrs(p.w1, p4 ? S_ * F_ * 2 : 0);
    const rsrc_t rw2 = mkrs(p.w2, p35 ? F_ * S_ * 2 : 0);
#define W2_ARGS rw2, dmarow * 4096u + dmach + (u32)st * 128u, (u32)(448 * w + 224 * ROT5) * 4096u, (u32)(448 * w + 224 * (ROT5 ^ 1)) * 4096u, lds0 + ringw
#define W1_ARGS rw1, dmarow * 3584u + dmach + (u32)ft * 128u, (u32)(512 * w + 256 * ROT4) * 3584u, (u32)(512 * w + 256 * (ROT4 ^ 1)) * 3584u, lds0 + ringw
    {
    IDS
    const int tb = 4 * T + w;
    const u32 shift = (head & 1) * 8;
    const u32 half = (((tq >> 1) & 1) ^ 1) * 64, col = shift + ch16 * 32 + tp * 8;
    lptr wtk = L + (32 * h + tq) * 128 + half + col, wtv = L + (32 * h + tq) * 128 + (half ^ 64) + col, wtq = L + L_WQ + (32 * h + tq) * 64 + col;
    v4u X[28];
    f32x16 kt, vv;
#pragma unroll
    for (int i = 0; i < 16; ++i) { kt[i] = 0.f; vv[i] = 0.f; }
    float ssq;
    u32 tc2 = 0, tc3 = 0, tc4 = 0, tc5 = 0, tc6 = 0;
    v4u wk[28], wv[28], wq[28];
    constexpr int PD = 3;
#define WROW(k) (((k) >> 2) * 64 + ((k) & 3) * 8)
#define WFK(k) trd2(wtk + WROW(k) * 128, wtk + WROW(k) * 128 + 512)
#define WFV(k) trd2(wtv + WROW(k) * 128, wtv + WROW(k) * 128 + 512)
#define WFQ(k) trd2(wtq + WROW(k) * 64, wtq + WROW(k) * 64 + 256)
    auto kv_head = [&]() {
#pragma unroll
      for (int k = 0; k < PD; ++k) { wk[k] = WFK(k); wv[k] = WFV(k); }
      SB();
    };
    auto kv_step = [&](auto I, auto&& mem) {
      constexpr int k = I;
      kt = mfma(wk[k], X[k], kt);
      SB();
      if constexpr (k + PD < 28) wk[k + PD] = WFK(k + PD);
      mem();
      SB();
      vv = mfma(X[k], wv[k], vv);
      SB();
      if constexpr (k + PD < 28) wv[k + PD] = WFV(k + PD);
      SB();
    };
    const v4u ones = {0x3F803F80u, 0x3F803F80u, 0x3F803F80u, 0x3F803F80u};
    if (l == 0) {
      v2u e0;
      asm volatile("global_load_dwordx2 %0, %1, %2 sc1" : "=v"(e0) : "v"(WS_EPOCH), "s"(p.ws));
      u32 tcc;
      {
        u64 pc; u32 back, len;
        asm volatile("s_getpc_b64 %0\n.Lfwd_here:\n\ts_mov_b32 %1, .Lfwd_here-_Z9fused_fwd6Params\n\ts_mov_b32 %2, .Lfwd_last-_Z9fused_fwd6Params" : "=s"(pc), "=s"(back), "=s"(len));
        tcc = touch(mkrs((const void*)(pc - back), CODE_TOUCH ? len : 0), lane * 128, (u32)(bi * 4 + w) * 8192u);
      }
      const rsrc_t rx = mkrs(p.x, S_ * D_ * 2), rwq = mkrs(p.wqkv, D_ * 3 * D_ * 2);
      const u32 hoff = (u32)(head * 56) - shift;
      const u32 xv = dmarow * 896u + (lane & 7) * 16u, xs = (u32)(tb * 32) * 896u;
      lptr xb = L + L_WO + w * 15360;
      lptr xw = xb + dmarow * 128, xr = xb + r * 128;
      const u32 xsw = (r >> 1) & 7, xc0 = ((lane & 7) ^ (lane >> 4)) * 16;
      const u32 kvv = dmarow * 2688u + 896u + ((((lane >> 2) ^ (lane >> 4) ^ 1) & 1) * 896u) + (lane & 3) * 16u + hoff;
      const u32 kvs = (u32)(112 * w) * 2688u, kvl0 = lds0 + (u32)(14 * w) * 1024u;
      const rsrc_t rwo = mkrs(p.wo, D_ * D_ * 2);
      const u32 wov = dmarow * 896u + dmach + (u32)dt * 128u, wos = (u32)(112 * w) * 896u, wol = lds0 + L_WO + (u32)w * 15360u;
      const u32 qv = (u32)(lane >> 2) * 2688u + (lane & 3) * 16u + hoff, ql = lds0 + L_WQ + (u32)(7 * w) * 1024u;
      sfor<0, 28>([&](auto I) { constexpr int k = I; X[k] = ldp<(k >> 2) * 128>(rx, xv, xs + (k & 3) * 7168u); });
      sfor<0, 7>([&](auto I) { constexpr int i = I; dma16<i * 8 * 2688, i * 1024>(rwq, kvv, kvs, kvl0); });
      STAMP(24);
      float sq4[4] = {0.f, 0.f, 0.f, 0.f};
      sfor<0, 7>([&](auto I) {
        constexpr int G = I;
        vmw<24 - 4 * G + 7 + 2 * G>(X[4 * G], X[4 * G + 1], X[4 * G + 2], X[4 * G + 3]);
#pragma unroll
        for (int i = 0; i < 4; ++i) *(LAS v4u*)(xw + i * 1024 + (xc0 ^ ((i & 1) * 64))) = X[4 * G + i];
#pragma unroll
        for (int c = 0; c < 4; ++c) X[4 * G + c] = *(LAS v4u*)(xr + (((4 * h + c) ^ xsw) * 16));
        if constexpr (2 * G < 7) dma16<(7 + 2 * G) * 8 * 2688, (7 + 2 * G) * 1024>(rwq, kvv, kvs, kvl0); else dma16<(2 * G - 7) * 16 * 2688, (2 * G - 7) * 1024>(rwq, qv, kvs, ql);
        if constexpr (2 * G + 1 < 7) dma16<(8 + 2 * G) * 8 * 2688, (8 + 2 * G) * 1024>(rwq, kvv, kvs, kvl0); else dma16<(2 * G - 6) * 16 * 2688, (2 * G - 6) * 1024>(rwq, qv, kvs, ql);
#pragma unroll
        for (int i = 0; i < 16; ++i) { const bf16x2 xx = asbf2(X[4 * G + (i >> 2)][i & 3]); sq4[i & 3] = __builtin_amdgcn_fdot2_f32_bf16(xx, xx, sq4[i & 3], false); }
        pin4(sq4);
        SB();
      });
      ssq = (sq4[0] + sq4[1]) + (sq4[2] + sq4[3]);
      STAMP(25);
      f32x16 ck, cv;
#pragma unroll
      for (int i = 0; i < 16; ++i) { ck[i] = 0.f; cv[i] = 0.f; }
      lptr ck0 = L + (112 * w + 8 * h + tq) * 128 + half + col, cv0 = L + (112 * w + 8 * h + tq) * 128 + (half ^ 64) + col;
      vmw0<7>();
      STAMP(26);
      {
        v4u f[14];
#pragma unroll
        for (int i = 0; i < 7; ++i) { f[2 * i] = trd2(ck0 + i * 2048, ck0 + i * 2048 + 512); f[2 * i + 1] = trd2(cv0 + i * 2048, cv0 + i * 2048 + 512); }
        SB();
        sfor<0, 7>([&](auto I) { constexpr int i = I;
          ck = mfma(ones, f[2 * i], ck); SB(); w_issue<2 * i, 2 * i + 1, 896u>(rwo, wov, wos, wol); SB();
          cv = mfma(ones, f[2 * i + 1], cv); SB(); w_issue<2 * i + 1, 2 * i + 2, 896u>(rwo, wov, wos, wol); SB(); });
        w_issue<14, 15, 896u>(rwo, wov, wos, wol);
      }
      {
        const u32 gw = (u32)(b * 4 + w);
        const u32 l2 = gw * 24 + (lane < 24 ? lane : 0), r2 = l2 >> 5, l1 = gw * 21 + (lane < 21 ? lane : 0), r1 = l1 / 28;
        tc2 = touch(mkrs(p.w2, TOUCH ? S_ * F_ * 2 : 0), ((r2 / 192) * 448 + r2 % 192) * 4096u + (l2 & 31) * 128u, 0);
        tc3 = touch(mkrs(p.w1, TOUCH ? S_ * F_ * 2 : 0), ((r1 / 192) * 512 + r1 % 192) * 3584u + (l1 % 28) * 128u, 0);
      }
      STAMP(27);
      touch_done(tcc);
      asm volatile("" : "+v"(e0));
      tag = (((u64)(u32)__builtin_amdgcn_readfirstlane(e0.y) << 32) | (u64)(u32)__builtin_amdgcn_readfirstlane(e0.x)) + 1ull;
      *(LAS float*)(L + L_CSP + w * 384 + 128 + lane * 4) = h ? cv[0] : ck[0];
      bar();
      {
        lptr c28 = L + (112 * w + lane) * 128 + (((lane >> 1) & 1) ^ 1) * 64 + shift + 56;
        *(LAS unsigned short*)c28 = 0x3F80;
        if (lane < 48) *(LAS unsigned short*)(c28 + 64 * 128) = 0x3F80;
        float a0 = 0.f;
#pragma unroll
        for (int v = 0; v < 4; ++v) a0 += *(LAS float*)(L + L_CSP + v * 384 + 128 + lane * 4);
        *(LAS float*)(L + L_CSF + w * 512 + 128 + lane * 4) = a0;
      }
      bar();
      kv_head();
      STAMP(1);
      SB();
      sfor<0, 28>([&](auto I) { kv_step(I, []() {}); });
      asm volatile("" : "+a"(kt), "+a"(vv));
    } else {
      kv_head();
      const Poll pq = wait_flags(p.ws, flag_off(0, 4, (2 * T + (w >> 1)) * 8 + (lane < 7 ? lane : 0)), tag);
      STAMP(1);
      const u32 xb = WS_XF + (u32)(tb * 28) * 1024u;
      const u32 sqv = (u32)(7 * h) * 128u + r * 4, sqs = WS_SQ + (u32)tb * 1792u;
      u32 sp[7];
      sfor<0, LA1>([&](auto I) { constexpr int k = I; X[k] = lda<k>(rws, lane * 16, xb); });
      sfor<0, 28>([&](auto I) {
        constexpr int k = I;
        vmw<(k + LA1 <= 35) ? LA1 - 1 : 34 - k>(X[k]);
        if constexpr (k == 0) poll_done(pq);
        kv_step(I, [&]() {
          if constexpr (k + LA1 < 28) X[k + LA1] = lda<k + LA1>(rws, lane * 16, xb);
          else if constexpr (k + LA1 < 35) sp[k + LA1 - 28] = lda1<(k + LA1 - 28) * 128>(rws, sqv, sqs);
        });
      });
      asm volatile("s_waitcnt vmcnt(0)" : "+v"(sp[0]), "+v"(sp[1]), "+v"(sp[2]), "+v"(sp[3]), "+v"(sp[4]), "+v"(sp[5]), "+v"(sp[6]));
      asm volatile("" : "+a"(kt), "+a"(vv));
      ssq = ((asf(sp[0]) + asf(sp[1])) + (asf(sp[2]) + asf(sp[3]))) + ((asf(sp[4]) + asf(sp[5])) + asf(sp[6]));
    }
    STAMP(18);
    float mean, rstd;
    {
      const float sum = half_add(h ? kt[12] : 0.f);
      const float sq = half_add(ssq);
      mean = sum * (1.f / 448.f);
      const float var = fmaxf(sq * (1.f / 448.f) - mean * mean, 0.f);
      rstd = __builtin_amdgcn_rsqf(var + EPS);
    }
    const float mr = mean * rstd;
    *(LAS float*)(L + L_MR + w * 256 + h * 128 + r * 4) = h ? mr : rstd;
    const u32 kvbase = WS_KV + l * KV_L + (u32)head * (64u * 4096u);
    lptr kvl = L + L_INBOX + lane * 16;
    v4u K0, K1, V0, V1;
    {
      u32 o[8];
#pragma unroll
      for (int i = 0; i < 4; ++i) {
        const f32x4 cs = *(LAS f32x4*)(L + L_CSF + w * 512 + 128 + (8 * i + 4 * h) * 4);
        o[2 * i] = pk(kt[4 * i] * rstd - mr * cs[0], kt[4 * i + 1] * rstd - mr * cs[1]);
        o[2 * i + 1] = pk(kt[4 * i + 2] * rstd - mr * cs[2], kt[4 * i + 3] * rstd - mr * cs[3]);
      }
      if (h) { o[6] = 0u; o[7] = 0u; }
      K0 = v4u{o[0], o[1], o[2], o[3]}; K1 = v4u{o[4], o[5], o[6], o[7]};
      st_sc1(K0, rws, lane * 16, kvbase + tb * 4096);
      st_sc1(K1, rws, lane * 16 + 1024, kvbase + tb * 4096);
      const float csv = *(LAS float*)(L + L_CSF + w * 512 + 256 + r * 4);
#pragma unroll
      for (int i = 0; i < 4; ++i) {
        const f32x4 rr = *(LAS f32x4*)(L + L_MR + w * 256 + (8 * i + 4 * h) * 4);
        const f32x4 mm = *(LAS f32x4*)(L + L_MR + w * 256 + 128 + (8 * i + 4 * h) * 4);
        o[2 * i] = pk(vv[4 * i] * rr[0] - mm[0] * csv, vv[4 * i + 1] * rr[1] - mm[1] * csv);
        o[2 * i + 1] = pk(vv[4 * i + 2] * rr[2] - mm[2] * csv, vv[4 * i + 3] * rr[3] - mm[3] * csv);
      }
      if (r >= 28) {
#pragma unroll
        for (int i = 0; i < 8; ++i) o[i] = 0x3F803F80u;
      }
      V0 = v4u{o[0], o[1], o[2], o[3]}; V1 = v4u{o[4], o[5], o[6], o[7]};
      st_sc1(V0, rws, lane * 16 + 2048, kvbase + tb * 4096);
      st_sc1(V1, rws, lane * 16 + 3072, kvbase + tb * 4096);
    }
    STAMP(19);
    if (l == 0) {
      vmw0<TLBT ? 8 : 6>();
      lptr t0 = L + L_WO + w * 15360 + (4 * h + tq) * 128 + (((4 * wa + cpart) ^ sw) * 16) + (tp & 1) * 8;
      lptr t1 = L + L_WO + w * 15360 + (4 * h + tq) * 128 + (((4 * (wa ^ 1) + cpart) ^ sw) * 16) + (tp & 1) * 8;
      lptr cq0 = L + L_WQ + (112 * w + 8 * h + tq) * 64 + col;
      v4u f[7];
#pragma unroll
      for (int i = 0; i < 7; ++i) f[i] = trd2(cq0 + i * 1024, cq0 + i * 1024 + 256);
#pragma unroll
      for (int k = 0; k < 8; ++k) {
        const int ro = (28 * (k >> 1) + 16 * (k & 1)) * 128;
        wf[k][0] = trd2(t0 + ro, t0 + ro + 1024); wf[k][1] = trd2(t1 + ro, t1 + ro + 1024);
      }
      SB();
      f32x16 cq;
#pragma unroll
      for (int i = 0; i < 16; ++i) cq[i] = 0.f;
#pragma unroll
      for (int i = 0; i < 7; ++i) cq = mfma(ones, f[i], cq);
      *(LAS float*)(L + L_CSP + w * 384 + r * 4) = cq[0];
      st_sc1(v4u{0u, 0u, 0u, 0u}, rws, head < 14 ? WS_STATS + h * STATS_L + (u32)((tb * 14 + head) * 512 + r * 16) : 0x40000000u, 0);
    }
    publish(p.ws, flag_off(l, 0, T * 16 + head), tag);
    touch_done(tc2); touch_done(tc3); touch_done(tc5); touch_done(tc6);
    STAMP(2);
#pragma unroll
    for (int k = 0; k < 2 * PD; ++k) wq[k] = WFQ(k);
    {
      lptr own = kvl + (8 + w) * 4096;
      *(LAS v4u*)own = K0; *(LAS v4u*)(own + 1024) = K1; *(LAS v4u*)(own + 2048) = V0; *(LAS v4u*)(own + 3072) = V1;
    }
    if (l == 0) {
      float a0 = 0.f;
#pragma unroll
      for (int v = 0; v < 4; ++v) a0 += *(LAS float*)(L + L_CSP + v * 384 + r * 4);
      *(LAS float*)(L + L_CSF + w * 512 + r * 4) = a0;
      const u32 wib = WS_WI + (u32)head * 86016u + (u32)tb * 1024u;
      st_sc1(*(LAS v4u*)(L + tb * 1024 + lane * 16), rws, lane * 16, wib);
      st_sc1(*(LAS v4u*)(L + (tb < 20 ? tb + 64 : tb) * 1024 + lane * 16), rws, lane * 16, tb < 20 ? wib + 65536u : 0x40000000u);
      const u32 xo = (u32)((2 * st + wb) * 32 + r) * 896u + (u32)(dt * 64 + wa * 32 + 4 * h) * 2u;
      const rsrc_t rx = mkrs(p.x, S_ * D_ * 2);
#pragma unroll
      for (int i = 0; i < 4; ++i) {
        const v2u v = __builtin_amdgcn_raw_buffer_load_b64(rx, xo + i * 16, 0, 0);
        xres[2 * i] = v.x; xres[2 * i + 1] = v.y;
      }
    }
    {
      const int tsrc = T - 2 + (w >> 1);
      const u32 f1 = opqv(flag_off(l, 0, (tsrc < 0 ? T : tsrc) * 16 + head));
      Poll pq;
      f32x16 qt;
#pragma unroll
      for (int i = 0; i < 16; ++i) qt[i] = 0.f;
      SB();
      sfor<0, 28>([&](auto I) {
        constexpr int k = I;
        qt = mfma(wq[k], X[k], qt);
        SB();
        if constexpr (k + 2 * PD < 28) wq[k + 2 * PD] = WFQ(k + 2 * PD);
        if constexpr (k == QP0) pq.a = poll_one(p.ws, f1);
        if constexpr (k == QP0 + 6) pq.b = poll_one(p.ws, f1);
        if constexpr (k == QP0 + 12) pq.c = poll_one(p.ws, f1);
        SB();
      });
      asm volatile("" : "+a"(qt));
      poll_finish(pq, p.ws, f1, tag);
      STAMP(3);
      const u32 so = tsrc >= 0 ? kvbase + (u32)(4 * T - 8 + 2 * w) * 4096u : 0x40000000u;
      v4u t[8];
      t[0] = lda<0>(rws, lane * 16, so); t[1] = lda<1>(rws, lane * 16, so); t[2] = lda<4>(rws, lane * 16, so); t[3] = lda<5>(rws, lane * 16, so);
      t[4] = lda<2>(rws, lane * 16, so); t[5] = lda<3>(rws, lane * 16, so); t[6] = lda<6>(rws, lane * 16, so); t[7] = lda<7>(rws, lane * 16, so);
      SB();
      v4u Q0, Q1;
      {
        const float sc = 0.18898223650461362f * 1.4426950408889634f;
        const float rs = rstd * sc, ms = mr * sc;
        u32 o[8];
#pragma unroll
        for (int i = 0; i < 4; ++i) {
          const f32x4 cs = *(LAS f32x4*)(L + L_CSF + w * 512 + (8 * i + 4 * h) * 4);
          o[2 * i] = pk(qt[4 * i] * rs - ms * cs[0], qt[4 * i + 1] * rs - ms * cs[1]);
          o[2 * i + 1] = pk(qt[4 * i + 2] * rs - ms * cs[2], qt[4 * i + 3] * rs - ms * cs[3]);
        }
        if (h) { o[6] = 0x00003F80u; o[7] = 0u; }
        Q0 = v4u{o[0], o[1], o[2], o[3]}; Q1 = v4u{o[4], o[5], o[6], o[7]};
      }
      f32x16 s8 = qk_own(K0, K1, Q0, Q1);
      u32 em[16], pm[8];
      f32x16 c0;
#pragma unroll
      for (int i = 0; i < 16; ++i) { const int key = (i & 3) + 8 * (i >> 2) + 4 * h; em[i] = key > r ? 0xffffffffu : 0u; c0[i] = asf(~em[i] & 0xF149F2CAu); }
#pragma unroll
      for (int i = 0; i < 8; ++i) pm[i] = (em[2 * i] & 0x0000ffffu) | (em[2 * i + 1] & 0xffff0000u);
#pragma unroll
      for (int i = 0; i < 16; ++i) asm volatile("" : "+v"(em[i]));
#pragma unroll
      for (int i = 0; i < 8; ++i) asm volatile("" : "+v"(pm[i]));
      tie_far(s8);
      float m4[4];
#pragma unroll
      for (int i = 0; i < 16; ++i) s8[i] = asf((em[i] & 0xF149F2CAu) | (~em[i] & asu(s8[i])));
#pragma unroll
      for (int i = 0; i < 4; ++i) m4[i] = fmaxf(fmaxf(s8[4 * i], s8[4 * i + 1]), fmaxf(s8[4 * i + 2], s8[4 * i + 3]));
      asm volatile("" : "+v"(c0), "+v"(m4[0]), "+v"(m4[1]), "+v"(m4[2]), "+v"(m4[3]));
      SB();
      {
        lptr dst = kvl + w * 8192;
        vmw<4>(t[0], t[1], t[2], t[3]);
        poll_done(pq);
        if (tsrc < 0 && h) { t[1][2] = 0xF149u; t[3][2] = 0xF149u; }
#pragma unroll
        for (int i = 0; i < 4; ++i) *(LAS v4u*)(dst + (i & 1) * 1024 + (i >> 1) * 4096) = t[i];
        vmw<0>(t[4], t[5], t[6], t[7]);
#pragma unroll
        for (int i = 0; i < 4; ++i) *(LAS v4u*)(dst + 2048 + (i & 1) * 1024 + (i >> 1) * 4096) = t[4 + i];
      }
      bar();
      {
        const u32 l1 = (u32)(b * 4 + w) * 35 + (lane < 35 ? lane : 0), r1 = l1 / 28;
        tc4 = touch(mkrs(p.w1, (TOUCH && l == 0) ? S_ * F_ * 2 : 0), ((r1 / 320) * 512 + 192 + r1 % 320) * 3584u + (l1 % 28) * 128u, 0);
      }
      STAMP(17);
      lptr kq = kvl + w * 4096;
      v4u Kr[8][2], Vr[8][2];
#pragma unroll
      for (int j = 0; j < 4; ++j) { Kr[j][0] = *(LAS v4u*)(kq + j * 4096); Kr[j][1] = *(LAS v4u*)(kq + j * 4096 + 1024); }
      SB();
      {
        f32x16 s[8];
        auto mx = [&](const f32x16& t, int half) {
#pragma unroll
          for (int i = 0; i < 4; ++i) m4[i] = fmaxf(fmaxf(m4[i], t[8 * half + 2 * i]), t[8 * half + 2 * i + 1]);
        };
        sfor<0, 8>([&](auto I) {
          constexpr int n = I;
          if constexpr (n == 0) s[0] = qk1(Kr[0][0], Q0, c0); else s[n] = qk1z(Kr[n][0], Q0);
          SB();
          if constexpr (n < 4) Kr[n + 4][0] = *(LAS v4u*)(kq + (n + 4) * 4096);
          Vr[n][0] = *(LAS v4u*)(kq + n * 4096 + 2048);
          if constexpr (n >= 2) mx(s[n - 2], 1);
          SB();
          qk2(s[n], Kr[n][1], Q1);
          SB();
          if constexpr (n < 4) Kr[n + 4][1] = *(LAS v4u*)(kq + (n + 4) * 4096 + 1024);
          Vr[n][1] = *(LAS v4u*)(kq + n * 4096 + 3072);
          if constexpr (n >= 1) { tie(s[n - 1]); mx(s[n - 1], 0); }
          SB();
        });
        mx(s[6], 1);
        SB();
        asm volatile("s_nop 7" : "+v"(s[7]));
        mx(s[7], 0); mx(s[7], 1);
      }
      const float nm = -half_max(fmaxf(fmaxf(m4[0], m4[1]), fmaxf(m4[2], m4[3])));
      f32x16 cm, o;
#pragma unroll
      for (int i = 0; i < 16; ++i) { cm[i] = nm; o[i] = 0.f; }
      {
        f32x16 s[9];
        u32 pp[9][8];
        float e[16];
        asm volatile("" : "+v"(cm));
        SB(); nop2();
        s[1] = qk1(Kr[1][0], Q0, cm); qk2(s[1], Kr[1][1], Q1);
        s8 = qk_own_c(K0, K1, Q0, Q1, cm);
        SB();
        sfor<1, 9>([&](auto I) {
          constexpr int c = I, nx = (c + 1) & 7;
          tie(s[c]);
          if constexpr (c == 8) {
            tie(s8);
#pragma unroll
            for (int i = 0; i < 16; ++i) s[8][i] = asf((asu(s[8][i]) & em[i]) | (asu(s8[i]) & ~em[i]));
          }
          auto ex = [&](int q) {
#pragma unroll
            for (int i = 0; i < 4; ++i) e[4 * q + i] = __builtin_amdgcn_exp2f(s[c][4 * q + i]);
          };
          auto cv = [&](int q) { pp[c][2 * q] = pk(e[4 * q], e[4 * q + 1]); pp[c][2 * q + 1] = pk(e[4 * q + 2], e[4 * q + 3]); };
          if constexpr (c < 8) s[c + 1] = qk1(Kr[nx][0], Q0, cm);
          SB(); if constexpr (c < 7) w_issue2<4 * c - 4, 4 * c - 3, 4096u, 28>(W2_ARGS); ex(0); SB();
          if constexpr (c < 8) qk2(s[c + 1], Kr[nx][1], Q1);
          SB(); if constexpr (c < 7) w_issue2<4 * c - 3, 4 * c - 2, 4096u, 28>(W2_ARGS); ex(1); cv(0); SB();
          if constexpr (c > 1) o = mfma(Vr[c - 1][1], v4u{pp[c - 1][4], pp[c - 1][5], pp[c - 1][6], pp[c - 1][7]}, o);
          SB(); if constexpr (c < 7) w_issue2<4 * c - 2, 4 * c - 1, 4096u, 28>(W2_ARGS); ex(2); cv(1); SB();
          if constexpr (c < 8) o = mfma(Vr[c][0], v4u{pp[c][0], pp[c][1], pp[c][2], pp[c][3]}, o);
          else {
            o = mfma(Vr[0][0], v4u{pp[8][0] & pm[0], pp[8][1] & pm[1], pp[8][2] & pm[2], pp[8][3] & pm[3]}, o);
            o = mfma(V0, v4u{pp[8][0] & ~pm[0], pp[8][1] & ~pm[1], pp[8][2] & ~pm[2], pp[8][3] & ~pm[3]}, o);
          }
          SB(); if constexpr (c < 7) w_issue2<4 * c - 1, 4 * c, 4096u, 28>(W2_ARGS); ex(3); cv(2); cv(3); SB();
        });
        o = mfma(Vr[0][1], v4u{pp[8][4] & pm[4], pp[8][5] & pm[5], pp[8][6] & pm[6], pp[8][7] & pm[7]}, o);
        o = mfma(V1, v4u{pp[8][4] & ~pm[4], pp[8][5] & ~pm[5], pp[8][6] & ~pm[6], pp[8][7] & ~pm[7]}, o);
      }
      const float inv = __builtin_amdgcn_rcpf(half_add(h ? o[12] : 0.f));
      u32 oo[8];
#pragma unroll
      for (int i = 0; i < 8; ++i) oo[i] = pk(o[2 * i] * inv, o[2 * i + 1] * inv);
      if (h) { oo[6] = 0u; oo[7] = 0u; }
      const u32 ob = WS_OF + l * OF_L + (u32)(tb * 16 + head) * 2048u;
      st_sc1(v4u{oo[0], oo[1], oo[2], oo[3]}, rws, lane * 16, ob);
      st_sc1(v4u{oo[4], oo[5], oo[6], oo[7]}, rws, lane * 16 + 1024, ob);
      publish(p.ws, flag_off(l, 1, T * 16 + head), tag);
      touch_done(tc4);
      STAMP(4);
    }
    }
    v4u bw4[20][2], bw5[16][2];
#define T5_PTRS \
    lptr t0 = L + ringw + (4 * h + tq) * 128 + (((4 * wb + cpart) ^ sw) * 16) + (tp & 1) * 8; \
    lptr t1 = L + ringw + (4 * h + tq) * 128 + (((4 * (wb ^ 1) + cpart) ^ sw) * 16) + (tp & 1) * 8;
#define T4_PTRS \
    lptr n0 = L + ringw + (8 * h + tq) * 128 + (((4 * wa + cpart) ^ sw) * 16) + (tp & 1) * 8; \
    lptr n1 = L + ringw + (8 * h + tq) * 128 + (((4 * (wa ^ 1) + cpart) ^ sw) * 16) + (tp & 1) * 8;
    {
      IDS
      T5_PTRS
      w_take<0, 12, 1024>(t0, t1, bw5);
      lgk0();
      w_issue2<0, 24, 3584u, 32>(W1_ARGS);
    }
    if (p35) {
      IDS
      const Poll pq = wait_flags(p.ws, flag_off(l, 1, (st >> 1) * 16 + 4 * w + (lane & 3)), tag);
      STAMP(5);
      const u32 o0 = WS_OF + l * OF_L + (u32)((2 * st + wb) * 16 + 4 * w) * 2048u;
      const u32 o1 = WS_OF + l * OF_L + (u32)((2 * st + (wb ^ 1)) * 16 + 4 * w) * 2048u;
      v4u of[8][2];
      sfor<0, LA3>([&](auto I) { constexpr int k = I; of[k][0] = lda<k>(rws, lane * 16, o0); of[k][1] = lda<k>(rws, lane * 16, o1); });
      f32x16 acc[4];
#pragma unroll
      for (int q = 0; q < 4; ++q)
#pragma unroll
        for (int i = 0; i < 16; ++i) acc[q][i] = 0.f;
      sfor<0, 8>([&](auto I) {
        constexpr int k = I;
        vmw<(k + LA3 <= 8) ? 2 * LA3 - 2 : 14 - 2 * k>(of[k][0], of[k][1]);
        if constexpr (k == 0) poll_done(pq);
        acc[1] = mfma(wf[k][0], of[k][1], acc[1]);
        SB();
        if constexpr (k + LA3 < 8) of[k + LA3][0] = lda<k + LA3>(rws, lane * 16, o0);
        SB();
        acc[2] = mfma(wf[k][1], of[k][0], acc[2]);
        acc[3] = mfma(wf[k][1], of[k][1], acc[3]);
        SB();
        if constexpr (k + LA3 < 8) of[k + LA3][1] = lda<k + LA3>(rws, lane * 16, o1);
        SB();
        acc[0] = mfma(wf[k][0], of[k][0], acc[0]);
        if constexpr (k < 7) SB();
      });
      const f32x16 y = reduce4(L, acc, w, lane);
      float xv[16], s1 = 0.f, s2 = 0.f;
#pragma unroll
      for (int i = 0; i < 8; ++i) {
        xres[i] = pk(blo(xres[i]) + y[2 * i], bhi(xres[i]) + y[2 * i + 1]);
        xv[2 * i] = blo(xres[i]); xv[2 * i + 1] = bhi(xres[i]);
        s1 += xv[2 * i] + xv[2 * i + 1];
        s2 += xv[2 * i] * xv[2 * i] + xv[2 * i + 1] * xv[2 * i + 1];
      }
      s1 = half_add(s1); s2 = half_add(s2);
      const u32 tg = (u32)tag | 1u;
      const u32 sb = WS_STATS + l * STATS_L + (u32)((2 * st + wb) * 14) * 512u;
      if (h == 0) st_sc1(v4u{asu(s1), tg, asu(s2), tg}, rws, r * 16, sb + (u32)(2 * dt + wa) * 512u);
      STAMP(6);
      {
        T4_PTRS
        w_take<0, NB, 512>(n0, n1, bw4);
        lgk0();
        w_issue2<24, 24 + 2 * NB, 3584u, 32>(W1_ARGS);
      }
      float t1s, t2s;
      for (u32 spins = 0;;) {
        v4u gr[7];
#pragma unroll
        for (int i = 0; i < 7; ++i) gr[i] = ld_sc1(rws, (u32)(7 * h) * 512u + r * 16 + i * 512, sb);
        bool ok = true; t1s = 0.f; t2s = 0.f;
#pragma unroll
        for (int i = 0; i < 7; ++i) { ok &= (gr[i].y == tg) & (gr[i].w == tg); t1s += asf(gr[i].x); t2s += asf(gr[i].z); }
        if (__all(ok)) break;
        asm volatile("" ::: "memory");
        if (spin_out(p.ws, spins, (u32)tag)) break;
      }
      STAMP(7);
      t1s = half_add(t1s); t2s = half_add(t2s);
      const float m1 = t1s * (1.f / 448.f);
      const float var = fmaxf(t2s * (1.f / 448.f) - m1 * m1, 0.f);
      const float r1 = __builtin_amdgcn_rsqf(var + EPS);
      const float r2 = __builtin_amdgcn_rsqf(var * r1 * r1 + EPS);
      const float rr = r1 * r2, mm = m1 * rr;
      lptr tw = L + L_TR + w * 2304;
#pragma unroll
      for (int i = 0; i < 4; ++i)
        *(LAS v2u*)(tw + r * 72 + i * 16 + h * 8) = v2u{pk(xv[4 * i] * rr - mm, xv[4 * i + 1] * rr - mm), pk(xv[4 * i + 2] * rr - mm, xv[4 * i + 3] * rr - mm)};
      lgk0();
      lptr tr = tw + (8 * h + tq) * 72 + ch16 * 32 + tp * 8;
      const u32 hb = WS_HF + l * HF_L + (u32)((2 * dt + wa) * 128 + (2 * st + wb) * 2) * 1024u;
      st_sc1(trd2(tr, tr + 4 * 72), rws, lane * 16, hb);
      st_sc1(trd2(tr + 16 * 72, tr + 20 * 72), rws, lane * 16 + 1024, hb);
      publish(p.ws, flag_off(l, 2, dt * 32 + st), tag);
      STAMP(8);
    } else {
      IDS
      T4_PTRS
      drain();
      w_take<0, NB, 512>(n0, n1, bw4);
      lgk0();
      w_issue2<24, 24 + 2 * NB, 3584u, 32>(W1_ARGS);
      drain();
    }
    if (p4) {
      IDS
      T4_PTRS
      v2u bias[4];
      {
        const rsrc_t rb1 = mkrs(p.b1, F_ * 2);
        const u32 bo = (u32)(ft * 64 + wa * 32 + 4 * h) * 2u;
        sfor<0, 4>([&](auto I) { constexpr int i = I; bias[i] = ldp2<16 * i>(rb1, bo); });
      }
      w_take<NB, 20, 512>(n0, n1, bw4);
      lgk0();
      w_issue2<24 + 2 * NB, 64, 3584u, 32>(W1_ARGS);
      STAMP(9);
      const Poll pq = wait_flags(p.ws, flag_off(l, 2, dt4 * 32 + 8 * w + (lane & 7)), tag);
      asm volatile("" : "+v"(bias[0]), "+v"(bias[1]), "+v"(bias[2]), "+v"(bias[3]));
      STAMP(10);
      f32x16 acc[4];
      const u32 hb = WS_HF + l * HF_L + (u32)(32 * w) * 1024u;
      gemm_main<32, 20, 512, true, LA4>(rws, lane * 16, hb + (u32)(2 * dt4 + wb) * 131072u, hb + (u32)(2 * dt4 + (wb ^ 1)) * 131072u, ROT4, n0, n1, bw4, acc,
                                        [&](auto I) { constexpr int i = I; w_issue2<24 + i, 25 + i, 4096u, 28>(W2_ARGS); });
      poll_done(pq);
      STAMP(11);
      const f32x16 y = reduce4(L, acc, w, lane);
      u32 o[8];
#pragma unroll
      for (int i = 0; i < 8; ++i) {
        const u32 bb = (i & 1) ? bias[i >> 1].y : bias[i >> 1].x;
        float gv[2];
#pragma unroll
        for (int e = 0; e < 2; ++e) {
          const float u = y[2 * i + e] + (e ? bhi(bb) : blo(bb));
          const float z = u * (-2.302208198f - 0.1029432397f * u * u);
          gv[e] = u * __builtin_amdgcn_rcpf(1.f + __builtin_amdgcn_exp2f(z));
        }
        o[i] = pk(gv[0], gv[1]);
      }
      const u32 ub = WS_UF + l * UF_L + (u32)((2 * dt4 + wb) * 112 + 4 * ft + 2 * wa) * 1024u;
      st_sc1(v4u{o[0], o[1], o[2], o[3]}, rws, lane * 16, ub);
      st_sc1(v4u{o[4], o[5], o[6], o[7]}, rws, lane * 16 + 1024, ub);
      publish(p.ws, flag_off(l, 3, dt4 * 32 + ft), tag);
      STAMP(12);
    } else {
      IDS
      {
        const rsrc_t rwf = mkrs(p.w2, F_ * S_ * 2);
        const int nb = (g & 1) ? 4 : 11, ib = bi - ((g & 1) ? 28 : 21);
        for (int row = ib * 4 + w; row < F_; row += nb * 4 * 8) {
          sfor<0, 8>([&](auto I) { constexpr int i = I; dma16<0, i * 1024>(rwf, lane * 16 + pj * 1024, (u32)(row + i * nb * 4) * 4096u, lds0 + ringw); });
          asm volatile("s_waitcnt vmcnt(16)" ::: "memory");
        }
        drain();
      }
      w_issue2<24, 24 + 2 * LA4, 4096u, 28>(W2_ARGS);
      drain();
    }
    if (p35) {
      IDS
      T5_PTRS
      u32 bias;
      asm volatile("s_nop 4\n\tbuffer_load_ushort %0, %1, %2, 0 offen" : "=v"(bias) : "v"((u32)((2 * st + wb) * 32 + r) * 2u), "s"(mkrs(p.b2, S_ * 2)));
      w_issue2<24 + 2 * LA4, 48, 4096u, 28>(W2_ARGS);
      w_take<12, 16, 1024>(t0, t1, bw5);
      lgk0();
      w_issue2<48, 56, 4096u, 28>(W2_ARGS);
      STAMP(13);
      const Poll pq = wait_flags(p.ws, flag_off(l, 3, dt * 32 + 7 * w + (lane < 7 ? lane : 0)), tag);
      asm volatile("" : "+v"(bias));
      STAMP(14);
      const u32 wis = l == 0 ? WS_WI + (u32)head * 86016u + (u32)w * 21504u : 0x40000000u;
      v4u wi[21];
      f32x16 acc[4];
      const u32 ub = WS_UF + l * UF_L + (u32)(28 * w) * 1024u;
      gemm_main<28, 16, 1024, false, LA5>(rws, lane * 16, ub + (u32)(2 * dt + wa) * 114688u, ub + (u32)(2 * dt + (wa ^ 1)) * 114688u, ROT5, t0, t1, bw5, acc,
                                          [&](auto I) { constexpr int i = I; wi[i] = lda<i>(rws, lane * 16, wis); });
      poll_done(pq);
      STAMP(15);
      const f32x16 y = reduce4(L, acc, w, lane);
      const float bv = blo(bias);
#pragma unroll
      for (int i = 0; i < 8; ++i) xres[i] = pk(blo(xres[i]) + (y[2 * i] + bv), bhi(xres[i]) + (y[2 * i + 1] + bv));
      v4u xo[2];
#pragma unroll
      for (int s = 0; s < 2; ++s) {
        auto a = __builtin_amdgcn_permlane32_swap(xres[4 * s], xres[4 * s + 2], false, false);
        auto c = __builtin_amdgcn_permlane32_swap(xres[4 * s + 1], xres[4 * s + 3], false, false);
        xo[s] = v4u{a[0], c[0], a[1], c[1]};
      }
      if (l == 0) {
        const u32 xb = WS_XF + (u32)((2 * st + wb) * 28 + 4 * dt) * 1024u + wa * 512;
        st_sc1(xo[0], rws, h * 1024 + r * 16, xb);
        st_sc1(xo[1], rws, h * 1024 + r * 16 + 2048, xb);
        float q0 = 0.f, q1 = 0.f;
#pragma unroll
        for (int i = 0; i < 8; i += 2) { q0 = __builtin_amdgcn_fdot2_f32_bf16(asbf2(xres[i]), asbf2(xres[i]), q0, false); q1 = __builtin_amdgcn_fdot2_f32_bf16(asbf2(xres[i + 1]), asbf2(xres[i + 1]), q1, false); }
        const float q = half_add(q0 + q1);
        if (h == 0) __builtin_amdgcn_raw_buffer_store_b32(asu(q), rws, r * 4, WS_SQ + (u32)((2 * st + wb) * 14 + 2 * dt + wa) * 128u, 16);
        publish(p.ws, flag_off(0, 4, st * 8 + dt), tag);
        sfor<2 * LA5, 21>([&](auto I) { constexpr int i = I; wi[i] = lda<i>(rws, lane * 16, wis); });
        sfor<0, 21>([&](auto I) { constexpr int i = I; vmw<(i < 2 * LA5) ? 21 - 2 * LA5 : 20 - i>(wi[i]); *(LAS v4u*)(L + w * 21504 + i * 1024 + lane * 16) = wi[i]; });
        bar();
      } else {
        sfor<0, 2 * LA5>([&](auto I) { constexpr int i = I; vmw<0>(wi[i]); });
        const rsrc_t rout = mkrs(p.out, S_ * D_ * 2);
        const u32 oo = (u32)((2 * st + wb) * 32 + r) * 896u + (u32)(dt * 64 + wa * 32 + 8 * h) * 2u;
        __builtin_amdgcn_raw_buffer_store_b128(xo[0], rout, oo, 0, 0);
        __builtin_amdgcn_raw_buffer_store_b128(xo[1], rout, oo + 32, 0, 0);
        if (b == 0 && tid == 0) __hip_atomic_store((GAS u64*)(p.ws + WS_EPOCH), tag, RLX_AGENT);
      }
      STAMP(16);
    } else if (l == 0) {
      IDS
      const Poll pq = wait_flags(p.ws, flag_off(0, 1, (lane & 15) * 16 + head), tag);
      const u32 wis = WS_WI + (u32)head * 86016u + (u32)w * 21504u;
      v4u wi[21];
      bar();
      sfor<0, 21>([&](auto I) { constexpr int i = I; wi[i] = lda<i>(rws, lane * 16, wis); });
      sfor<0, 21>([&](auto I) { constexpr int i = I; vmw<20 - i>(wi[i]); *(LAS v4u*)(L + w * 21504 + i * 1024 + lane * 16) = wi[i]; });
      poll_done(pq);
      bar();
    }
  }
  if (blockIdx.x == DIAG_BLK) {
    const u32 ta = __builtin_amdgcn_readlane(DIAG_SRC, DIAG_A), tb_ = __builtin_amdgcn_readlane(DIAG_SRC, DIAG_B);
    const u32 t0 = (u32)__builtin_amdgcn_s_memrealtime();
    const u32 d = (tb_ - ta) * DIAG_MUL / DIAG_DIV;
    if (d < 200000u) while ((u32)__builtin_amdgcn_s_memrealtime() - t0 < d) __builtin_amdgcn_s_sleep(1);
  }
  asm volatile(".Lfwd_last:");
}

extern "C" void kernel_launch(void* const* d_in, const int* in_sizes, int n_in, void* d_out, int out_size, void* d_ws, size_t ws_size,
                              hipStream_t stream) {
  Params p;
  p.x = d_in[0]; p.wqkv = d_in[1]; p.wo = d_in[2]; p.w1 = d_in[4]; p.b1 = d_in[5]; p.w2 = d_in[6]; p.b2 = d_in[7];
  p.out = d_out; p.ws = (unsigned char*)d_ws;
  (void)hipFuncSetAttribute((const void*)fused_fwd, hipFuncAttributeMaxDynamicSharedMemorySize, L_END);
  hipLaunchKernelGGL(fused_fwd, dim3(256), dim3(256), L_END, stream, p);
}
```

```cpp
#include <hip/hip_runtime.h>
#include <hip/hip_bf16.h>
#include <type_traits>

typedef unsigned u32;
typedef unsigned long long u64;
typedef u32 v4u __attribute__((ext_vector_type(4)));
typedef u32 v2u __attribute__((ext_vector_type(2)));
typedef float f32x16 __attribute__((ext_vector_type(16)));
typedef float f32x4 __attribute__((ext_vector_type(4)));
typedef float f32x2 __attribute__((ext_vector_type(2)));
typedef __bf16 bf16x8 __attribute__((ext_vector_type(8)));
typedef __bf16 bf16x2 __attribute__((ext_vector_type(2)));
typedef short s16x4 __attribute__((ext_vector_type(4)));
#define LAS __attribute__((address_space(3)))
#define GAS __attribute__((address_space(1)))
#define DI __device__ __forceinline__
#define RLX_AGENT __ATOMIC_RELAXED, __HIP_MEMORY_SCOPE_AGENT
#define SB() __builtin_amdgcn_sched_barrier(0)
typedef __amdgpu_buffer_rsrc_t rsrc_t;
typedef LAS unsigned char* lptr;

#define DBG_STAMP 0
#define DBG_WAVE 0
#define POLL_GAP "3"
#define TOUCH 1
#define QP0 12
#define NB 10
#define CODE_TOUCH 1
#define TLBT 0
#define NOW 0
#define PA0 12
#define PA1 18
#define PB0 12
#define PC0 10
#define PC1 5
#define PD0 8
#define PD1 4
#define TB1 3
#define NODMA 0
#define LA4 6
#define LA5 6
#define LA1 12
#define LA3 6
#define ROT4 0
#define ROT5 0
#define ROT1 (head & 1)
#define WARM2 1

constexpr int S_ = 2048, D_ = 448, F_ = 1792;
constexpr u32 WS_EPOCH = 0, WS_TMO = 128;
constexpr u32 WS_FLAGS = 4096;
constexpr u32 WS_STATS = WS_FLAGS + 2 * 5 * 256 * 128;
constexpr u32 STATS_L = 64 * 14 * 512;
constexpr u32 WS_KV = 2u << 20, KV_L = 4u << 20;
constexpr u32 WS_OF = WS_KV + 2 * KV_L, OF_L = 2u << 20;
constexpr u32 WS_HF = WS_OF + 2 * OF_L, HF_L = 2u << 20;
constexpr u32 WS_UF = WS_HF + 2 * HF_L, UF_L = 2u << 20;
constexpr u32 WS_XF = WS_UF + 2 * UF_L;
constexpr u32 WS_SQ = WS_XF + 64 * 28 * 1024;
constexpr u32 WS_WI = WS_XF + (2u << 20);
constexpr u32 WS_END = WS_WI + (2u << 20);
constexpr u32 WS_DBG = WS_END;
static_assert(WS_STATS + 2 * STATS_L <= WS_KV, "ws map");
constexpr u32 L_RING = 0, RING_W = 24576;
constexpr u32 L_INBOX = 98304, INBOX_W = 12288;
constexpr u32 L_WQ = 57344;
constexpr u32 L_WO = 86016;
constexpr u32 L_CSP = 147456;
constexpr u32 L_CSF = L_CSP + 1536;
constexpr u32 L_MR = L_CSF + 2048;
constexpr u32 L_TR = L_MR + 1024;
constexpr u32 L_END = L_TR + 4 * 2304;
constexpr u32 kSpinCap = 1u << 16;
constexpr float EPS = 1e-5f;

struct Params {
  const void* x; const void* wqkv; const void* wo; const void* w1; const void* b1; const void* w2; const void* b2;
  void* out; unsigned char* ws;
};

DI f32x16 mfma(v4u a, v4u b, f32x16 c) {
  return __builtin_amdgcn_mfma_f32_32x32x16_bf16(__builtin_bit_cast(bf16x8, a), __builtin_bit_cast(bf16x8, b), c, 0, 0, 0);
}
DI f32x16 qk1(v4u k0, v4u q0, f32x16 ci) {
  f32x16 c;
  asm volatile("v_mfma_f32_32x32x16_bf16 %0, %1, %2, %3" : "=&v"(c) : "a"(k0), "v"(q0), "v"(ci));
  return c;
}
DI f32x16 qk1z(v4u k0, v4u q0) {
  f32x16 c;
  asm volatile("v_mfma_f32_32x32x16_bf16 %0, %1, %2, 0" : "=&v"(c) : "a"(k0), "v"(q0));
  return c;
}
DI void qk2(f32x16& c, v4u k1, v4u q1) { asm volatile("v_mfma_f32_32x32x16_bf16 %0, %1, %2, %0" : "+v"(c) : "a"(k1), "v"(q1)); }
DI f32x16 qk_own(v4u k0, v4u k1, v4u q0, v4u q1) {
  f32x16 c;
  asm volatile("s_nop 1\n\tv_mfma_f32_32x32x16_bf16 %0, %1, %3, 0\n\tv_mfma_f32_32x32x16_bf16 %0, %2, %4, %0" : "=&v"(c) : "v"(k0), "v"(k1), "v"(q0), "v"(q1));
  return c;
}
DI f32x16 qk_own_c(v4u k0, v4u k1, v4u q0, v4u q1, f32x16 ci) {
  f32x16 c;
  asm volatile("s_nop 1\n\tv_mfma_f32_32x32x16_bf16 %0, %1, %3, %5\n\tv_mfma_f32_32x32x16_bf16 %0, %2, %4, %0" : "=&v"(c) : "v"(k0), "v"(k1), "v"(q0), "v"(q1), "v"(ci));
  return c;
}
DI void tie(f32x16& c) { asm volatile("" : "+v"(c)); }
DI void tie_far(f32x16& c) { asm volatile("s_nop 7\n\ts_nop 4" : "+v"(c)); }
DI void nop2() { asm volatile("s_nop 1"); }
DI u32 pk(float lo, float hi) { f32x2 v = {lo, hi}; return __builtin_bit_cast(u32, __builtin_convertvector(v, bf16x2)); }
DI float asf(u32 v) { return __builtin_bit_cast(float, v); }
DI u32 asu(float v) { return __builtin_bit_cast(u32, v); }
DI bf16x2 asbf2(u32 v) { return __builtin_bit_cast(bf16x2, v); }
DI float blo(u32 v) { return __builtin_bit_cast(float, v << 16); }
DI float bhi(u32 v) { return __builtin_bit_cast(float, v & 0xffff0000u); }
DI void bar() { asm volatile("s_waitcnt lgkmcnt(0)\n\ts_barrier" ::: "memory"); }
DI void drain() { asm volatile("s_waitcnt vmcnt(0)" ::: "memory"); }
DI void lgk0() { asm volatile("s_waitcnt lgkmcnt(0)" ::: "memory"); }
template <u32 SI, u32 LI> DI void dma16(rsrc_t rs, u32 voff, u32 soff, u32 lds) {
  u32 t;
  asm volatile("s_add_u32 m0, %1, %5\n\ts_add_u32 %0, %4, %6\n\tbuffer_load_dwordx4 %2, %3, %0 offen lds"
               : "=&s"(t) : "s"(lds), "v"(voff), "s"(rs), "s"(soff), "i"(LI), "i"(SI) : "memory", "scc");
}
DI void dma_fake(rsrc_t rs, u32 voff, u32 lds) {
  asm volatile("s_mov_b32 m0, %0\n\ts_mov_b64 exec, 1\n\tbuffer_load_dword %1, %2, 0 offen lds\n\ts_mov_b64 exec, -1" :: "s"(lds), "v"(voff), "s"(rs) : "memory");
}
template <int K> DI v4u lda(rsrc_t rs, u32 voff, u32 soff) {
  v4u d; u32 t;
  asm volatile("s_add_u32 %1, %4, %5\n\tbuffer_load_dwordx4 %0, %2, %3, %1 offen offset:%6 sc1"
               : "=v"(d), "=&s"(t) : "v"(voff), "s"(rs), "s"(soff), "i"((K >> 2) * 4096), "i"((K & 3) * 1024) : "scc");
  return d;
}
template <int IMM> DI v4u ldp(rsrc_t rs, u32 voff, u32 soff) {
  v4u d;
  asm volatile("buffer_load_dwordx4 %0, %1, %2, %3 offen offset:%4" : "=v"(d) : "v"(voff), "s"(rs), "s"(soff), "i"(IMM));
  return d;
}
template <int IMM> DI v4u ldg(rsrc_t rs, u32 voff, u32 soff) {
  v4u d;
  asm volatile("buffer_load_dwordx4 %0, %1, %2, %3 offen offset:%4 sc1" : "=v"(d) : "v"(voff), "s"(rs), "s"(soff), "i"(IMM));
  return d;
}
template <int IMM> DI v2u ldp2(rsrc_t rs, u32 voff) {
  v2u d;
  asm volatile("buffer_load_dwordx2 %0, %1, %2, 0 offen offset:%3" : "=v"(d) : "v"(voff), "s"(rs), "i"(IMM));
  return d;
}
DI void pin4(float (&v)[4]) { asm volatile("" : "+v"(v[0]), "+v"(v[1]), "+v"(v[2]), "+v"(v[3])); }
template <int IMM> DI u32 lda1(rsrc_t rs, u32 voff, u32 soff) {
  u32 d;
  asm volatile("buffer_load_dword %0, %1, %2, %3 offen offset:%4 sc1" : "=v"(d) : "v"(voff), "s"(rs), "s"(soff), "i"(IMM));
  return d;
}
template <int N> DI void vmw0() { asm volatile("s_waitcnt vmcnt(%0)" ::"i"(N) : "memory"); }
template <int N> DI void vmw(v4u& a) { asm volatile("s_waitcnt vmcnt(%1)" : "+v"(a) : "i"(N)); }
template <int N> DI void vmw(v4u& a, v4u& b, v4u& c, v4u& d) { asm volatile("s_waitcnt vmcnt(%4)" : "+v"(a), "+v"(b), "+v"(c), "+v"(d) : "i"(N)); }
template <int N> DI void vmw(v4u& a, v4u& b) { asm volatile("s_waitcnt vmcnt(%2)" : "+v"(a), "+v"(b) : "i"(N)); }
template <int I, int N, class Fn> DI void sfor(Fn&& f) { if constexpr (I < N) { f(std::integral_constant<int, I>{}); sfor<I + 1, N>(f); } }
DI u32 opq(u32 v) { asm volatile("" : "+s"(v)); return v; }
DI u32 opqv(u32 v) { asm volatile("" : "+v"(v)); return v; }
DI v2u trd(lptr p) { return __builtin_bit_cast(v2u, __builtin_amdgcn_ds_read_tr16_b64_v4i16((LAS s16x4*)p)); }
DI v4u trd2(lptr p0, lptr p1) { v2u a = trd(p0), b = trd(p1); return v4u{a.x, a.y, b.x, b.y}; }
DI v4u ld_sc1(rsrc_t rs, u32 voff, u32 soff) { return __builtin_amdgcn_raw_buffer_load_b128(rs, voff, soff, 16); }
DI void st_sc1(v4u v, rsrc_t rs, u32 voff, u32 soff) { __builtin_amdgcn_raw_buffer_store_b128(v, rs, voff, soff, 16); }
DI rsrc_t mkrs(const void* p, u32 bytes) {
  u32 fl = 0x00020000;
  asm volatile("" : "+s"(p), "+s"(bytes), "+s"(fl));
  return __builtin_amdgcn_make_buffer_rsrc((void*)p, 0, bytes, fl);
}
DI float half_add(float v) {
  auto r = __builtin_amdgcn_permlane32_swap(asu(v), asu(v), false, false);
  const u32 a = r[0], b = r[1];
  return asf(a) + asf(b);
}
DI float half_max(float v) {
  auto r = __builtin_amdgcn_permlane32_swap(asu(v), asu(v), false, false);
  const u32 a = r[0], b = r[1];
  return fmaxf(asf(a), asf(b));
}
DI bool spin_out(unsigned char* ws, u32& spins, u32 tl) {
  ++spins;
  if ((spins & 63u) == 0u) {
    GAS u32* tmo = (GAS u32*)(ws + WS_TMO);
    if (__hip_atomic_load(tmo, RLX_AGENT) == tl) return true;
    if (spins > kSpinCap) { __hip_atomic_store(tmo, tl, RLX_AGENT); return true; }
  }
  return false;
}
struct Poll { v2u a, b, c; };
#define PL(R, O, B) "global_load_dwordx2 " R ", " O ", " B " sc1\n\t"
DI v2u poll_one(unsigned char* ws, u32 off) {
  v2u r;
  asm volatile(PL("%0", "%1", "%2") : "=v"(r) : "v"(off), "s"(ws) : "memory");
  return r;
}
DI void poll_finish(Poll& q, unsigned char* ws, u32 off, u64 tag) {
  u32 left;
#define PC(R) "s_waitcnt vmcnt(2)\n\tv_cmp_ne_u64 vcc, %6, " R "\n\ts_cbranch_vccz 2f\n\t" PL(R, "%4", "%5")
  asm volatile("s_mov_b32 %3, 0x8000\n1:\n\t" PC("%0") PC("%1") PC("%2") "s_sub_u32 %3, %3, 1\n\ts_cbranch_scc0 1b\n2:"
               : "+v"(q.a), "+v"(q.b), "+v"(q.c), "=&s"(left) : "v"(off), "s"(ws), "s"(tag) : "vcc", "scc", "memory");
#undef PC
  if (left == 0xffffffffu) { drain(); __hip_atomic_store((GAS u32*)(ws + WS_TMO), (u32)tag, RLX_AGENT); }
}
template <int CA, int CB, int CC> DI void poll_finish_mixed(Poll& q, unsigned char* ws, u32 off, u64 tag) {
  u32 left;
#define PE(R, N) "s_waitcnt vmcnt(" N ")\n\tv_cmp_ne_u64 vcc, %6, " R "\n\ts_cbranch_vccz 2f\n\t"
#define PC(R) "s_waitcnt vmcnt(2)\n\tv_cmp_ne_u64 vcc, %6, " R "\n\ts_cbranch_vccz 2f\n\t" PL(R, "%4", "%5")
  asm volatile("s_mov_b32 %3, 0x8000\n\t" PE("%0", "%7") PL("%0", "%4", "%5") PE("%1", "%8") PL("%1", "%4", "%5") PE("%2", "%9") PL("%2", "%4", "%5")
               "1:\n\t" PC("%0") PC("%1") PC("%2") "s_sub_u32 %3, %3, 1\n\ts_cbranch_scc0 1b\n2:"
               : "+v"(q.a), "+v"(q.b), "+v"(q.c), "=&s"(left) : "v"(off), "s"(ws), "s"(tag), "i"(CA), "i"(CB + 1), "i"(CC + 2) : "vcc", "scc", "memory");
#undef PE
#undef PC
  if (left == 0xffffffffu) { drain(); __hip_atomic_store((GAS u32*)(ws + WS_TMO), (u32)tag, RLX_AGENT); }
}
DI Poll wait_flags(unsigned char* ws, u32 off, u64 tag) {
  Poll q;
  q.a = poll_one(ws, off); asm volatile("s_sleep " POLL_GAP);
  q.b = poll_one(ws, off); asm volatile("s_sleep " POLL_GAP);
  q.c = poll_one(ws, off);
  poll_finish(q, ws, off, tag);
  return q;
}
DI void poll_done(const Poll& q) { asm volatile("" ::"v"(q.a), "v"(q.b), "v"(q.c)); }
DI u32 touch(rsrc_t rs, u32 voff, u32 soff) {
  u32 d;
  asm volatile("s_nop 4\n\tbuffer_load_dword %0, %1, %2, %3 offen" : "=v"(d) : "v"(voff), "s"(rs), "s"(soff));
  return d;
}
DI void touch_done(u32 d) { asm volatile("" ::"v"(d)); }
DI void publish(unsigned char* ws, u32 off, u64 tag) {
  asm volatile("s_waitcnt vmcnt(0) lgkmcnt(0)\n\ts_barrier" ::: "memory");
  if (threadIdx.x == 0) __hip_atomic_store((GAS u64*)(ws + off), tag, RLX_AGENT);
  asm volatile("" ::: "memory");
}
DI u32 flag_off(int l, int type, int idx) { return WS_FLAGS + (u32)(((l * 5 + type) << 8) + idx) * 128u; }

#define STAMP(i) do {} while (0)

DI void red_put(lptr L, const f32x16& a, int q, int i, int w, int lane) {
  *(LAS f32x4*)(L + L_INBOX + (u32)(w ^ q) * INBOX_W + (q - 1) * 4096 + lane * 16 + i * 1024) = f32x4{a[4 * i], a[4 * i + 1], a[4 * i + 2], a[4 * i + 3]};
}
DI f32x16 red_get(lptr L, const f32x16& own, int w, int lane) {
  bar();
  f32x16 r = own;
  lptr src = L + L_INBOX + (u32)w * INBOX_W + lane * 16;
#pragma unroll
  for (int q = 0; q < 3; ++q)
#pragma unroll
    for (int i = 0; i < 4; ++i) {
      const f32x4 v = *(LAS f32x4*)(src + q * 4096 + i * 1024);
      r[4 * i] += v[0]; r[4 * i + 1] += v[1]; r[4 * i + 2] += v[2]; r[4 * i + 3] += v[3];
    }
  return r;
}
template <int Z, class MM, class Mem> DI void red_tail(lptr L, f32x16 (&acc)[4], int w, int lane, MM&& mm, Mem&& mem) {
  sfor<0, 4 * Z>([&](auto I) {
    constexpr int i = I, qi = i / Z, z = i % Z, q = (qi + 1) & 3;
    mm(std::integral_constant<int, q>{}, std::integral_constant<int, z>{});
    SB();
    mem(I);
    if constexpr (qi > 0 && Z >= 6 && z >= 2) red_put(L, acc[qi], qi, z - 2, w, lane);
    if constexpr (qi > 0 && Z < 6 && z >= Z - 2) { red_put(L, acc[qi], qi, 2 * (z - Z + 2), w, lane); red_put(L, acc[qi], qi, 2 * (z - Z + 2) + 1, w, lane); }
    SB();
  });
}

template <int P0, int P1, u32 RS> DI void w_issue(rsrc_t rs, u32 voff, u32 soff, u32 ring) {
  sfor<P0, P1>([&](auto I) { constexpr int i = I; dma16<i * 8 * RS, (i % 24) * 1024>(rs, voff, soff, ring); });
}
template <int P0, int P1, u32 RS, int HP> DI void w_issue2(rsrc_t rs, u32 voff, u32 s0, u32 s1, u32 ring) {
  sfor<P0, P1>([&](auto I) { constexpr int i = I; dma16<(i % HP) * 8 * RS, (i % 24) * 1024>(rs, voff, i < HP ? s0 : s1, ring); });
}
template <int K0, int K1, int TSTEP, int NR> DI void w_take(lptr t0, lptr t1, v4u (&bw)[NR][2]) {
#pragma unroll
  for (int kk = K0; kk < K1; ++kk) {
    const int sl = (kk % 12) * 2048;
    bw[kk][0] = trd2(t0 + sl, t0 + sl + TSTEP);
    bw[kk][1] = trd2(t1 + sl, t1 + sl + TSTEP);
  }
}
template <int NR> DI void w_undef(v4u (&bw)[NR][2], int k0, int k1) {
#pragma unroll
  for (int kk = 0; kk < NR; ++kk) if (kk >= k0 && kk < k1) { asm volatile("" : "=v"(bw[kk][0])); asm volatile("" : "=v"(bw[kk][1])); }
}

template <int NK, int NR, int TSTEP, bool WA, int LA, class Tail>
DI f32x16 gemm_main(lptr L, int w, int lane, rsrc_t ws, u32 a0, u32 a1, lptr t0, lptr t1, const v4u (&bw)[NR][2], Tail&& tail) {
  constexpr int Z = 6;
  static_assert(NK - NR == 12 && NK - LA >= 15 && LA == Z, "ring is 12 slots and must be consumed before the tail");
  const u32 voff = lane * 16;
  v4u a[NK][2], wl[12][2];
  f32x16 acc[4];
#define KOF(n) ((n) < 2 ? (n) : (n) < 14 ? NR + (n) - 2 : (n) - 12)
  sfor<0, LA>([&](auto I) { constexpr int n = I; a[n][0] = lda<KOF(n)>(ws, voff, a0); a[n][1] = lda<KOF(n)>(ws, voff, a1); });
#pragma unroll
  for (int q = 0; q < 4; ++q)
#pragma unroll
    for (int i = 0; i < 16; ++i) acc[q][i] = 0.f;
  auto mm = [&](auto Q, auto N) {
    constexpr int q = Q, n = N;
    constexpr int wi = WA ? (q >> 1) : (q & 1), ai = WA ? (q & 1) : (q >> 1);
    v4u wv;
    if constexpr (n >= 2 && n < 14) wv = wl[n - 2][wi]; else wv = bw[KOF(n)][wi];
    acc[q] = WA ? mfma(wv, a[n][ai], acc[q]) : mfma(a[n][ai], wv, acc[q]);
  };
  using I0 = std::integral_constant<int, 0>; using I1 = std::integral_constant<int, 1>; using I2 = std::integral_constant<int, 2>; using I3 = std::integral_constant<int, 3>;
  SB();
  sfor<0, NK - Z>([&](auto I) {
    constexpr int n = I, sl = ((NR + n) % 12) * 2048;
    asm volatile("" ::: "memory");
    vmw<2 * LA - 2>(a[n][0], a[n][1]);
    asm volatile("" ::: "memory");
    mm(I1{}, I);
    SB();
    a[n + LA][0] = lda<KOF(n + LA)>(ws, voff, a0);
    SB();
    mm(I2{}, I);
    SB();
    if constexpr (n < 12) wl[n][0] = trd2(t0 + sl, t0 + sl + TSTEP);
    SB();
    mm(I3{}, I);
    SB();
    a[n + LA][1] = lda<KOF(n + LA)>(ws, voff, a1);
    SB();
    mm(I0{}, I);
    SB();
    if constexpr (n < 12) wl[n][1] = trd2(t1 + sl, t1 + sl + TSTEP);
    SB();
  });
  red_tail<Z>(L, acc, w, lane,
              [&](auto Q, auto Zi) { constexpr int z = Zi, n = NK - Z + z;
                if constexpr (Q == 1) vmw<2 * (Z - 1 - z) + (z + 1) / 2>(a[n][0], a[n][1]);
                mm(Q, std::integral_constant<int, n>{}); },
              [&](auto I) { constexpr int i = I; if constexpr ((i & 1) == 0) tail(std::integral_constant<int, i / 2>{}); });
#undef KOF
  return red_get(L, acc[0], w, lane);
}

__global__ __launch_bounds__(256, 1) void fused_fwd(Params p) {
  extern __shared__ __attribute__((aligned(16))) unsigned char smem[];
  lptr L = (lptr)smem;
  const u32 lds0 = (u32)(size_t)L;
#define IDS \
  const int tid = (int)opqv(threadIdx.x), lane = tid & 63, r = lane & 31, h = lane >> 5; \
  const int w = (int)opq(w_), b = (int)opq(blockIdx.x), g = b & 7, bi = b >> 3, pj = g >> 1; \
  const int T = 2 * g + (bi >> 4), head = bi & 15; \
  const int d3 = (bi * 43) >> 7, m3 = bi - 3 * d3; \
  const int st = 8 * pj + ((g & 1) ? d3 : (bi >> 2)), dt = (g & 1) ? 4 + m3 : (bi & 3); \
  const int ft = 7 * pj + ((g & 1) ? (bi >> 2) : d3), dt4 = (g & 1) ? (bi & 3) : 4 + m3; \
  const int wa = w >> 1, wb = w & 1; \
  const int ch16 = (lane >> 4) & 1, tq = (lane & 15) >> 2, tp = lane & 3; \
  const u32 ringw = L_RING + (u32)w * RING_W; \
  const u32 dmarow = lane >> 3, dmach = ((lane & 7) ^ (((lane >> 4) & 1) << 2)) * 16; \
  const u32 sw = (u32)((tq >> 1) & 1) << 2, cpart = 2 * ch16 + (tp >> 1); \
  (void)r; (void)h; (void)T; (void)head; (void)st; (void)dt; (void)ft; (void)dt4; (void)wa; (void)wb; (void)ch16; (void)tq; (void)tp; (void)ringw; (void)dmarow; (void)dmach; (void)sw; (void)cpart;
  const u32 w_ = __builtin_amdgcn_readfirstlane(threadIdx.x >> 6);
  const bool p35 = (blockIdx.x & 1) == 0 || (blockIdx.x >> 3) < 24;
  const bool p4 = (blockIdx.x >> 3) < ((blockIdx.x & 1) ? 28u : 21u);

  const rsrc_t rws = mkrs(p.ws, WS_END);
  u64 tag = 0;
  u32 xres[8];
  v4u wf[8][2];
#pragma unroll
  for (int i = 0; i < 8; ++i) { xres[i] = 0; asm volatile("" : "=a"(wf[i][0])); asm volatile("" : "=a"(wf[i][1])); }

#pragma unroll 1
  for (int l = 0; l < 2; ++l) {
    STAMP(0);
    const rsrc_t rw1 = mkrs(p.w1, (p4 && !NOW) ? S_ * F_ * 2 : 0);
    const rsrc_t rw2 = mkrs(p.w2, (p35 && !NOW) ? F_ * S_ * 2 : 0);
#define W2_ARGS rw2, dmarow * 4096u + dmach + (u32)st * 128u, (u32)(448 * w + 224 * ROT5) * 4096u, (u32)(448 * w + 224 * (ROT5 ^ 1)) * 4096u, lds0 + ringw
#define W1_ARGS rw1, dmarow * 3584u + dmach + (u32)ft * 128u, (u32)(512 * w + 256 * ROT4) * 3584u, (u32)(512 * w + 256 * (ROT4 ^ 1)) * 3584u, lds0 + ringw
    {
    IDS
    const int tb = 4 * T + w;
    const u32 shift = (head & 1) * 8;
    const u32 half = (((tq >> 1) & 1) ^ 1) * 64, col = shift + ch16 * 32 + tp * 8;
    lptr wtk = L + (32 * h + tq) * 128 + half + col, wtv = L + (32 * h + tq) * 128 + (half ^ 64) + col, wtq = L + L_WQ + (32 * h + tq) * 64 + col;
    v4u X[28];
    f32x16 kt, vv;
#pragma unroll
    for (int i = 0; i < 16; ++i) { kt[i] = 0.f; vv[i] = 0.f; }
    float ssq;
    u32 tc2 = 0, tc3 = 0, tc4 = 0, tc5 = 0;
    v4u wk[28], wv[28], wq[28];
    constexpr int PD = 3;
#define WROW(k) (((k) >> 2) * 64 + ((k) & 3) * 8)
#define WFK(k) trd2(wtk + WROW(k) * 128, wtk + WROW(k) * 128 + 512)
#define WFV(k) trd2(wtv + WROW(k) * 128, wtv + WROW(k) * 128 + 512)
#define WFQ(k) trd2(wtq + WROW(k) * 64, wtq + WROW(k) * 64 + 256)
    auto kv_head = [&]() {
#pragma unroll
      for (int k = 0; k < PD; ++k) { wk[k] = WFK(k); wv[k] = WFV(k); }
      SB();
    };
    auto kv_step = [&](auto I, auto&& mem) {
      constexpr int k = I;
      kt = mfma(wk[k], X[k], kt);
      SB();
      if constexpr (k + PD < 28) wk[k + PD] = WFK(k + PD);
      mem();
      SB();
      vv = mfma(X[k], wv[k], vv);
      SB();
      if constexpr (k + PD < 28) wv[k + PD] = WFV(k + PD);
      SB();
    };
    const u32 wid = (u32)(bi * 4 + w);
    auto touch_w2a = [&]() { const u32 n = wid * 48 + (lane < 48 ? lane : 0), rr = n >> 3;
      return touch(mkrs(p.w2, TOUCH ? S_ * F_ * 2 : 0), ((rr / 192) * 448 + rr % 192) * 4096u + (n & 7) * 128u, (u32)pj * 1024u); };
    auto touch_w1a = [&]() { const u32 n = wid * 42 + (lane < 42 ? lane : 0), rr = n / 7;
      return touch(mkrs(p.w1, TOUCH ? S_ * F_ * 2 : 0), ((rr / 192) * 512 + rr % 192) * 3584u + (n % 7) * 128u, (u32)pj * 896u); };
    auto touch_w1b = [&](u32 part) { const u32 n = wid * 35 + (lane < 35 ? lane : 0), rr = n / 7;
      return touch(mkrs(p.w1, (TOUCH && (l == 0 || (TOUCH & 4))) ? S_ * F_ * 2 : 0), ((rr / 160) * 512 + 192 + rr % 160) * 3584u + (n % 7) * 128u, (u32)pj * 896u + part * (160u * 3584u)); };
    const v4u ones = {0x3F803F80u, 0x3F803F80u, 0x3F803F80u, 0x3F803F80u};
    if (l == 0) {
      v2u e0;
      asm volatile("global_load_dwordx2 %0, %1, %2 sc1" : "=v"(e0) : "v"(WS_EPOCH), "s"(p.ws));
      u32 tcc;
      {
        u64 pc; u32 back, len;
        asm volatile("s_getpc_b64 %0\n.Lfwd_here:\n\ts_mov_b32 %1, .Lfwd_here-_Z9fused_fwd6Params\n\ts_mov_b32 %2, .Lfwd_last-_Z9fused_fwd6Params" : "=s"(pc), "=s"(back), "=s"(len));
        tcc = touch(mkrs((const void*)(pc - back), CODE_TOUCH ? len : 0), lane * 128, (u32)(bi * 4 + w) * 8192u);
      }
      const rsrc_t rx = mkrs(p.x, S_ * D_ * 2), rwq = mkrs(p.wqkv, D_ * 3 * D_ * 2);
      const u32 hoff = (u32)(head * 56) - shift;
      const u32 xv = dmarow * 896u + (lane & 7) * 16u, xs = (u32)(tb * 32) * 896u;
      lptr xb = L + L_WO + w * 15360;
      lptr xw = xb + dmarow * 128, xr = xb + r * 128;
      const u32 xsw = (r >> 1) & 7, xc0 = ((lane & 7) ^ (lane >> 4)) * 16;
      const u32 kvv = dmarow * 2688u + 896u + ((((lane >> 2) ^ (lane >> 4) ^ 1) & 1) * 896u) + (lane & 3) * 16u + hoff;
      const u32 kvs = (u32)(112 * w) * 2688u, kvl0 = lds0 + (u32)(14 * w) * 1024u;
      const rsrc_t rwo = mkrs(p.wo, D_ * D_ * 2);
      const u32 wov = dmarow * 896u + dmach + (u32)dt * 128u, wos = (u32)(112 * w) * 896u, wol = lds0 + L_WO + (u32)w * 15360u;
      const u32 qv = (u32)(lane >> 2) * 2688u + (lane & 3) * 16u + hoff, ql = lds0 + L_WQ + (u32)(7 * w) * 1024u;
      sfor<0, 28>([&](auto I) { constexpr int k = I; X[k] = ldp<(k >> 2) * 128>(rx, xv, xs + (k & 3) * 7168u); });
      sfor<0, 7>([&](auto I) { constexpr int i = I; dma16<i * 8 * 2688, i * 1024>(rwq, kvv, kvs, kvl0); });
      STAMP(24);
      float sq4[4] = {0.f, 0.f, 0.f, 0.f};
      sfor<0, 7>([&](auto I) {
        constexpr int G = I;
        vmw<24 - 4 * G + 7 + 2 * G>(X[4 * G], X[4 * G + 1], X[4 * G + 2], X[4 * G + 3]);
#pragma unroll
        for (int i = 0; i < 4; ++i) *(LAS v4u*)(xw + i * 1024 + (xc0 ^ ((i & 1) * 64))) = X[4 * G + i];
#pragma unroll
        for (int c = 0; c < 4; ++c) X[4 * G + c] = *(LAS v4u*)(xr + (((4 * h + c) ^ xsw) * 16));
        if constexpr (2 * G < 7) dma16<(7 + 2 * G) * 8 * 2688, (7 + 2 * G) * 1024>(rwq, kvv, kvs, kvl0); else dma16<(2 * G - 7) * 16 * 2688, (2 * G - 7) * 1024>(rwq, qv, kvs, ql);
        if constexpr (2 * G + 1 < 7) dma16<(8 + 2 * G) * 8 * 2688, (8 + 2 * G) * 1024>(rwq, kvv, kvs, kvl0); else dma16<(2 * G - 6) * 16 * 2688, (2 * G - 6) * 1024>(rwq, qv, kvs, ql);
#pragma unroll
        for (int i = 0; i < 16; ++i) { const bf16x2 xx = asbf2(X[4 * G + (i >> 2)][i & 3]); sq4[i & 3] = __builtin_amdgcn_fdot2_f32_bf16(xx, xx, sq4[i & 3], false); }
        pin4(sq4);
        SB();
      });
      ssq = (sq4[0] + sq4[1]) + (sq4[2] + sq4[3]);
      STAMP(25);
      f32x16 ck, cv;
#pragma unroll
      for (int i = 0; i < 16; ++i) { ck[i] = 0.f; cv[i] = 0.f; }
      lptr ck0 = L + (112 * w + 8 * h + tq) * 128 + half + col, cv0 = L + (112 * w + 8 * h + tq) * 128 + (half ^ 64) + col;
      vmw0<7>();
      STAMP(26);
      {
        v4u f[14];
#pragma unroll
        for (int i = 0; i < 7; ++i) { f[2 * i] = trd2(ck0 + i * 2048, ck0 + i * 2048 + 512); f[2 * i + 1] = trd2(cv0 + i * 2048, cv0 + i * 2048 + 512); }
        SB();
        sfor<0, 7>([&](auto I) { constexpr int i = I;
          ck = mfma(ones, f[2 * i], ck); SB(); w_issue<2 * i, 2 * i + 1, 896u>(rwo, wov, wos, wol); SB();
          cv = mfma(ones, f[2 * i + 1], cv); SB(); w_issue<2 * i + 1, 2 * i + 2, 896u>(rwo, wov, wos, wol); SB(); });
        w_issue<14, 15, 896u>(rwo, wov, wos, wol);
      }
      tc2 = touch_w2a(); tc3 = touch_w1a();
      STAMP(27);
      touch_done(tcc);
      asm volatile("" : "+v"(e0));
      tag = (((u64)(u32)__builtin_amdgcn_readfirstlane(e0.y) << 32) | (u64)(u32)__builtin_amdgcn_readfirstlane(e0.x)) + 1ull;
      *(LAS float*)(L + L_CSP + w * 384 + 128 + lane * 4) = h ? cv[0] : ck[0];
      bar();
      {
        lptr c28 = L + (112 * w + lane) * 128 + (((lane >> 1) & 1) ^ 1) * 64 + shift + 56;
        *(LAS unsigned short*)c28 = 0x3F80;
        if (lane < 48) *(LAS unsigned short*)(c28 + 64 * 128) = 0x3F80;
        float a0 = 0.f;
#pragma unroll
        for (int v = 0; v < 4; ++v) a0 += *(LAS float*)(L + L_CSP + v * 384 + 128 + lane * 4);
        *(LAS float*)(L + L_CSF + w * 512 + 128 + lane * 4) = a0;
      }
      bar();
      kv_head();
      STAMP(1);
      SB();
      sfor<0, 28>([&](auto I) { kv_step(I, []() {}); });
      asm volatile("" : "+a"(kt), "+a"(vv));
    } else {
      kv_head();
      const Poll pq = wait_flags(p.ws, flag_off(0, 4, (2 * T + (w >> 1)) * 8 + (lane < 56 ? lane >> 3 : 6)), tag);
      STAMP(1);
      const u32 xb = WS_XF + (u32)(tb * 28) * 1024u;
      const u32 sqv = (u32)(7 * h) * 128u + r * 4, sqs = WS_SQ + (u32)tb * 1792u;
      u32 sp[7];
      sfor<0, LA1>([&](auto I) { constexpr int k = I; X[k] = lda<k>(rws, lane * 16, xb); });
      sfor<0, 28>([&](auto I) {
        constexpr int k = I;
        vmw<(k + LA1 <= 35) ? LA1 - 1 : 34 - k>(X[k]);
        if constexpr (k == 0) poll_done(pq);
        kv_step(I, [&]() {
          if constexpr (k + LA1 < 28) X[k + LA1] = lda<k + LA1>(rws, lane * 16, xb);
          else if constexpr (k + LA1 < 35) sp[k + LA1 - 28] = lda1<(k + LA1 - 28) * 128>(rws, sqv, sqs);
        });
      });
      asm volatile("s_waitcnt vmcnt(0)" : "+v"(sp[0]), "+v"(sp[1]), "+v"(sp[2]), "+v"(sp[3]), "+v"(sp[4]), "+v"(sp[5]), "+v"(sp[6]));
      if (TOUCH & 2) { tc2 = touch_w2a(); tc3 = touch_w1a(); }
      asm volatile("" : "+a"(kt), "+a"(vv));
      ssq = ((asf(sp[0]) + asf(sp[1])) + (asf(sp[2]) + asf(sp[3]))) + ((asf(sp[4]) + asf(sp[5])) + asf(sp[6]));
    }
    STAMP(18);
    float mean, rstd;
    {
      const float sum = half_add(h ? kt[12] : 0.f);
      const float sq = half_add(ssq);
      mean = sum * (1.f / 448.f);
      const float var = fmaxf(sq * (1.f / 448.f) - mean * mean, 0.f);
      rstd = __builtin_amdgcn_rsqf(var + EPS);
    }
    const float mr = mean * rstd;
    *(LAS float*)(L + L_MR + w * 256 + h * 128 + r * 4) = h ? mr : rstd;
    const u32 kvbase = WS_KV + l * KV_L + (u32)head * (64u * 4096u);
    lptr kvl = L + L_INBOX + lane * 16;
    v4u K0, K1, V0, V1;
    {
      u32 o[8];
#pragma unroll
      for (int i = 0; i < 4; ++i) {
        const f32x4 cs = *(LAS f32x4*)(L + L_CSF + w * 512 + 128 + (8 * i + 4 * h) * 4);
        o[2 * i] = pk(kt[4 * i] * rstd - mr * cs[0], kt[4 * i + 1] * rstd - mr * cs[1]);
        o[2 * i + 1] = pk(kt[4 * i + 2] * rstd - mr * cs[2], kt[4 * i + 3] * rstd - mr * cs[3]);
      }
      if (h) { o[6] = 0u; o[7] = 0u; }
      K0 = v4u{o[0], o[1], o[2], o[3]}; K1 = v4u{o[4], o[5], o[6], o[7]};
      st_sc1(K0, rws, lane * 16, kvbase + tb * 4096);
      st_sc1(K1, rws, lane * 16 + 1024, kvbase + tb * 4096);
      const float csv = *(LAS float*)(L + L_CSF + w * 512 + 256 + r * 4);
#pragma unroll
      for (int i = 0; i < 4; ++i) {
        const f32x4 rr = *(LAS f32x4*)(L + L_MR + w * 256 + (8 * i + 4 * h) * 4);
        const f32x4 mm = *(LAS f32x4*)(L + L_MR + w * 256 + 128 + (8 * i + 4 * h) * 4);
        o[2 * i] = pk(vv[4 * i] * rr[0] - mm[0] * csv, vv[4 * i + 1] * rr[1] - mm[1] * csv);
        o[2 * i + 1] = pk(vv[4 * i + 2] * rr[2] - mm[2] * csv, vv[4 * i + 3] * rr[3] - mm[3] * csv);
      }
      if (r >= 28) {
#pragma unroll
        for (int i = 0; i < 8; ++i) o[i] = 0x3F803F80u;
      }
      V0 = v4u{o[0], o[1], o[2], o[3]}; V1 = v4u{o[4], o[5], o[6], o[7]};
      st_sc1(V0, rws, lane * 16 + 2048, kvbase + tb * 4096);
      st_sc1(V1, rws, lane * 16 + 3072, kvbase + tb * 4096);
    }
    STAMP(19);
    if (l == 0) {
      vmw0<6>();
      lptr t0 = L + L_WO + w * 15360 + (4 * h + tq) * 128 + (((4 * wa + cpart) ^ sw) * 16) + (tp & 1) * 8;
      lptr t1 = L + L_WO + w * 15360 + (4 * h + tq) * 128 + (((4 * (wa ^ 1) + cpart) ^ sw) * 16) + (tp & 1) * 8;
      lptr cq0 = L + L_WQ + (112 * w + 8 * h + tq) * 64 + col;
      v4u f[7];
#pragma unroll
      for (int i = 0; i < 7; ++i) f[i] = trd2(cq0 + i * 1024, cq0 + i * 1024 + 256);
#pragma unroll
      for (int k = 0; k < 8; ++k) {
        const int ro = (28 * (k >> 1) + 16 * (k & 1)) * 128;
        wf[k][0] = trd2(t0 + ro, t0 + ro + 1024); wf[k][1] = trd2(t1 + ro, t1 + ro + 1024);
      }
      SB();
      f32x16 cq;
#pragma unroll
      for (int i = 0; i < 16; ++i) cq[i] = 0.f;
#pragma unroll
      for (int i = 0; i < 7; ++i) cq = mfma(ones, f[i], cq);
      *(LAS float*)(L + L_CSP + w * 384 + r * 4) = cq[0];
      st_sc1(v4u{0u, 0u, 0u, 0u}, rws, head < 14 ? WS_STATS + h * STATS_L + (u32)((tb * 14 + head) * 512 + r * 16) : 0x40000000u, 0);
    }
    publish(p.ws, flag_off(l, 0, T * 16 + head), tag);
    touch_done(tc2); touch_done(tc3);
    STAMP(2);
#pragma unroll
    for (int k = 0; k < 2 * PD; ++k) wq[k] = WFQ(k);
    {
      lptr own = kvl + (8 + w) * 4096;
      *(LAS v4u*)own = K0; *(LAS v4u*)(own + 1024) = K1; *(LAS v4u*)(own + 2048) = V0; *(LAS v4u*)(own + 3072) = V1;
    }
    if (l == 0) {
      float a0 = 0.f;
#pragma unroll
      for (int v = 0; v < 4; ++v) a0 += *(LAS float*)(L + L_CSP + v * 384 + r * 4);
      *(LAS float*)(L + L_CSF + w * 512 + r * 4) = a0;
      const u32 wib = WS_WI + (u32)head * 86016u + (u32)tb * 1024u;
      st_sc1(*(LAS v4u*)(L + tb * 1024 + lane * 16), rws, lane * 16, wib);
      st_sc1(*(LAS v4u*)(L + (tb < 20 ? tb + 64 : tb) * 1024 + lane * 16), rws, lane * 16, tb < 20 ? wib + 65536u : 0x40000000u);
      const u32 xo = (u32)((2 * st + wb) * 32 + r) * 896u + (u32)(dt * 64 + wa * 32 + 4 * h) * 2u;
      const rsrc_t rx = mkrs(p.x, S_ * D_ * 2);
#pragma unroll
      for (int i = 0; i < 4; ++i) {
        const v2u v = __builtin_amdgcn_raw_buffer_load_b64(rx, xo + i * 16, 0, 0);
        xres[2 * i] = v.x; xres[2 * i + 1] = v.y;
      }
    }
    {
      const int tsrc = T - 2 + (w >> 1);
      const u32 f1 = opqv(flag_off(l, 0, (tsrc < 0 ? T : tsrc) * 16 + head));
      Poll pq;
      f32x16 qt;
#pragma unroll
      for (int i = 0; i < 16; ++i) qt[i] = 0.f;
      SB();
      sfor<0, 28>([&](auto I) {
        constexpr int k = I;
        qt = mfma(wq[k], X[k], qt);
        SB();
        if constexpr (k + 2 * PD < 28) wq[k + 2 * PD] = WFQ(k + 2 * PD);
        if constexpr (k == QP0) pq.a = poll_one(p.ws, f1);
        if constexpr (k == QP0 + 6) pq.b = poll_one(p.ws, f1);
        if constexpr (k == QP0 + 12) pq.c = poll_one(p.ws, f1);
        SB();
      });
      asm volatile("" : "+a"(qt));
      poll_finish(pq, p.ws, f1, tag);
      STAMP(3);
      const u32 so = tsrc >= 0 ? kvbase + (u32)(4 * T - 8 + 2 * w) * 4096u : 0x40000000u;
      v4u t[8];
      t[0] = lda<0>(rws, lane * 16, so); t[1] = lda<1>(rws, lane * 16, so); t[2] = lda<4>(rws, lane * 16, so); t[3] = lda<5>(rws, lane * 16, so);
      t[4] = lda<2>(rws, lane * 16, so); t[5] = lda<3>(rws, lane * 16, so); t[6] = lda<6>(rws, lane * 16, so); t[7] = lda<7>(rws, lane * 16, so);
      SB();
      v4u Q0, Q1;
      {
        const float sc = 0.18898223650461362f * 1.4426950408889634f;
        const float rs = rstd * sc, ms = mr * sc;
        u32 o[8];
#pragma unroll
        for (int i = 0; i < 4; ++i) {
          const f32x4 cs = *(LAS f32x4*)(L + L_CSF + w * 512 + (8 * i + 4 * h) * 4);
          o[2 * i] = pk(qt[4 * i] * rs - ms * cs[0], qt[4 * i + 1] * rs - ms * cs[1]);
          o[2 * i + 1] = pk(qt[4 * i + 2] * rs - ms * cs[2], qt[4 * i + 3] * rs - ms * cs[3]);
        }
        if (h) { o[6] = 0x00003F80u; o[7] = 0u; }
        Q0 = v4u{o[0], o[1], o[2], o[3]}; Q1 = v4u{o[4], o[5], o[6], o[7]};
      }
      f32x16 s8 = qk_own(K0, K1, Q0, Q1);
      u32 em[16], pm[8];
      f32x16 c0;
#pragma unroll
      for (int i = 0; i < 16; ++i) { const int key = (i & 3) + 8 * (i >> 2) + 4 * h; em[i] = key > r ? 0xffffffffu : 0u; c0[i] = asf(~em[i] & 0xF149F2CAu); }
#pragma unroll
      for (int i = 0; i < 8; ++i) pm[i] = (em[2 * i] & 0x0000ffffu) | (em[2 * i + 1] & 0xffff0000u);
#pragma unroll
      for (int i = 0; i < 16; ++i) asm volatile("" : "+v"(em[i]));
#pragma unroll
      for (int i = 0; i < 8; ++i) asm volatile("" : "+v"(pm[i]));
      tie_far(s8);
      float m4[4];
#pragma unroll
      for (int i = 0; i < 16; ++i) s8[i] = asf((em[i] & 0xF149F2CAu) | (~em[i] & asu(s8[i])));
#pragma unroll
      for (int i = 0; i < 4; ++i) m4[i] = fmaxf(fmaxf(s8[4 * i], s8[4 * i + 1]), fmaxf(s8[4 * i + 2], s8[4 * i + 3]));
      asm volatile("" : "+v"(c0), "+v"(m4[0]), "+v"(m4[1]), "+v"(m4[2]), "+v"(m4[3]));
      SB();
      {
        lptr dst = kvl + w * 8192;
        vmw<4>(t[0], t[1], t[2], t[3]);
        poll_done(pq);
        if (tsrc < 0 && h) { t[1][2] = 0xF149u; t[3][2] = 0xF149u; }
#pragma unroll
        for (int i = 0; i < 4; ++i) *(LAS v4u*)(dst + (i & 1) * 1024 + (i >> 1) * 4096) = t[i];
        vmw<0>(t[4], t[5], t[6], t[7]);
#pragma unroll
        for (int i = 0; i < 4; ++i) *(LAS v4u*)(dst + 2048 + (i & 1) * 1024 + (i >> 1) * 4096) = t[4 + i];
      }
      bar();
      tc4 = touch_w1b(0);
      STAMP(17);
      lptr kq = kvl + w * 4096;
      v4u Kr[8][2], Vr[8][2];
#pragma unroll
      for (int j = 0; j < 4; ++j) { Kr[j][0] = *(LAS v4u*)(kq + j * 4096); Kr[j][1] = *(LAS v4u*)(kq + j * 4096 + 1024); }
      SB();
      {
        f32x16 s[8];
        auto mx = [&](const f32x16& t, int half) {
#pragma unroll
          for (int i = 0; i < 4; ++i) m4[i] = fmaxf(fmaxf(m4[i], t[8 * half + 2 * i]), t[8 * half + 2 * i + 1]);
        };
        sfor<0, 8>([&](auto I) {
          constexpr int n = I;
          if constexpr (n == 0) s[0] = qk1(Kr[0][0], Q0, c0); else s[n] = qk1z(Kr[n][0], Q0);
          SB();
          if constexpr (n < 4) Kr[n + 4][0] = *(LAS v4u*)(kq + (n + 4) * 4096);
          Vr[n][0] = *(LAS v4u*)(kq + n * 4096 + 2048);
          if constexpr (n >= 2) mx(s[n - 2], 1);
          SB();
          qk2(s[n], Kr[n][1], Q1);
          SB();
          if constexpr (n < 4) Kr[n + 4][1] = *(LAS v4u*)(kq + (n + 4) * 4096 + 1024);
          Vr[n][1] = *(LAS v4u*)(kq + n * 4096 + 3072);
          if constexpr (n >= 1) { tie(s[n - 1]); mx(s[n - 1], 0); }
          SB();
        });
        mx(s[6], 1);
        SB();
        asm volatile("s_nop 7" : "+v"(s[7]));
        mx(s[7], 0); mx(s[7], 1);
      }
      const float nm = -half_max(fmaxf(fmaxf(m4[0], m4[1]), fmaxf(m4[2], m4[3])));
      f32x16 cm, o;
#pragma unroll
      for (int i = 0; i < 16; ++i) { cm[i] = nm; o[i] = 0.f; }
      {
        f32x16 s[9];
        u32 pp[9][8];
        float e[16];
        asm volatile("" : "+v"(cm));
        SB(); nop2();
        s[1] = qk1(Kr[1][0], Q0, cm); qk2(s[1], Kr[1][1], Q1);
        s8 = qk_own_c(K0, K1, Q0, Q1, cm);
        SB();
        sfor<1, 9>([&](auto I) {
          constexpr int c = I, nx = (c + 1) & 7;
          tie(s[c]);
          if constexpr (c == 8) {
            tie(s8);
#pragma unroll
            for (int i = 0; i < 16; ++i) s[8][i] = asf((asu(s[8][i]) & em[i]) | (asu(s8[i]) & ~em[i]));
          }
          auto ex = [&](int q) {
#pragma unroll
            for (int i = 0; i < 4; ++i) e[4 * q + i] = __builtin_amdgcn_exp2f(s[c][4 * q + i]);
          };
          auto cv = [&](int q) { pp[c][2 * q] = pk(e[4 * q], e[4 * q + 1]); pp[c][2 * q + 1] = pk(e[4 * q + 2], e[4 * q + 3]); };
          if constexpr (c < 8) s[c + 1] = qk1(Kr[nx][0], Q0, cm);
          SB(); if constexpr (c < 7) w_issue2<4 * c - 4, 4 * c - 3, 4096u, 28>(W2_ARGS); if constexpr (c == TB1) tc5 = touch_w1b(1); ex(0); SB();
          if constexpr (c < 8) qk2(s[c + 1], Kr[nx][1], Q1);
          SB(); if constexpr (c < 7) w_issue2<4 * c - 3, 4 * c - 2, 4096u, 28>(W2_ARGS); ex(1); cv(0); SB();
          if constexpr (c > 1) o = mfma(Vr[c - 1][1], v4u{pp[c - 1][4], pp[c - 1][5], pp[c - 1][6], pp[c - 1][7]}, o);
          SB(); if constexpr (c < 7) w_issue2<4 * c - 2, 4 * c - 1, 4096u, 28>(W2_ARGS); ex(2); cv(1); SB();
          if constexpr (c < 8) o = mfma(Vr[c][0], v4u{pp[c][0], pp[c][1], pp[c][2], pp[c][3]}, o);
          else {
            o = mfma(Vr[0][0], v4u{pp[8][0] & pm[0], pp[8][1] & pm[1], pp[8][2] & pm[2], pp[8][3] & pm[3]}, o);
            o = mfma(V0, v4u{pp[8][0] & ~pm[0], pp[8][1] & ~pm[1], pp[8][2] & ~pm[2], pp[8][3] & ~pm[3]}, o);
          }
          SB(); if constexpr (c < 7) w_issue2<4 * c - 1, 4 * c, 4096u, 28>(W2_ARGS); ex(3); cv(2); cv(3); SB();
        });
        o = mfma(Vr[0][1], v4u{pp[8][4] & pm[4], pp[8][5] & pm[5], pp[8][6] & pm[6], pp[8][7] & pm[7]}, o);
        o = mfma(V1, v4u{pp[8][4] & ~pm[4], pp[8][5] & ~pm[5], pp[8][6] & ~pm[6], pp[8][7] & ~pm[7]}, o);
      }
      const float inv = __builtin_amdgcn_rcpf(half_add(h ? o[12] : 0.f));
      u32 oo[8];
#pragma unroll
      for (int i = 0; i < 8; ++i) oo[i] = pk(o[2 * i] * inv, o[2 * i + 1] * inv);
      if (h) { oo[6] = 0u; oo[7] = 0u; }
      const u32 ob = WS_OF + l * OF_L + (u32)(tb * 16 + head) * 2048u;
      st_sc1(v4u{oo[0], oo[1], oo[2], oo[3]}, rws, lane * 16, ob);
      st_sc1(v4u{oo[4], oo[5], oo[6], oo[7]}, rws, lane * 16 + 1024, ob);
      publish(p.ws, flag_off(l, 1, T * 16 + head), tag);
      touch_done(tc4); touch_done(tc5);
      STAMP(4);
    }
    }
    v4u bw4[20][2], bw5[16][2];
#define T5_PTRS \
    lptr t0 = L + ringw + (4 * h + tq) * 128 + (((4 * wb + cpart) ^ sw) * 16) + (tp & 1) * 8; \
    lptr t1 = L + ringw + (4 * h + tq) * 128 + (((4 * (wb ^ 1) + cpart) ^ sw) * 16) + (tp & 1) * 8;
#define T4_PTRS \
    lptr n0 = L + ringw + (8 * h + tq) * 128 + (((4 * wa + cpart) ^ sw) * 16) + (tp & 1) * 8; \
    lptr n1 = L + ringw + (8 * h + tq) * 128 + (((4 * (wa ^ 1) + cpart) ^ sw) * 16) + (tp & 1) * 8;
    {
      IDS
      T5_PTRS
      w_take<0, 12, 1024>(t0, t1, bw5);
      lgk0();
    }
    if (p35) {
      IDS
      const u32 f2 = flag_off(l, 1, (st >> 1) * 16 + 4 * w + (lane >> 4));
      Poll pq;
      w_issue2<0, PA0, 3584u, 32>(W1_ARGS); pq.a = poll_one(p.ws, f2);
      w_issue2<PA0, PA1, 3584u, 32>(W1_ARGS); pq.b = poll_one(p.ws, f2);
      w_issue2<PA1, 24, 3584u, 32>(W1_ARGS); pq.c = poll_one(p.ws, f2);
      poll_finish_mixed<24 - PA0 + 2, 24 - PA1 + 1, 0>(pq, p.ws, f2, tag);
      STAMP(5);
      const u32 o0 = WS_OF + l * OF_L + (u32)((2 * st + wb) * 16 + 4 * w) * 2048u;
      const u32 o1 = WS_OF + l * OF_L + (u32)((2 * st + (wb ^ 1)) * 16 + 4 * w) * 2048u;
      v4u of[8][2];
      sfor<0, 6>([&](auto I) { constexpr int k = I; of[k][0] = lda<k>(rws, lane * 16, o0); of[k][1] = lda<k>(rws, lane * 16, o1); });
      f32x16 acc[4];
#pragma unroll
      for (int q = 0; q < 4; ++q)
#pragma unroll
        for (int i = 0; i < 16; ++i) acc[q][i] = 0.f;
      sfor<0, 4>([&](auto I) {
        constexpr int k = I;
        vmw<(k <= 2) ? 10 : 14 - 2 * k>(of[k][0], of[k][1]);
        if constexpr (k == 0) poll_done(pq);
        acc[1] = mfma(wf[k][0], of[k][1], acc[1]);
        SB();
        if constexpr (k < 2) of[k + 6][0] = lda<k + 6>(rws, lane * 16, o0);
        SB();
        acc[2] = mfma(wf[k][1], of[k][0], acc[2]);
        acc[3] = mfma(wf[k][1], of[k][1], acc[3]);
        SB();
        if constexpr (k < 2) of[k + 6][1] = lda<k + 6>(rws, lane * 16, o1);
        SB();
        acc[0] = mfma(wf[k][0], of[k][0], acc[0]);
        SB();
      });
      red_tail<4>(L, acc, w, lane,
                  [&](auto Q, auto Zi) { constexpr int q = Q, k = 4 + Zi;
                    if constexpr (q == 1) vmw<14 - 2 * k>(of[k][0], of[k][1]);
                    acc[q] = mfma(wf[k][q >> 1], of[k][q & 1], acc[q]); },
                  [&](auto) {});
      STAMP(20);
      const f32x16 y = red_get(L, acc[0], w, lane);
      STAMP(21);
      float xv[16], s1 = 0.f, s2 = 0.f;
#pragma unroll
      for (int i = 0; i < 8; ++i) {
        xres[i] = pk(blo(xres[i]) + y[2 * i], bhi(xres[i]) + y[2 * i + 1]);
        xv[2 * i] = blo(xres[i]); xv[2 * i + 1] = bhi(xres[i]);
        s1 += xv[2 * i] + xv[2 * i + 1];
        s2 += xv[2 * i] * xv[2 * i] + xv[2 * i + 1] * xv[2 * i + 1];
      }
      s1 = half_add(s1); s2 = half_add(s2);
      const u32 tg = (u32)tag | 1u;
      const u32 sb = WS_STATS + l * STATS_L + (u32)((2 * st + wb) * 14) * 512u;
      if (h == 0) st_sc1(v4u{asu(s1), tg, asu(s2), tg}, rws, r * 16, sb + (u32)(2 * dt + wa) * 512u);
      STAMP(6);
      {
        T4_PTRS
        w_take<0, NB, 512>(n0, n1, bw4);
        lgk0();
      }
      float t1s, t2s;
      {
        v4u gr[7];
        const u32 gv = (u32)(7 * h) * 512u + r * 16;
        w_issue2<24, 24 + PB0, 3584u, 32>(W1_ARGS);
        sfor<0, 7>([&](auto I) { constexpr int i = I; gr[i] = ldg<i * 512>(rws, gv, sb); });
        w_issue2<24 + PB0, 24 + 2 * NB, 3584u, 32>(W1_ARGS);
        vmw<2 * NB - PB0>(gr[0], gr[1], gr[2], gr[3]); vmw<2 * NB - PB0>(gr[4], gr[5]); vmw<2 * NB - PB0>(gr[6]);
        for (u32 spins = 0;;) {
          bool ok = true; t1s = 0.f; t2s = 0.f;
#pragma unroll
          for (int i = 0; i < 7; ++i) { ok &= (gr[i].y == tg) & (gr[i].w == tg); t1s += asf(gr[i].x); t2s += asf(gr[i].z); }
          if (__all(ok)) break;
          if (spin_out(p.ws, spins, (u32)tag)) break;
          asm volatile("" ::: "memory");
#pragma unroll
          for (int i = 0; i < 7; ++i) gr[i] = ld_sc1(rws, gv + i * 512, sb);
        }
      }
      STAMP(7);
      t1s = half_add(t1s); t2s = half_add(t2s);
      const float m1 = t1s * (1.f / 448.f);
      const float var = fmaxf(t2s * (1.f / 448.f) - m1 * m1, 0.f);
      const float r1 = __builtin_amdgcn_rsqf(var + EPS);
      const float r2 = __builtin_amdgcn_rsqf(var * r1 * r1 + EPS);
      const float rr = r1 * r2, mm = m1 * rr;
      lptr tw = L + L_TR + w * 2304;
#pragma unroll
      for (int i = 0; i < 4; ++i)
        *(LAS v2u*)(tw + r * 72 + i * 16 + h * 8) = v2u{pk(xv[4 * i] * rr - mm, xv[4 * i + 1] * rr - mm), pk(xv[4 * i + 2] * rr - mm, xv[4 * i + 3] * rr - mm)};
      lgk0();
      lptr tr = tw + (8 * h + tq) * 72 + ch16 * 32 + tp * 8;
      const u32 hb = WS_HF + l * HF_L + (u32)((2 * dt + wa) * 128 + (2 * st + wb) * 2) * 1024u;
      st_sc1(trd2(tr, tr + 4 * 72), rws, lane * 16, hb);
      st_sc1(trd2(tr + 16 * 72, tr + 20 * 72), rws, lane * 16 + 1024, hb);
      publish(p.ws, flag_off(l, 2, dt * 32 + st), tag);
      STAMP(8);
    } else {
      IDS
      T4_PTRS
      w_issue2<0, 24, 3584u, 32>(W1_ARGS);
      drain();
      w_take<0, NB, 512>(n0, n1, bw4);
      lgk0();
      w_issue2<24, 24 + 2 * NB, 3584u, 32>(W1_ARGS);
      drain();
    }
    if (p4) {
      IDS
      T4_PTRS
      v2u bias[4];
      {
        const rsrc_t rb1 = mkrs(p.b1, F_ * 2);
        const u32 bo = (u32)(ft * 64 + wa * 32 + 4 * h) * 2u;
        sfor<0, 4>([&](auto I) { constexpr int i = I; bias[i] = ldp2<16 * i>(rb1, bo); });
      }
      w_take<NB, 20, 512>(n0, n1, bw4);
      lgk0();
      const u32 f3 = flag_off(l, 2, dt4 * 32 + 8 * w + (lane >> 3));
      Poll pq;
      w_issue2<24 + 2 * NB, 64 - PC0, 3584u, 32>(W1_ARGS); pq.a = poll_one(p.ws, f3);
      w_issue2<64 - PC0, 64 - PC1, 3584u, 32>(W1_ARGS); pq.b = poll_one(p.ws, f3);
      w_issue2<64 - PC1, 64, 3584u, 32>(W1_ARGS); pq.c = poll_one(p.ws, f3);
      STAMP(9);
      poll_finish_mixed<PC0 + 2, PC1 + 1, 0>(pq, p.ws, f3, tag);
      STAMP(10);
      const u32 hb = WS_HF + l * HF_L + (u32)(32 * w) * 1024u;
      const f32x16 y = gemm_main<32, 20, 512, true, LA4>(L, w, lane, rws, hb + (u32)(2 * dt4 + wb) * 131072u, hb + (u32)(2 * dt4 + (wb ^ 1)) * 131072u, n0, n1, bw4,
                                                         [&](auto I) { constexpr int i = I; w_issue2<24 + i, 25 + i, 4096u, 28>(W2_ARGS); });
      poll_done(pq);
      asm volatile("" : "+v"(bias[0]), "+v"(bias[1]), "+v"(bias[2]), "+v"(bias[3]));
      STAMP(11);
      STAMP(22);
      u32 o[8];
#pragma unroll
      for (int i = 0; i < 8; ++i) {
        const u32 bb = (i & 1) ? bias[i >> 1].y : bias[i >> 1].x;
        float gv[2];
#pragma unroll
        for (int e = 0; e < 2; ++e) {
          const float u = y[2 * i + e] + (e ? bhi(bb) : blo(bb));
          const float z = u * (-2.302208198f - 0.1029432397f * u * u);
          gv[e] = u * __builtin_amdgcn_rcpf(1.f + __builtin_amdgcn_exp2f(z));
        }
        o[i] = pk(gv[0], gv[1]);
      }
      const u32 ub = WS_UF + l * UF_L + (u32)((2 * dt4 + wb) * 112 + 4 * ft + 2 * wa) * 1024u;
      st_sc1(v4u{o[0], o[1], o[2], o[3]}, rws, lane * 16, ub);
      st_sc1(v4u{o[4], o[5], o[6], o[7]}, rws, lane * 16 + 1024, ub);
      STAMP(23);
      publish(p.ws, flag_off(l, 3, dt4 * 32 + ft), tag);
      STAMP(12);
    } else {
      IDS
      {
        const rsrc_t rwf = mkrs(p.w2, F_ * S_ * 2);
        const int nb = (g & 1) ? 4 : 11, ib = bi - ((g & 1) ? 28 : 21);
        for (int row = ib * 4 + w; row < F_; row += nb * 4 * 8) {
          sfor<0, 8>([&](auto I) { constexpr int i = I; dma16<0, i * 1024>(rwf, lane * 16 + pj * 1024, (u32)(row + i * nb * 4) * 4096u, lds0 + ringw); });
          asm volatile("s_waitcnt vmcnt(16)" ::: "memory");
        }
        drain();
      }
      w_issue2<24, 24 + 2 * LA4, 4096u, 28>(W2_ARGS);
      drain();
    }
    if (p35) {
      IDS
      T5_PTRS
      u32 bias;
      asm volatile("s_nop 4\n\tbuffer_load_ushort %0, %1, %2, 0 offen" : "=v"(bias) : "v"((u32)((2 * st + wb) * 32 + r) * 2u), "s"(mkrs(p.b2, S_ * 2)));
      w_take<12, 16, 1024>(t0, t1, bw5);
      lgk0();
      const u32 f4 = flag_off(l, 3, dt * 32 + 7 * w + (lane < 56 ? lane >> 3 : 6));
      Poll pq;
      w_issue2<24 + 2 * LA4, 56 - PD0, 4096u, 28>(W2_ARGS); pq.a = poll_one(p.ws, f4);
      w_issue2<56 - PD0, 56 - PD1, 4096u, 28>(W2_ARGS); pq.b = poll_one(p.ws, f4);
      w_issue2<56 - PD1, 56, 4096u, 28>(W2_ARGS); pq.c = poll_one(p.ws, f4);
      STAMP(13);
      poll_finish_mixed<PD0 + 2, PD1 + 1, 0>(pq, p.ws, f4, tag);
      asm volatile("" : "+v"(bias));
      STAMP(14);
      const u32 wis = l == 0 ? WS_WI + (u32)head * 86016u + (u32)w * 21504u : 0x40000000u;
      v4u wi[21];
      const u32 ub = WS_UF + l * UF_L + (u32)(28 * w) * 1024u;
      const f32x16 y = gemm_main<28, 16, 1024, false, LA5>(L, w, lane, rws, ub + (u32)(2 * dt + wa) * 114688u, ub + (u32)(2 * dt + (wa ^ 1)) * 114688u, t0, t1, bw5,
                                                           [&](auto I) { constexpr int i = I; wi[i] = lda<i>(rws, lane * 16, wis); });
      poll_done(pq);
      STAMP(15);
      const float bv = blo(bias);
#pragma unroll
      for (int i = 0; i < 8; ++i) xres[i] = pk(blo(xres[i]) + (y[2 * i] + bv), bhi(xres[i]) + (y[2 * i + 1] + bv));
      v4u xo[2];
#pragma unroll
      for (int s = 0; s < 2; ++s) {
        auto a = __builtin_amdgcn_permlane32_swap(xres[4 * s], xres[4 * s + 2], false, false);
        auto c = __builtin_amdgcn_permlane32_swap(xres[4 * s + 1], xres[4 * s + 3], false, false);
        xo[s] = v4u{a[0], c[0], a[1], c[1]};
      }
      if (l == 0) {
        const u32 xb = WS_XF + (u32)((2 * st + wb) * 28 + 4 * dt) * 1024u + wa * 512;
        st_sc1(xo[0], rws, h * 1024 + r * 16, xb);
        st_sc1(xo[1], rws, h * 1024 + r * 16 + 2048, xb);
        float q0 = 0.f, q1 = 0.f;
#pragma unroll
        for (int i = 0; i < 8; i += 2) { q0 = __builtin_amdgcn_fdot2_f32_bf16(asbf2(xres[i]), asbf2(xres[i]), q0, false); q1 = __builtin_amdgcn_fdot2_f32_bf16(asbf2(xres[i + 1]), asbf2(xres[i + 1]), q1, false); }
        const float q = half_add(q0 + q1);
        if (h == 0) __builtin_amdgcn_raw_buffer_store_b32(asu(q), rws, r * 4, WS_SQ + (u32)((2 * st + wb) * 14 + 2 * dt + wa) * 128u, 16);
        publish(p.ws, flag_off(0, 4, st * 8 + dt), tag);
        sfor<2 * LA5, 21>([&](auto I) { constexpr int i = I; wi[i] = lda<i>(rws, lane * 16, wis); });
        sfor<0, 21>([&](auto I) { constexpr int i = I; vmw<(i < 2 * LA5) ? 21 - 2 * LA5 : 20 - i>(wi[i]); *(LAS v4u*)(L + w * 21504 + i * 1024 + lane * 16) = wi[i]; });
        bar();
      } else {
        sfor<0, 2 * LA5>([&](auto I) { constexpr int i = I; vmw<0>(wi[i]); });
        const rsrc_t rout = mkrs(p.out, S_ * D_ * 2);
        const u32 oo = (u32)((2 * st + wb) * 32 + r) * 896u + (u32)(dt * 64 + wa * 32 + 8 * h) * 2u;
        __builtin_amdgcn_raw_buffer_store_b128(xo[0], rout, oo, 0, 0);
        __builtin_amdgcn_raw_buffer_store_b128(xo[1], rout, oo + 32, 0, 0);
        if (b == 0 && tid == 0) __hip_atomic_store((GAS u64*)(p.ws + WS_EPOCH), tag, RLX_AGENT);
      }
      STAMP(16);
    } else if (l == 0) {
      IDS
      const Poll pq = wait_flags(p.ws, flag_off(0, 1, (lane >> 2) * 16 + head), tag);
      const u32 wis = WS_WI + (u32)head * 86016u + (u32)w * 21504u;
      v4u wi[21];
      bar();
      sfor<0, 21>([&](auto I) { constexpr int i = I; wi[i] = lda<i>(rws, lane * 16, wis); });
      sfor<0, 21>([&](auto I) { constexpr int i = I; vmw<20 - i>(wi[i]); *(LAS v4u*)(L + w * 21504 + i * 1024 + lane * 16) = wi[i]; });
      poll_done(pq);
      bar();
    }
  }
  asm volatile(".Lfwd_last:");
}

extern "C" void kernel_launch(void* const* d_in, const int* in_sizes, int n_in, void* d_out, int out_size, void* d_ws, size_t ws_size,
                              hipStream_t stream) {
  Params p;
  p.x = d_in[0]; p.wqkv = d_in[1]; p.wo = d_in[2]; p.w1 = d_in[4]; p.b1 = d_in[5]; p.w2 = d_in[6]; p.b2 = d_in[7];
  p.out = d_out; p.ws = (unsigned char*)d_ws;
  (void)hipFuncSetAttribute((const void*)fused_fwd, hipFuncAttributeMaxDynamicSharedMemorySize, L_END);
  hipLaunchKernelGGL(fused_fwd, dim3(256), dim3(256), L_END, stream, p);
}
```

```cpp
#include <hip/hip_runtime.h>
#include <hip/hip_bf16.h>
#include <type_traits>

typedef unsigned u32;
typedef unsigned long long u64;
typedef u32 v4u __attribute__((ext_vector_type(4)));
typedef u32 v2u __attribute__((ext_vector_type(2)));
typedef float f32x16 __attribute__((ext_vector_type(16)));
typedef float f32x4 __attribute__((ext_vector_type(4)));
typedef float f32x2 __attribute__((ext_vector_type(2)));
typedef __bf16 bf16x8 __attribute__((ext_vector_type(8)));
typedef __bf16 bf16x2 __attribute__((ext_vector_type(2)));
typedef short s16x4 __attribute__((ext_vector_type(4)));
#define LAS __attribute__((address_space(3)))
#define GAS __attribute__((address_space(1)))
#define DI __device__ __forceinline__
#define RLX_AGENT __ATOMIC_RELAXED, __HIP_MEMORY_SCOPE_AGENT
#define SB() __builtin_amdgcn_sched_barrier(0)
typedef __amdgpu_buffer_rsrc_t rsrc_t;
typedef LAS unsigned char* lptr;

#define DBG_STAMP 0
#define DBG_WAVE 0
#define POLL_GAP "3"
#define TOUCH 1
#define QP0 12
#define NB 10
#define CODE_TOUCH 1
#define TLBT 0
#define NOW 0
#define PA0 12
#define PA1 18
#define PB0 12
#define PC0 10
#define PC1 5
#define PD0 8
#define PD1 4
#define TB1 3
#define NODMA 0
#define SAFE_SUM 1.0e18f
#define LA4 6
#define LA5 6
#define LA1 10
#define LA3 6
#define ROT4 0
#define ROT5 0
#define ROT1 (head & 1)
#define WARM2 2

constexpr int S_ = 2048, D_ = 448, F_ = 1792;
constexpr u32 WS_EPOCH = 0, WS_TMO = 128;
constexpr u32 WS_FLAGS = 4096;
constexpr u32 WS_STATS = WS_FLAGS + 2 * 5 * 256 * 128;
constexpr u32 STATS_L = 64 * 14 * 512;
constexpr u32 WS_KV = 2u << 20, KV_L = 4u << 20;
constexpr u32 WS_OF = WS_KV + 2 * KV_L, OF_L = 2u << 20;
constexpr u32 WS_HF = WS_OF + 2 * OF_L, HF_L = 2u << 20;
constexpr u32 WS_UF = WS_HF + 2 * HF_L, UF_L = 2u << 20;
constexpr u32 WS_XF = WS_UF + 2 * UF_L;
constexpr u32 WS_SQ = WS_XF + 64 * 28 * 1024;
constexpr u32 WS_WI = WS_XF + (2u << 20);
constexpr u32 WS_END = WS_WI + (2u << 20);
constexpr u32 WS_DBG = WS_END;
static_assert(WS_STATS + 2 * STATS_L <= WS_KV, "ws map");
constexpr u32 L_RING = 0, RING_W = 24576;
constexpr u32 L_INBOX = 98304, INBOX_W = 12288;
constexpr u32 L_WQ = 57344;
constexpr u32 L_WO = 86016;
constexpr u32 L_CSP = 147456;
constexpr u32 L_CSF = L_CSP + 1536;
constexpr u32 L_MR = L_CSF + 2048;
constexpr u32 L_TR = L_MR + 1024;
constexpr u32 L_END = L_TR + 4 * 2304;
constexpr u32 kSpinCap = 1u << 16;
constexpr float EPS = 1e-5f;

struct Params {
  const void* x; const void* wqkv; const void* wo; const void* w1; const void* b1; const void* w2; const void* b2;
  void* out; unsigned char* ws;
};

DI f32x16 mfma(v4u a, v4u b, f32x16 c) {
  return __builtin_amdgcn_mfma_f32_32x32x16_bf16(__builtin_bit_cast(bf16x8, a), __builtin_bit_cast(bf16x8, b), c, 0, 0, 0);
}
DI f32x16 qk1(v4u k0, v4u q0, f32x16 ci) {
  f32x16 c;
  asm volatile("v_mfma_f32_32x32x16_bf16 %0, %1, %2, %3" : "=&v"(c) : "a"(k0), "v"(q0), "v"(ci));
  return c;
}
DI f32x16 qk1z(v4u k0, v4u q0) {
  f32x16 c;
  asm volatile("v_mfma_f32_32x32x16_bf16 %0, %1, %2, 0" : "=&v"(c) : "a"(k0), "v"(q0));
  return c;
}
DI void qk2(f32x16& c, v4u k1, v4u q1) { asm volatile("v_mfma_f32_32x32x16_bf16 %0, %1, %2, %0" : "+v"(c) : "a"(k1), "v"(q1)); }
DI f32x16 qk_own(v4u k0, v4u k1, v4u q0, v4u q1) {
  f32x16 c;
  asm volatile("s_nop 1\n\tv_mfma_f32_32x32x16_bf16 %0, %1, %3, 0\n\tv_mfma_f32_32x32x16_bf16 %0, %2, %4, %0" : "=&v"(c) : "v"(k0), "v"(k1), "v"(q0), "v"(q1));
  return c;
}
DI f32x16 qk_own_c(v4u k0, v4u k1, v4u q0, v4u q1, f32x16 ci) {
  f32x16 c;
  asm volatile("s_nop 1\n\tv_mfma_f32_32x32x16_bf16 %0, %1, %3, %5\n\tv_mfma_f32_32x32x16_bf16 %0, %2, %4, %0" : "=&v"(c) : "v"(k0), "v"(k1), "v"(q0), "v"(q1), "v"(ci));
  return c;
}
DI void tie(f32x16& c) { asm volatile("" : "+v"(c)); }
DI void tie_far(f32x16& c) { asm volatile("s_nop 7\n\ts_nop 4" : "+v"(c)); }
DI void nop2() { asm volatile("s_nop 1"); }
DI u32 pk(float lo, float hi) { f32x2 v = {lo, hi}; return __builtin_bit_cast(u32, __builtin_convertvector(v, bf16x2)); }
DI float asf(u32 v) { return __builtin_bit_cast(float, v); }
DI u32 asu(float v) { return __builtin_bit_cast(u32, v); }
DI bf16x2 asbf2(u32 v) { return __builtin_bit_cast(bf16x2, v); }
DI float blo(u32 v) { return __builtin_bit_cast(float, v << 16); }
DI float bhi(u32 v) { return __builtin_bit_cast(float, v & 0xffff0000u); }
DI void bar() { asm volatile("s_waitcnt lgkmcnt(0)\n\ts_barrier" ::: "memory"); }
DI void drain() { asm volatile("s_waitcnt vmcnt(0)" ::: "memory"); }
DI void lgk0() { asm volatile("s_waitcnt lgkmcnt(0)" ::: "memory"); }
template <u32 SI, u32 LI> DI void dma16(rsrc_t rs, u32 voff, u32 soff, u32 lds) {
  u32 t;
  asm volatile("s_add_u32 m0, %1, %5\n\ts_add_u32 %0, %4, %6\n\tbuffer_load_dwordx4 %2, %3, %0 offen lds"
               : "=&s"(t) : "s"(lds), "v"(voff), "s"(rs), "s"(soff), "i"(LI), "i"(SI) : "memory", "scc");
}
DI void dma_fake(rsrc_t rs, u32 voff, u32 lds) {
  asm volatile("s_mov_b32 m0, %0\n\ts_mov_b64 exec, 1\n\tbuffer_load_dword %1, %2, 0 offen lds\n\ts_mov_b64 exec, -1" :: "s"(lds), "v"(voff), "s"(rs) : "memory");
}
template <int K> DI v4u lda(rsrc_t rs, u32 voff, u32 soff) {
  v4u d; u32 t;
  asm volatile("s_add_u32 %1, %4, %5\n\tbuffer_load_dwordx4 %0, %2, %3, %1 offen offset:%6 sc1"
               : "=v"(d), "=&s"(t) : "v"(voff), "s"(rs), "s"(soff), "i"((K >> 2) * 4096), "i"((K & 3) * 1024) : "scc");
  return d;
}
template <int IMM> DI v4u ldp(rsrc_t rs, u32 voff, u32 soff) {
  v4u d;
  asm volatile("buffer_load_dwordx4 %0, %1, %2, %3 offen offset:%4" : "=v"(d) : "v"(voff), "s"(rs), "s"(soff), "i"(IMM));
  return d;
}
template <int IMM> DI v4u ldg(rsrc_t rs, u32 voff, u32 soff) {
  v4u d;
  asm volatile("buffer_load_dwordx4 %0, %1, %2, %3 offen offset:%4 sc1" : "=v"(d) : "v"(voff), "s"(rs), "s"(soff), "i"(IMM));
  return d;
}
template <int IMM> DI v2u ldp2(rsrc_t rs, u32 voff) {
  v2u d;
  asm volatile("buffer_load_dwordx2 %0, %1, %2, 0 offen offset:%3" : "=v"(d) : "v"(voff), "s"(rs), "i"(IMM));
  return d;
}
DI float mul1(float a, float b) { float d; asm volatile("v_mul_f32 %0, %1, %2" : "=v"(d) : "v"(a), "v"(b)); return d; }
DI void pin7(v2u (&v)[7]) { asm volatile("" : "+v"(v[0]), "+v"(v[1]), "+v"(v[2]), "+v"(v[3]), "+v"(v[4]), "+v"(v[5]), "+v"(v[6])); }
DI void pin4(float (&v)[4]) { asm volatile("" : "+v"(v[0]), "+v"(v[1]), "+v"(v[2]), "+v"(v[3])); }
template <int IMM> DI v2u lda2(rsrc_t rs, u32 voff, u32 soff) {
  v2u d;
  asm volatile("buffer_load_dwordx2 %0, %1, %2, %3 offen offset:%4 sc1" : "=v"(d) : "v"(voff), "s"(rs), "s"(soff), "i"(IMM));
  return d;
}
template <int N> DI void vmw0() { asm volatile("s_waitcnt vmcnt(%0)" ::"i"(N) : "memory"); }
template <int N> DI void vmw(v4u& a) { asm volatile("s_waitcnt vmcnt(%1)" : "+v"(a) : "i"(N)); }
template <int N> DI void vmw(v4u& a, v4u& b, v4u& c, v4u& d) { asm volatile("s_waitcnt vmcnt(%4)" : "+v"(a), "+v"(b), "+v"(c), "+v"(d) : "i"(N)); }
template <int N> DI void vmw(v4u& a, v4u& b) { asm volatile("s_waitcnt vmcnt(%2)" : "+v"(a), "+v"(b) : "i"(N)); }
template <int I, int N, class Fn> DI void sfor(Fn&& f) { if constexpr (I < N) { f(std::integral_constant<int, I>{}); sfor<I + 1, N>(f); } }
DI u32 opq(u32 v) { v = __builtin_amdgcn_readfirstlane(v); asm volatile("" : "+s"(v)); return v; }
DI u32 opqv(u32 v) { asm volatile("" : "+v"(v)); return v; }
DI v2u trd(lptr p) { return __builtin_bit_cast(v2u, __builtin_amdgcn_ds_read_tr16_b64_v4i16((LAS s16x4*)p)); }
DI v4u trd2(lptr p0, lptr p1) { v2u a = trd(p0), b = trd(p1); return v4u{a.x, a.y, b.x, b.y}; }
DI v4u ld_sc1(rsrc_t rs, u32 voff, u32 soff) { return __builtin_amdgcn_raw_buffer_load_b128(rs, voff, soff, 16); }
DI void st_sc1(v4u v, rsrc_t rs, u32 voff, u32 soff) { __builtin_amdgcn_raw_buffer_store_b128(v, rs, voff, soff, 16); }
DI rsrc_t mkrs(const void* p, u32 bytes) {
  u32 fl = 0x00020000;
  asm volatile("" : "+s"(p), "+s"(bytes), "+s"(fl));
  return __builtin_amdgcn_make_buffer_rsrc((void*)p, 0, bytes, fl);
}
DI float half_add(float v) {
  auto r = __builtin_amdgcn_permlane32_swap(asu(v), asu(v), false, false);
  const u32 a = r[0], b = r[1];
  return asf(a) + asf(b);
}
DI float half_max(float v) {
  auto r = __builtin_amdgcn_permlane32_swap(asu(v), asu(v), false, false);
  const u32 a = r[0], b = r[1];
  return fmaxf(asf(a), asf(b));
}
DI bool spin_out(unsigned char* ws, u32& spins, u32 tl) {
  ++spins;
  if ((spins & 63u) == 0u) {
    GAS u32* tmo = (GAS u32*)(ws + WS_TMO);
    if (__hip_atomic_load(tmo, RLX_AGENT) == tl) return true;
    if (spins > kSpinCap) { __hip_atomic_store(tmo, tl, RLX_AGENT); return true; }
  }
  return false;
}
struct Poll { v2u a, b, c; };
#define PL(R, O, B) "global_load_dwordx2 " R ", " O ", " B " sc1\n\t"
DI v2u poll_one(unsigned char* ws, u32 off) {
  v2u r;
  asm volatile(PL("%0", "%1", "%2") : "=v"(r) : "v"(off), "s"(ws) : "memory");
  return r;
}
DI void poll_finish(Poll& q, unsigned char* ws, u32 off, u64 tag) {
  u32 left;
#define PC(R) "s_waitcnt vmcnt(2)\n\tv_cmp_ne_u64 vcc, %6, " R "\n\ts_cbranch_vccz 2f\n\t" PL(R, "%4", "%5")
  asm volatile("s_mov_b32 %3, 0x8000\n1:\n\t" PC("%0") PC("%1") PC("%2") "s_sub_u32 %3, %3, 1\n\ts_cbranch_scc0 1b\n2:"
               : "+v"(q.a), "+v"(q.b), "+v"(q.c), "=&s"(left) : "v"(off), "s"(ws), "s"(tag) : "vcc", "scc", "memory");
#undef PC
  if (left == 0xffffffffu) { drain(); __hip_atomic_store((GAS u32*)(ws + WS_TMO), (u32)tag, RLX_AGENT); }
}
template <int CA, int CB, int CC> DI void poll_finish_mixed(Poll& q, unsigned char* ws, u32 off, u64 tag) {
  u32 left;
#define PE(R, N) "s_waitcnt vmcnt(" N ")\n\tv_cmp_ne_u64 vcc, %6, " R "\n\ts_cbranch_vccz 2f\n\t"
#define PC(R) "s_waitcnt vmcnt(2)\n\tv_cmp_ne_u64 vcc, %6, " R "\n\ts_cbranch_vccz 2f\n\t" PL(R, "%4", "%5")
  asm volatile("s_mov_b32 %3, 0x8000\n\t" PE("%0", "%7") PL("%0", "%4", "%5") PE("%1", "%8") PL("%1", "%4", "%5") PE("%2", "%9") PL("%2", "%4", "%5")
               "1:\n\t" PC("%0") PC("%1") PC("%2") "s_sub_u32 %3, %3, 1\n\ts_cbranch_scc0 1b\n2:"
               : "+v"(q.a), "+v"(q.b), "+v"(q.c), "=&s"(left) : "v"(off), "s"(ws), "s"(tag), "i"(CA), "i"(CB + 1), "i"(CC + 2) : "vcc", "scc", "memory");
#undef PE
#undef PC
  if (left == 0xffffffffu) { drain(); __hip_atomic_store((GAS u32*)(ws + WS_TMO), (u32)tag, RLX_AGENT); }
}
DI Poll wait_flags(unsigned char* ws, u32 off, u64 tag) {
  Poll q;
  q.a = poll_one(ws, off); asm volatile("s_sleep " POLL_GAP);
  q.b = poll_one(ws, off); asm volatile("s_sleep " POLL_GAP);
  q.c = poll_one(ws, off);
  poll_finish(q, ws, off, tag);
  return q;
}
DI void poll_done(const Poll& q) { asm volatile("" ::"v"(q.a), "v"(q.b), "v"(q.c)); }
DI u32 touch(rsrc_t rs, u32 voff, u32 soff) {
  u32 d;
  asm volatile("s_nop 4\n\tbuffer_load_dword %0, %1, %2, %3 offen" : "=v"(d) : "v"(voff), "s"(rs), "s"(soff));
  return d;
}
DI void touch_done(u32 d) { asm volatile("" ::"v"(d)); }
DI void publish(unsigned char* ws, u32 off, u64 tag) {
  asm volatile("s_waitcnt vmcnt(0) lgkmcnt(0)\n\ts_barrier" ::: "memory");
  if (threadIdx.x == 0) __hip_atomic_store((GAS u64*)(ws + off), tag, RLX_AGENT);
  asm volatile("" ::: "memory");
}
DI u32 flag_off(int l, int type, int idx) { return WS_FLAGS + (u32)(((l * 5 + type) << 8) + idx) * 128u; }

#define STAMP(i) do {} while (0)

DI void red_put(lptr L, const f32x16& a, int q, int i, int w, int lane) {
  *(LAS f32x4*)(L + L_INBOX + (u32)(w ^ q) * INBOX_W + (q - 1) * 4096 + lane * 16 + i * 1024) = f32x4{a[4 * i], a[4 * i + 1], a[4 * i + 2], a[4 * i + 3]};
}
DI f32x16 red_get(lptr L, const f32x16& own, int w, int lane) {
  bar();
  f32x16 r = own;
  lptr src = L + L_INBOX + (u32)w * INBOX_W + lane * 16;
#pragma unroll
  for (int q = 0; q < 3; ++q)
#pragma unroll
    for (int i = 0; i < 4; ++i) {
      const f32x4 v = *(LAS f32x4*)(src + q * 4096 + i * 1024);
      r[4 * i] += v[0]; r[4 * i + 1] += v[1]; r[4 * i + 2] += v[2]; r[4 * i + 3] += v[3];
    }
  return r;
}
template <int P0, int P1, u32 RS> DI void w_issue(rsrc_t rs, u32 voff, u32 soff, u32 ring) {
  sfor<P0, P1>([&](auto I) { constexpr int i = I; dma16<i * 8 * RS, (i % 24) * 1024>(rs, voff, soff, ring); });
}
template <int P0, int P1, u32 RS, int HP> DI void w_issue2(rsrc_t rs, u32 voff, u32 s0, u32 s1, u32 ring) {
  sfor<P0, P1>([&](auto I) { constexpr int i = I; dma16<(i % HP) * 8 * RS, (i % 24) * 1024>(rs, voff, i < HP ? s0 : s1, ring); });
}
template <int K0, int K1, int TSTEP, int NR> DI void w_take(lptr t0, lptr t1, v4u (&bw)[NR][2]) {
#pragma unroll
  for (int kk = K0; kk < K1; ++kk) {
    const int sl = (kk % 12) * 2048;
    bw[kk][0] = trd2(t0 + sl, t0 + sl + TSTEP);
    bw[kk][1] = trd2(t1 + sl, t1 + sl + TSTEP);
  }
}
template <int NR> DI void w_undef(v4u (&bw)[NR][2], int k0, int k1) {
#pragma unroll
  for (int kk = 0; kk < NR; ++kk) if (kk >= k0 && kk < k1) { asm volatile("" : "=v"(bw[kk][0])); asm volatile("" : "=v"(bw[kk][1])); }
}

template <int NK, int NR, int TSTEP, bool WA, int LS, int Z, bool TAIL, class Tail>
DI f32x16 gemm_main(lptr L, int w, int lane, rsrc_t ws, u32 a0, u32 a1, lptr t0, lptr t1, const v4u (&bw)[NR][2], f32x16 init, const Poll& pq, Tail&& tail) {
  constexpr bool RING = NR < NK;
  constexpr int NS = 2 * NK, LAG = 3;
  static_assert(!RING || (NK - NR == 12 && NS - LS >= 30 && NK - Z >= 14), "ring is 12 slots and must be consumed before the tail");
  const u32 voff = lane * 16;
  v4u a[NS], wl[12][2];
  f32x16 acc[4];
#define KOF(n) (!RING ? (n) : (n) < 2 ? (n) : (n) < 14 ? NR + (n) - 2 : (n) - 12)
#define SN(j) ((j) < NS - 2 * Z ? (j) / 2 : (j) < NS - Z ? (j) - (NK - Z) : (j) - NK)
#define SH(j) ((j) < NS - 2 * Z ? (j) & 1 : (j) < NS - Z ? 1 : 0)
  auto ld = [&](auto J) { constexpr int j = J; a[j] = lda<KOF(SN(j))>(ws, voff, SH(j) ? a1 : a0); };
  sfor<0, LS>(ld);
  acc[0] = init;
#pragma unroll
  for (int q = 1; q < 4; ++q)
#pragma unroll
    for (int i = 0; i < 16; ++i) acc[q][i] = 0.f;
  auto mm = [&](auto J, auto WI) {
    constexpr int j = J, wi = WI, n = SN(j), q = WA ? 2 * wi + SH(j) : 2 * SH(j) + wi;
    v4u wv;
    if constexpr (RING && n >= 2 && n < 14) wv = wl[n - 2][wi]; else wv = bw[KOF(n)][wi];
    acc[q] = WA ? mfma(wv, a[j], acc[q]) : mfma(a[j], wv, acc[q]);
  };
  using I0 = std::integral_constant<int, 0>; using I1 = std::integral_constant<int, 1>;
  constexpr int QE = WA ? 1 : 2, QL = WA ? 2 : 1;
  SB();
  sfor<0, NS>([&](auto J) {
    constexpr int j = J, e = j - (NS - Z);
    asm volatile("" ::: "memory");
    vmw<(TAIL || j + LS <= NS) ? LS - 1 : NS - 1 - j>(a[j]);
    asm volatile("" ::: "memory");
    if constexpr (j == 0) poll_done(pq);
    mm(J, I1{});
    SB();
    if constexpr (j + LS < NS) ld(std::integral_constant<int, j + LS>{}); else if constexpr (TAIL) tail(std::integral_constant<int, j + LS - NS>{});
    if constexpr (e >= 1 && e <= 4 && Z >= 5) red_put(L, acc[3], 3, e - 1, w, lane);
    if constexpr (e >= 1 && e <= 2 && Z < 5) { red_put(L, acc[3], 3, 2 * e - 2, w, lane); red_put(L, acc[3], 3, 2 * e - 1, w, lane); }
    SB();
    if constexpr (j < NS - LAG) mm(J, I0{});
    SB();
    if constexpr (RING && j < 24) { constexpr int sl = ((NR + j / 2) % 12) * 2048; lptr t = (j & 1) ? t1 : t0; wl[j / 2][j & 1] = trd2(t + sl, t + sl + TSTEP); }
    if constexpr (e >= 1 && e <= 4 && Z >= 5) red_put(L, acc[QE], QE, e - 1, w, lane);
    if constexpr (e >= 1 && e <= 2 && Z < 5) { red_put(L, acc[QE], QE, 2 * e - 2, w, lane); red_put(L, acc[QE], QE, 2 * e - 1, w, lane); }
    SB();
  });
  sfor<NS - LAG, NS>([&](auto J) { mm(J, I0{}); });
  SB();
#pragma unroll
  for (int i = 0; i < 4; ++i) red_put(L, acc[QL], QL, i, w, lane);
#undef KOF
#undef SN
#undef SH
  return red_get(L, acc[0], w, lane);
}

__global__ __launch_bounds__(256, 1) void fused_fwd(Params p) {
  extern __shared__ __attribute__((aligned(16))) unsigned char smem[];
  lptr L = (lptr)smem;
  const u32 lds0 = (u32)(size_t)L;
#define IDS \
  const int tid = (int)opqv(threadIdx.x), lane = tid & 63, r = lane & 31, h = lane >> 5; \
  const int w = (int)opq(w_), b = (int)opq(blockIdx.x), g = b & 7, bi = b >> 3, pj = g >> 1; \
  const int T = 4 * pj + (bi >> 3), head = (g & 1) * 8 + (bi & 7); \
  const u32 pk_ = opq(tiles_); \
  const int st = pk_ & 31, dt = (pk_ >> 8) & 7, ft = (pk_ >> 16) & 31, dt4 = pk_ >> 24; \
  const int wa = w >> 1, wb = w & 1; \
  const int ch16 = (lane >> 4) & 1, tq = (lane & 15) >> 2, tp = lane & 3; \
  const u32 ringw = L_RING + (u32)w * RING_W; \
  const u32 dmarow = lane >> 3, dmach = ((lane & 7) ^ (((lane >> 4) & 1) << 2)) * 16; \
  const u32 sw = (u32)((tq >> 1) & 1) << 2, cpart = 2 * ch16 + (tp >> 1); \
  (void)r; (void)h; (void)T; (void)head; (void)st; (void)dt; (void)ft; (void)dt4; (void)wa; (void)wb; (void)ch16; (void)tq; (void)tp; (void)ringw; (void)dmarow; (void)dmach; (void)sw; (void)cpart;
  const u32 w_ = __builtin_amdgcn_readfirstlane(threadIdx.x >> 6);
  u32 tiles_;
  {
    const u32 g = blockIdx.x & 7, bi = blockIdx.x >> 3, pj = g >> 1, d3 = (bi * 43) >> 7, m3 = bi - 3 * d3;
    const u32 st = 8 * pj + (bi < 24 ? d3 : 4 * (g & 1) + (bi & 3)), dt = bi < 24 ? 4 * (g & 1) + m3 : 3;
    const u32 ft = 7 * pj + (bi < 21 ? d3 : 4 * (g & 1) + ((bi - 21) & 3)), dt4 = bi < 21 ? 4 * (g & 1) + m3 : 3;
    tiles_ = opq(st | (dt << 8) | (ft << 16) | (dt4 << 24));
  }
  const bool p35 = (blockIdx.x >> 3) < 28;
  const bool p4 = (blockIdx.x >> 3) < ((blockIdx.x & 1) ? 24u : 25u);

  const rsrc_t rws = mkrs(p.ws, WS_END);
  u64 tag = 0;
  u32 xres[8];
  v4u wf[8][2];
#pragma unroll
  for (int i = 0; i < 8; ++i) { xres[i] = 0; asm volatile("" : "=a"(wf[i][0])); asm volatile("" : "=a"(wf[i][1])); }

#pragma unroll 1
  for (int l = 0; l < 2; ++l) {
    STAMP(0);
    const rsrc_t rw1 = mkrs(p.w1, (p4 && !NOW) ? S_ * F_ * 2 : 0);
    const rsrc_t rw2 = mkrs(p.w2, (p35 && !NOW) ? F_ * S_ * 2 : 0);
    v4u bw4[20][2], bw5[16][2];
#define T5_PTRS \
    lptr t0 = L + ringw + (4 * h + tq) * 128 + (((4 * wb + cpart) ^ sw) * 16) + (tp & 1) * 8; \
    lptr t1 = L + ringw + (4 * h + tq) * 128 + (((4 * (wb ^ 1) + cpart) ^ sw) * 16) + (tp & 1) * 8;
#define T4_PTRS \
    lptr n0 = L + ringw + (8 * h + tq) * 128 + (((4 * wa + cpart) ^ sw) * 16) + (tp & 1) * 8; \
    lptr n1 = L + ringw + (8 * h + tq) * 128 + (((4 * (wa ^ 1) + cpart) ^ sw) * 16) + (tp & 1) * 8;
#define W2_ARGS rw2, dmarow * 4096u + dmach + (u32)st * 128u, (u32)(448 * w + 224 * ROT5) * 4096u, (u32)(448 * w + 224 * (ROT5 ^ 1)) * 4096u, lds0 + ringw
#define W1_ARGS rw1, dmarow * 3584u + dmach + (u32)ft * 128u, (u32)(512 * w + 256 * ROT4) * 3584u, (u32)(512 * w + 256 * (ROT4 ^ 1)) * 3584u, lds0 + ringw
    {
    IDS
    const int tb = 4 * T + w;
    const u32 shift = (head & 1) * 8;
    const u32 half = (((tq >> 1) & 1) ^ 1) * 64, col = shift + ch16 * 32 + tp * 8;
    lptr wtk = L + (32 * h + tq) * 128 + half + col, wtv = L + (32 * h + tq) * 128 + (half ^ 64) + col, wtq = L + L_WQ + (32 * h + tq) * 64 + col;
    v4u X[28];
    f32x16 kt, vv, rr, ck_, cv_;
#pragma unroll
    for (int i = 0; i < 16; ++i) { kt[i] = 0.f; vv[i] = 0.f; }
    float mean, rstd, nmr, csv;
    constexpr int LN0 = LA1 + 2;
    static_assert(LN0 + 6 <= 20, "order of the stages");
    auto ln_part = [&](auto I, float sum, float ssq) {
      constexpr int k = I;
      if constexpr (k == LN0) {
        mean = half_add(sum) * (1.f / 448.f);
        rstd = __builtin_amdgcn_rsqf(fmaxf(half_add(ssq) * (1.f / 448.f) - mean * mean, 0.f) + EPS);
        nmr = -mean * rstd;
        *(LAS float*)(L + L_MR + w * 256 + lane * 4) = h ? nmr : rstd;
        csv = *(LAS float*)(L + L_CSF + w * 512 + 256 + r * 4);
      }
      if constexpr (k >= LN0 + 2 && k < LN0 + 6) {
        constexpr int i = k - LN0 - 2;
        const f32x4 cs = *(LAS f32x4*)(L + L_CSF + w * 512 + 128 + (8 * i + 4 * h) * 4);
        const f32x4 r4 = *(LAS f32x4*)(L + L_MR + w * 256 + (8 * i + 4 * h) * 4);
        const f32x4 m4 = *(LAS f32x4*)(L + L_MR + w * 256 + 128 + (8 * i + 4 * h) * 4);
#pragma unroll
        for (int e = 0; e < 4; ++e) { ck_[4 * i + e] = cs[e]; cv_[4 * i + e] = m4[e]; rr[4 * i + e] = r4[e]; }
      }
      if constexpr (k >= 20) {
        constexpr int i = 2 * (k - 20);
        ck_[i] = mul1(ck_[i], nmr); cv_[i] = mul1(cv_[i], csv); ck_[i + 1] = mul1(ck_[i + 1], nmr); cv_[i + 1] = mul1(cv_[i + 1], csv);
      }
    };
    u32 tc2 = 0, tc3 = 0, tc4 = 0;
    v4u wk[28], wv[28], wq[28];
    constexpr int PD = 3;
#define WROW(k) (((k) >> 2) * 64 + ((k) & 3) * 8)
#define WFK(k) trd2(wtk + WROW(k) * 128, wtk + WROW(k) * 128 + 512)
#define WFV(k) trd2(wtv + WROW(k) * 128, wtv + WROW(k) * 128 + 512)
#define WFQ(k) trd2(wtq + WROW(k) * 64, wtq + WROW(k) * 64 + 256)
    auto kv_head = [&]() {
#pragma unroll
      for (int k = 0; k < PD; ++k) { wk[k] = WFK(k); wv[k] = WFV(k); }
      SB();
    };
    auto kv_step = [&](auto I, auto&& mem) {
      constexpr int k = I;
      kt = mfma(wk[k], X[k], kt);
      SB();
      if constexpr (k + PD < 28) wk[k + PD] = WFK(k + PD);
      mem();
      SB();
      vv = mfma(X[k], wv[k], vv);
      SB();
      if constexpr (k + PD < 28) wv[k + PD] = WFV(k + PD);
      SB();
    };
    const u32 gw = (u32)(b * 4 + w);
    auto touch_w2a = [&]() { const u32 n = gw * 24 + (lane < 24 ? lane : 0), rr_ = n >> 5;
      return touch(mkrs(p.w2, TOUCH ? S_ * F_ * 2 : 0), ((rr_ / 192) * 448 + rr_ % 192) * 4096u + (n & 31) * 128u, 0); };
    auto touch_w1a = [&]() { const u32 n = gw * 21 + (lane < 21 ? lane : 0), rr_ = n / 28;
      return touch(mkrs(p.w1, TOUCH ? S_ * F_ * 2 : 0), ((rr_ / 192) * 512 + rr_ % 192) * 3584u + (n % 28) * 128u, 0); };
    const v4u ones = {0x3F803F80u, 0x3F803F80u, 0x3F803F80u, 0x3F803F80u};
    if (l == 0) {
      v2u e0;
      asm volatile("global_load_dwordx2 %0, %1, %2 sc1" : "=v"(e0) : "v"(WS_EPOCH), "s"(p.ws));
      u32 tcc;
      {
        u64 pc; u32 back, len;
        asm volatile("s_getpc_b64 %0\n.Lfwd_here:\n\ts_mov_b32 %1, .Lfwd_here-_Z9fused_fwd6Params\n\ts_mov_b32 %2, .Lfwd_last-_Z9fused_fwd6Params" : "=s"(pc), "=s"(back), "=s"(len));
        tcc = touch(mkrs((const void*)(pc - back), CODE_TOUCH ? len : 0), lane * 128, (u32)(bi * 4 + w) * 8192u);
      }
      const rsrc_t rx = mkrs(p.x, S_ * D_ * 2), rwq = mkrs(p.wqkv, D_ * 3 * D_ * 2);
      const u32 hoff = (u32)(head * 56) - shift;
      const u32 xv = dmarow * 896u + (lane & 7) * 16u, xs = (u32)(tb * 32) * 896u;
      lptr xb = L + L_WO + w * 15360;
      lptr xw = xb + dmarow * 128, xr = xb + r * 128;
      const u32 xsw = (r >> 1) & 7, xc0 = ((lane & 7) ^ (lane >> 4)) * 16;
      const u32 kvv = dmarow * 2688u + 896u + ((((lane >> 2) ^ (lane >> 4) ^ 1) & 1) * 896u) + (lane & 3) * 16u + hoff;
      const u32 kvs = (u32)(112 * w) * 2688u, kvl0 = lds0 + (u32)(14 * w) * 1024u;
      const rsrc_t rwo = mkrs(p.wo, D_ * D_ * 2);
      const u32 wov = dmarow * 896u + dmach + (u32)dt * 128u, wos = (u32)(112 * w) * 896u, wol = lds0 + L_WO + (u32)w * 15360u;
      const u32 qv = (u32)(lane >> 2) * 2688u + (lane & 3) * 16u + hoff, ql = lds0 + L_WQ + (u32)(7 * w) * 1024u;
      sfor<0, 28>([&](auto I) { constexpr int k = I; X[k] = ldp<(k >> 2) * 128>(rx, xv, xs + (k & 3) * 7168u); });
      sfor<0, 7>([&](auto I) { constexpr int i = I; dma16<i * 8 * 2688, i * 1024>(rwq, kvv, kvs, kvl0); });
      STAMP(24);
      float sq4[4] = {0.f, 0.f, 0.f, 0.f};
      sfor<0, 7>([&](auto I) {
        constexpr int G = I;
        vmw<24 - 4 * G + 7 + 2 * G>(X[4 * G], X[4 * G + 1], X[4 * G + 2], X[4 * G + 3]);
#pragma unroll
        for (int i = 0; i < 4; ++i) *(LAS v4u*)(xw + i * 1024 + (xc0 ^ ((i & 1) * 64))) = X[4 * G + i];
#pragma unroll
        for (int c = 0; c < 4; ++c) X[4 * G + c] = *(LAS v4u*)(xr + (((4 * h + c) ^ xsw) * 16));
        if constexpr (2 * G < 7) dma16<(7 + 2 * G) * 8 * 2688, (7 + 2 * G) * 1024>(rwq, kvv, kvs, kvl0); else dma16<(2 * G - 7) * 16 * 2688, (2 * G - 7) * 1024>(rwq, qv, kvs, ql);
        if constexpr (2 * G + 1 < 7) dma16<(8 + 2 * G) * 8 * 2688, (8 + 2 * G) * 1024>(rwq, kvv, kvs, kvl0); else dma16<(2 * G - 6) * 16 * 2688, (2 * G - 6) * 1024>(rwq, qv, kvs, ql);
#pragma unroll
        for (int i = 0; i < 16; ++i) {
          const bf16x2 xx = asbf2(X[4 * G + (i >> 2)][i & 3]);
          sq4[i & 1] = __builtin_amdgcn_fdot2_f32_bf16(xx, xx, sq4[i & 1], false);
          sq4[2 + (i & 1)] = __builtin_amdgcn_fdot2_f32_bf16(xx, asbf2(0x3F803F80u), sq4[2 + (i & 1)], false);
        }
        pin4(sq4);
        SB();
      });
      STAMP(25);
      f32x16 ck, cv;
#pragma unroll
      for (int i = 0; i < 16; ++i) { ck[i] = 0.f; cv[i] = 0.f; }
      lptr ck0 = L + (112 * w + 8 * h + tq) * 128 + half + col, cv0 = L + (112 * w + 8 * h + tq) * 128 + (half ^ 64) + col;
      vmw0<7>();
      STAMP(26);
      {
        v4u f[14];
#pragma unroll
        for (int i = 0; i < 7; ++i) { f[2 * i] = trd2(ck0 + i * 2048, ck0 + i * 2048 + 512); f[2 * i + 1] = trd2(cv0 + i * 2048, cv0 + i * 2048 + 512); }
        SB();
        sfor<0, 7>([&](auto I) { constexpr int i = I;
          ck = mfma(ones, f[2 * i], ck); SB(); w_issue<2 * i, 2 * i + 1, 896u>(rwo, wov, wos, wol); SB();
          cv = mfma(ones, f[2 * i + 1], cv); SB(); w_issue<2 * i + 1, 2 * i + 2, 896u>(rwo, wov, wos, wol); SB(); });
        w_issue<14, 15, 896u>(rwo, wov, wos, wol);
      }
      tc2 = touch_w2a(); tc3 = touch_w1a();
      STAMP(27);
      touch_done(tcc);
      asm volatile("" : "+v"(e0));
      tag = (((u64)(u32)__builtin_amdgcn_readfirstlane(e0.y) << 32) | (u64)(u32)__builtin_amdgcn_readfirstlane(e0.x)) + 1ull;
      *(LAS float*)(L + L_CSP + w * 384 + 128 + lane * 4) = h ? cv[0] : ck[0];
      bar();
      kv_head();
      {
        float a0 = 0.f;
#pragma unroll
        for (int v = 0; v < 4; ++v) a0 += *(LAS float*)(L + L_CSP + v * 384 + 128 + lane * 4);
        *(LAS float*)(L + L_CSF + w * 512 + 128 + lane * 4) = a0;
      }
      const float su0 = sq4[2] + sq4[3], sq0 = sq4[0] + sq4[1];
      STAMP(1);
      SB();
      sfor<0, 28>([&](auto I) { kv_step(I, [&]() { ln_part(I, su0, sq0); }); });
      asm volatile("" : "+a"(kt), "+a"(vv));
    } else {
      kv_head();
      const Poll pq = wait_flags(p.ws, flag_off(0, 4, (2 * T + (w >> 1)) * 8 + (lane < 56 ? lane >> 3 : 6)), tag);
      STAMP(1);
      const u32 xb = WS_XF + (u32)(tb * 28) * 1024u;
      const u32 sqv = (u32)(7 * h) * 256u + r * 8, sqs = WS_SQ + (u32)tb * 3584u;
      v2u sp[7];
      sfor<0, LA1>([&](auto I) { constexpr int k = I; X[k] = lda<k>(rws, lane * 16, xb); });
      sfor<0, 7>([&](auto I) { constexpr int i = I; sp[i] = lda2<i * 256>(rws, sqv, sqs); });
      float su1 = 0.f, sq1 = 0.f;
      sfor<0, 28>([&](auto I) {
        constexpr int k = I;
        vmw<((k + LA1 <= 28) ? LA1 - 1 : 27 - k) + (k < LA1 ? 7 : 0)>(X[k]);
        if constexpr (k == 0) poll_done(pq);
        if constexpr (k == LA1) pin7(sp);
        kv_step(I, [&]() {
          if constexpr (k + LA1 < 28) X[k + LA1] = lda<k + LA1>(rws, lane * 16, xb);
          if constexpr (k == LA1) su1 = ((asf(sp[0].x) + asf(sp[1].x)) + (asf(sp[2].x) + asf(sp[3].x))) + ((asf(sp[4].x) + asf(sp[5].x)) + asf(sp[6].x));
          if constexpr (k == LA1 + 1) sq1 = ((asf(sp[0].y) + asf(sp[1].y)) + (asf(sp[2].y) + asf(sp[3].y))) + ((asf(sp[4].y) + asf(sp[5].y)) + asf(sp[6].y));
          ln_part(I, su1, sq1);
        });
      });
      asm volatile("" : "+a"(kt), "+a"(vv));
    }
    STAMP(18);
    const u32 kvbase = WS_KV + l * KV_L + (u32)head * (64u * 4096u);
    lptr kvl = L + L_INBOX + lane * 16;
    v4u K0, K1, V0, V1;
    {
      u32 o[8];
#pragma unroll
      for (int i = 0; i < 8; ++i) o[i] = pk(kt[2 * i] * rstd + ck_[2 * i], kt[2 * i + 1] * rstd + ck_[2 * i + 1]);
      if (h) { o[6] = 0u; o[7] = 0u; }
      K0 = v4u{o[0], o[1], o[2], o[3]}; K1 = v4u{o[4], o[5], o[6], o[7]};
      st_sc1(K0, rws, lane * 16, kvbase + tb * 4096);
      st_sc1(K1, rws, lane * 16 + 1024, kvbase + tb * 4096);
#pragma unroll
      for (int i = 0; i < 8; ++i) o[i] = pk(vv[2 * i] * rr[2 * i] + cv_[2 * i], vv[2 * i + 1] * rr[2 * i + 1] + cv_[2 * i + 1]);
      if (r >= 28) {
#pragma unroll
        for (int i = 0; i < 8; ++i) o[i] = 0x3F803F80u;
      }
      V0 = v4u{o[0], o[1], o[2], o[3]}; V1 = v4u{o[4], o[5], o[6], o[7]};
      st_sc1(V0, rws, lane * 16 + 2048, kvbase + tb * 4096);
      st_sc1(V1, rws, lane * 16 + 3072, kvbase + tb * 4096);
    }
    SB();
    u32 em[16];
#pragma unroll
    for (int i = 0; i < 16; ++i) { const int key = (i & 3) + 8 * (i >> 2) + 4 * h; em[i] = key > r ? 0xffffffffu : 0u; }
#pragma unroll
    for (int i = 0; i < 16; ++i) asm volatile("" : "+v"(em[i]));
    u32 t4v;
    { const u32 n = gw * 35 + (lane < 35 ? lane : 0), rr_ = n / 28; t4v = opqv(((rr_ / 320) * 512 + 192 + rr_ % 320) * 3584u + (n % 28) * 128u); }
    STAMP(19);
    if (l == 0) {
      vmw0<6>();
      lptr t0 = L + L_WO + w * 15360 + (4 * h + tq) * 128 + (((4 * wa + cpart) ^ sw) * 16) + (tp & 1) * 8;
      lptr t1 = L + L_WO + w * 15360 + (4 * h + tq) * 128 + (((4 * (wa ^ 1) + cpart) ^ sw) * 16) + (tp & 1) * 8;
      lptr cq0 = L + L_WQ + (112 * w + 8 * h + tq) * 64 + col;
      v4u f[7];
#pragma unroll
      for (int i = 0; i < 7; ++i) f[i] = trd2(cq0 + i * 1024, cq0 + i * 1024 + 256);
#pragma unroll
      for (int k = 0; k < 8; ++k) {
        const int ro = (28 * (k >> 1) + 16 * (k & 1)) * 128;
        wf[k][0] = trd2(t0 + ro, t0 + ro + 1024); wf[k][1] = trd2(t1 + ro, t1 + ro + 1024);
      }
      SB();
      f32x16 cq;
#pragma unroll
      for (int i = 0; i < 16; ++i) cq[i] = 0.f;
#pragma unroll
      for (int i = 0; i < 7; ++i) cq = mfma(ones, f[i], cq);
      *(LAS float*)(L + L_CSP + w * 384 + r * 4) = cq[0];
      st_sc1(v4u{0u, 0u, 0u, 0u}, rws, head < 14 ? WS_STATS + h * STATS_L + (u32)((tb * 14 + head) * 512 + r * 16) : 0x40000000u, 0);
    }
    publish(p.ws, flag_off(l, 0, T * 16 + head), tag);
    touch_done(tc2); touch_done(tc3);
    STAMP(2);
#pragma unroll
    for (int k = 0; k < 2 * PD; ++k) wq[k] = WFQ(k);
    {
      lptr own = kvl + (8 + w) * 4096;
      *(LAS v4u*)own = K0; *(LAS v4u*)(own + 1024) = K1; *(LAS v4u*)(own + 2048) = V0; *(LAS v4u*)(own + 3072) = V1;
    }
    if (l == 0) {
      float a0 = 0.f;
#pragma unroll
      for (int v = 0; v < 4; ++v) a0 += *(LAS float*)(L + L_CSP + v * 384 + r * 4);
      *(LAS float*)(L + L_CSF + w * 512 + r * 4) = a0;
      const u32 wib = WS_WI + (u32)head * 86016u + (u32)tb * 1024u;
      st_sc1(*(LAS v4u*)(L + tb * 1024 + lane * 16), rws, lane * 16, wib);
      st_sc1(*(LAS v4u*)(L + (tb < 20 ? tb + 64 : tb) * 1024 + lane * 16), rws, lane * 16, tb < 20 ? wib + 65536u : 0x40000000u);
      const u32 xo = (u32)((2 * st + wb) * 32 + r) * 896u + (u32)(dt * 64 + wa * 32 + 4 * h) * 2u;
      const rsrc_t rx = mkrs(p.x, S_ * D_ * 2);
#pragma unroll
      for (int i = 0; i < 4; ++i) {
        const v2u v = __builtin_amdgcn_raw_buffer_load_b64(rx, xo + i * 16, 0, 0);
        xres[2 * i] = v.x; xres[2 * i + 1] = v.y;
      }
    }
    {
      const int tsrc = T - 2 + (w >> 1);
      const u32 f1 = opqv(flag_off(l, 0, (tsrc < 0 ? T : tsrc) * 16 + head));
      Poll pq;
      f32x16 qt;
#pragma unroll
      for (int i = 0; i < 4; ++i) {
        const f32x4 cs = *(LAS f32x4*)(L + L_CSF + w * 512 + (8 * i + 4 * h) * 4);
#pragma unroll
        for (int e = 0; e < 4; ++e) qt[4 * i + e] = -mean * cs[e];
      }
      SB();
      sfor<0, 28>([&](auto I) {
        constexpr int k = I;
        qt = mfma(wq[k], X[k], qt);
        SB();
        if constexpr (k + 2 * PD < 28) wq[k + 2 * PD] = WFQ(k + 2 * PD);
        if constexpr (k == QP0) pq.a = poll_one(p.ws, f1);
        if constexpr (k == QP0 + 6) pq.b = poll_one(p.ws, f1);
        if constexpr (k == QP0 + 12) pq.c = poll_one(p.ws, f1);
        SB();
      });
      asm volatile("" : "+a"(qt));
      poll_finish(pq, p.ws, f1, tag);
      STAMP(3);
      const u32 so = tsrc >= 0 ? kvbase + (u32)(4 * T - 8 + 2 * w) * 4096u : 0x40000000u;
      v4u t[8];
      t[0] = lda<0>(rws, lane * 16, so); t[1] = lda<1>(rws, lane * 16, so); t[2] = lda<4>(rws, lane * 16, so); t[3] = lda<5>(rws, lane * 16, so);
      t[4] = lda<2>(rws, lane * 16, so); t[5] = lda<3>(rws, lane * 16, so); t[6] = lda<6>(rws, lane * 16, so); t[7] = lda<7>(rws, lane * 16, so);
      SB();
      v4u Q0, Q1;
      {
        const float rs = rstd * (0.18898223650461362f * 1.4426950408889634f);
        u32 o[8];
#pragma unroll
        for (int i = 0; i < 8; ++i) o[i] = pk(qt[2 * i] * rs, qt[2 * i + 1] * rs);
        if (h) { o[6] = 0x00003F80u; o[7] = 0u; }
        Q0 = v4u{o[0], o[1], o[2], o[3]}; Q1 = v4u{o[4], o[5], o[6], o[7]};
      }
      float nm;
      f32x16 cm, c0, o;
      {
        f32x16 s8 = qk_own(K0, K1, Q0, Q1);
        tie_far(s8);
#pragma unroll
        for (int i = 0; i < 16; ++i) s8[i] = asf((em[i] & 0xF149F2CAu) | (~em[i] & asu(s8[i])));
        float m4[4];
#pragma unroll
        for (int i = 0; i < 4; ++i) m4[i] = fmaxf(fmaxf(s8[4 * i], s8[4 * i + 1]), fmaxf(s8[4 * i + 2], s8[4 * i + 3]));
        nm = -half_max(fmaxf(fmaxf(m4[0], m4[1]), fmaxf(m4[2], m4[3])));
        u32 pp[8];
#pragma unroll
        for (int i = 0; i < 8; ++i) pp[i] = pk(__builtin_amdgcn_exp2f(s8[2 * i] + nm), __builtin_amdgcn_exp2f(s8[2 * i + 1] + nm));
#pragma unroll
        for (int i = 0; i < 16; ++i) o[i] = 0.f;
        o = mfma(V0, v4u{pp[0], pp[1], pp[2], pp[3]}, o);
        o = mfma(V1, v4u{pp[4], pp[5], pp[6], pp[7]}, o);
#pragma unroll
        for (int i = 0; i < 16; ++i) { cm[i] = nm; c0[i] = asf((em[i] & asu(nm)) | (~em[i] & 0xF149F2CAu)); }
        asm volatile("" : "+v"(cm), "+v"(c0), "+a"(o));
      }
      const u32 w2v = opqv(dmarow * 4096u + dmach + (u32)st * 128u), w2s = opq((u32)(448 * w) * 4096u), w2l = opq(lds0 + ringw);
      const rsrc_t rt4 = mkrs(p.w1, (TOUCH && l == 0) ? S_ * F_ * 2 : 0);
      lptr kq = kvl + w * 4096;
      SB();
      {
        lptr dst = kvl + w * 8192;
        vmw<4>(t[0], t[1], t[2], t[3]);
        poll_done(pq);
        if (tsrc < 0 && h) { t[1][2] = 0xF149u; t[3][2] = 0xF149u; }
#pragma unroll
        for (int i = 0; i < 4; ++i) *(LAS v4u*)(dst + (i & 1) * 1024 + (i >> 1) * 4096) = t[i];
        vmw<0>(t[4], t[5], t[6], t[7]);
#pragma unroll
        for (int i = 0; i < 4; ++i) *(LAS v4u*)(dst + 2048 + (i & 1) * 1024 + (i >> 1) * 4096) = t[4 + i];
      }
      bar();
      STAMP(17);
      {
        v4u Kr[8][2], Vr[8][2];
        f32x16 s[8];
        u32 pp[8][8];
        float e[16];
#define KLD(j) do { Kr[j][0] = *(LAS v4u*)(kq + (j) * 4096); Kr[j][1] = *(LAS v4u*)(kq + (j) * 4096 + 1024); } while (0)
#define VLD(j) do { Vr[j][0] = *(LAS v4u*)(kq + (j) * 4096 + 2048); Vr[j][1] = *(LAS v4u*)(kq + (j) * 4096 + 3072); } while (0)
#define W2P(i) w_issue<i, (i) + 1, 4096u>(rw2, w2v, w2s, w2l)
        KLD(0); KLD(1); KLD(2); VLD(0);
        tc4 = touch(rt4, t4v, 0);
        SB();
        s[0] = qk1(Kr[0][0], Q0, c0); qk2(s[0], Kr[0][1], Q1);
        s[1] = qk1(Kr[1][0], Q0, cm); qk2(s[1], Kr[1][1], Q1);
        SB();
        sfor<0, 8>([&](auto I) {
          constexpr int c = I;
          tie(s[c]);
          auto ex = [&](int q) {
#pragma unroll
            for (int i = 0; i < 4; ++i) e[4 * q + i] = __builtin_amdgcn_exp2f(s[c][4 * q + i]);
          };
          auto cv = [&](int q) { pp[c][2 * q] = pk(e[4 * q], e[4 * q + 1]); pp[c][2 * q + 1] = pk(e[4 * q + 2], e[4 * q + 3]); };
          SB(); ex(0); if constexpr (c < 6) W2P(4 * c); if constexpr (c < 5) KLD(c + 3); SB();
          if constexpr (c < 6) s[c + 2] = qk1(Kr[c + 2][0], Q0, cm);
          SB(); ex(1); cv(0); if constexpr (c < 6) W2P(4 * c + 1); if constexpr (c < 7) VLD(c + 1); SB();
          if constexpr (c < 6) qk2(s[c + 2], Kr[c + 2][1], Q1);
          SB(); cv(1); ex(2); if constexpr (c < 6) W2P(4 * c + 2); SB();
          o = mfma(Vr[c][0], v4u{pp[c][0], pp[c][1], pp[c][2], pp[c][3]}, o);
          SB(); ex(3); cv(2); cv(3); if constexpr (c < 6) W2P(4 * c + 3); SB();
          o = mfma(Vr[c][1], v4u{pp[c][4], pp[c][5], pp[c][6], pp[c][7]}, o);
        });
      }
      float lsum = half_add(h ? o[12] : 0.f);
      if (!__all(lsum < SAFE_SUM)) {
        float mx = -nm;
#pragma unroll 1
        for (int pass = 0; pass < 2; ++pass) {
#pragma unroll
          for (int i = 0; i < 16; ++i) o[i] = 0.f;
#pragma unroll 1
          for (int j = 0; j < 9; ++j) {
            f32x16 sj = qk_own(*(LAS v4u*)(kq + j * 4096), *(LAS v4u*)(kq + j * 4096 + 1024), Q0, Q1);
            tie_far(sj);
            const u32 keep = (j == 0 || j == 8) ? 0u : 0xffffffffu, flip = j == 8 ? 0xffffffffu : 0u;
            u32 pp[8];
#pragma unroll
            for (int i = 0; i < 16; ++i) { const u32 m = (em[i] ^ flip) | keep; sj[i] = asf((m & asu(sj[i])) | (~m & 0xF149F2CAu)); if (pass == 0) mx = fmaxf(mx, sj[i]); }
#pragma unroll
            for (int i = 0; i < 8; ++i) pp[i] = pk(__builtin_amdgcn_exp2f(sj[2 * i] - mx), __builtin_amdgcn_exp2f(sj[2 * i + 1] - mx));
            o = mfma(*(LAS v4u*)(kq + j * 4096 + 2048), v4u{pp[0], pp[1], pp[2], pp[3]}, o);
            o = mfma(*(LAS v4u*)(kq + j * 4096 + 3072), v4u{pp[4], pp[5], pp[6], pp[7]}, o);
          }
          mx = half_max(mx);
        }
        lsum = half_add(h ? o[12] : 0.f);
      }
      const float inv = __builtin_amdgcn_rcpf(lsum);
      u32 oo[8];
#pragma unroll
      for (int i = 0; i < 8; ++i) oo[i] = pk(o[2 * i] * inv, o[2 * i + 1] * inv);
      if (h) { oo[6] = 0u; oo[7] = 0u; }
      const u32 ob = WS_OF + l * OF_L + (u32)(tb * 16 + head) * 2048u;
      st_sc1(v4u{oo[0], oo[1], oo[2], oo[3]}, rws, lane * 16, ob);
      st_sc1(v4u{oo[4], oo[5], oo[6], oo[7]}, rws, lane * 16 + 1024, ob);
      {
        T5_PTRS
        vmw0<2>();
        w_take<0, 12, 1024>(t0, t1, bw5);
      }
      publish(p.ws, flag_off(l, 1, T * 16 + head), tag);
      touch_done(tc4);
      STAMP(4);
    }
    }
    if (p35) {
      v2u bias1[4];
      u32 bias2;
      {
      IDS
      {
        const rsrc_t rb1 = mkrs(p.b1, F_ * 2);
        const u32 bo = (u32)(ft * 64 + wa * 32 + 4 * h) * 2u;
        asm volatile("s_nop 4\n\tbuffer_load_ushort %0, %1, %2, 0 offen" : "=v"(bias2) : "v"((u32)((2 * st + wb) * 32 + r) * 2u), "s"(mkrs(p.b2, S_ * 2)));
        sfor<0, 4>([&](auto I) { constexpr int i = I; bias1[i] = ldp2<16 * i>(rb1, bo); });
      }
      const u32 f2 = flag_off(l, 1, (st >> 1) * 16 + 4 * w + (lane >> 4));
      Poll pq;
      f32x16 init;
#pragma unroll
      for (int i = 0; i < 8; ++i) { init[2 * i] = blo(xres[i]); init[2 * i + 1] = bhi(xres[i]); }
      asm volatile("" : "+a"(init));
      w_issue2<0, PA0, 3584u, 32>(W1_ARGS); pq.a = poll_one(p.ws, f2);
      w_issue2<PA0, PA1, 3584u, 32>(W1_ARGS); pq.b = poll_one(p.ws, f2);
      w_issue2<PA1, 24, 3584u, 32>(W1_ARGS); pq.c = poll_one(p.ws, f2);
      const u32 o0 = opq(WS_OF + l * OF_L + (u32)st * 65536u + (u32)wb * 32768u + (u32)w * 8192u);
      const u32 o1 = opq(o0 ^ 32768u);
      poll_finish_mixed<24 - PA0 + 2, 24 - PA1 + 1, 0>(pq, p.ws, f2, tag);
      asm volatile("" : "+v"(bias1[0]), "+v"(bias1[1]), "+v"(bias1[2]), "+v"(bias1[3]), "+v"(bias2));
      STAMP(5);
      const f32x16 y = gemm_main<8, 8, 0, true, 12, 4, false>(L, w, lane, rws, o0, o1, L, L, wf, init, pq, [&](auto) {});
      STAMP(21);
      float s1 = 0.f, s2 = 0.f;
#pragma unroll
      for (int i = 0; i < 8; ++i) {
        xres[i] = pk(y[2 * i], y[2 * i + 1]);
        s1 = __builtin_amdgcn_fdot2_f32_bf16(asbf2(xres[i]), asbf2(0x3F803F80u), s1, false);
        s2 = __builtin_amdgcn_fdot2_f32_bf16(asbf2(xres[i]), asbf2(xres[i]), s2, false);
      }
      s1 = half_add(s1); s2 = half_add(s2);
      const u32 tg = (u32)tag | 1u;
      const u32 sb = WS_STATS + l * STATS_L + (u32)((2 * st + wb) * 14) * 512u;
      __builtin_amdgcn_raw_buffer_store_b64(v2u{asu(h ? s2 : s1), tg}, rws, r * 16 + h * 8, sb + (u32)(2 * dt + wa) * 512u, 16);
      SB();
      STAMP(6);
      T4_PTRS
      w_take<0, NB, 512>(n0, n1, bw4);
      f32x16 xv;
#pragma unroll
      for (int i = 0; i < 8; ++i) { xv[2 * i] = blo(xres[i]); xv[2 * i + 1] = bhi(xres[i]); }
      asm volatile("s_waitcnt lgkmcnt(0)" : "+v"(xv)::"memory");
      float t1s, t2s;
      {
        v4u gr[7];
        const u32 gv = (u32)(7 * h) * 512u + r * 16;
        w_issue2<24, 24 + PB0, 3584u, 32>(W1_ARGS);
        sfor<0, 7>([&](auto I) { constexpr int i = I; gr[i] = ldg<i * 512>(rws, gv, sb); });
        w_issue2<24 + PB0, 24 + 2 * NB, 3584u, 32>(W1_ARGS);
        vmw<2 * NB - PB0>(gr[0], gr[1], gr[2], gr[3]); vmw<2 * NB - PB0>(gr[4], gr[5]); vmw<2 * NB - PB0>(gr[6]);
        for (u32 spins = 0;;) {
          u32 bad = 0; t1s = 0.f; t2s = 0.f;
#pragma unroll
          for (int i = 0; i < 7; ++i) { bad |= (gr[i].y ^ tg) | (gr[i].w ^ tg); t1s += asf(gr[i].x); t2s += asf(gr[i].z); }
          if (__all(bad == 0)) break;
          if (spin_out(p.ws, spins, (u32)tag)) break;
          asm volatile("" ::: "memory");
#pragma unroll
          for (int i = 0; i < 7; ++i) gr[i] = ld_sc1(rws, gv + i * 512, sb);
        }
      }
      STAMP(7);
      t1s = half_add(t1s); t2s = half_add(t2s);
      const float m1 = t1s * (1.f / 448.f);
      const float var = fmaxf(t2s * (1.f / 448.f) - m1 * m1, 0.f);
      const float r1 = __builtin_amdgcn_rsqf(var + EPS);
      const float r2 = __builtin_amdgcn_rsqf(var * r1 * r1 + EPS);
      const float rr = r1 * r2, mm = m1 * rr;
      lptr tw = L + L_TR + w * 2304;
#pragma unroll
      for (int i = 0; i < 4; ++i)
        *(LAS v2u*)(tw + r * 72 + i * 16 + h * 8) = v2u{pk(xv[4 * i] * rr - mm, xv[4 * i + 1] * rr - mm), pk(xv[4 * i + 2] * rr - mm, xv[4 * i + 3] * rr - mm)};
      asm volatile("" ::: "memory");
      lptr tr = tw + (8 * h + tq) * 72 + ch16 * 32 + tp * 8;
      const u32 hb = WS_HF + l * HF_L + (u32)((2 * dt + wa) * 128 + (2 * st + wb) * 2) * 1024u;
      st_sc1(trd2(tr, tr + 4 * 72), rws, lane * 16, hb);
      st_sc1(trd2(tr + 16 * 72, tr + 20 * 72), rws, lane * 16 + 1024, hb);
      vmw0<2>();
      w_take<NB, 20, 512>(n0, n1, bw4);
      publish(p.ws, flag_off(l, 2, dt * 32 + st), tag);
      STAMP(8);
      }
      if (p4) {
        IDS
        T4_PTRS
        const u32 f3 = flag_off(l, 2, dt4 * 32 + 8 * w + (lane >> 3));
        Poll pq;
        f32x16 init;
#pragma unroll
        for (int i = 0; i < 8; ++i) { const u32 bb = (i & 1) ? bias1[i >> 1].y : bias1[i >> 1].x; init[2 * i] = blo(bb); init[2 * i + 1] = bhi(bb); }
        asm volatile("" : "+a"(init));
        w_issue2<24 + 2 * NB, 64 - PC0, 3584u, 32>(W1_ARGS); pq.a = poll_one(p.ws, f3);
        w_issue2<64 - PC0, 64 - PC1, 3584u, 32>(W1_ARGS); pq.b = poll_one(p.ws, f3);
        w_issue2<64 - PC1, 64, 3584u, 32>(W1_ARGS); pq.c = poll_one(p.ws, f3);
        const u32 hb = WS_HF + l * HF_L + (u32)(32 * w) * 1024u;
        const u32 h0 = opq(hb + (u32)(2 * dt4 + wb) * 131072u), h1 = opq(h0 ^ 131072u);
        STAMP(9);
        poll_finish_mixed<PC0 + 2, PC1 + 1, 0>(pq, p.ws, f3, tag);
        STAMP(10);
        const f32x16 y = gemm_main<32, 20, 512, true, 12, 6, true>(L, w, lane, rws, h0, h1, n0, n1, bw4, init, pq,
                                                                   [&](auto I) { constexpr int i = I; w_issue2<24 + i, 25 + i, 4096u, 28>(W2_ARGS); });
        STAMP(11);
        u32 o[8];
#pragma unroll
        for (int i = 0; i < 8; ++i) {
          const f32x2 u = {y[2 * i], y[2 * i + 1]};
          const f32x2 z = u * (u * u * -0.1029432397f - 2.302208198f);
          const f32x2 d = f32x2{__builtin_amdgcn_exp2f(z[0]), __builtin_amdgcn_exp2f(z[1])} + 1.f;
          const f32x2 gv = u * f32x2{__builtin_amdgcn_rcpf(d[0]), __builtin_amdgcn_rcpf(d[1])};
          o[i] = pk(gv[0], gv[1]);
        }
        const u32 ub = WS_UF + l * UF_L + (u32)((2 * dt4 + wb) * 112 + 4 * ft + 2 * wa) * 1024u;
        st_sc1(v4u{o[0], o[1], o[2], o[3]}, rws, lane * 16, ub);
        st_sc1(v4u{o[4], o[5], o[6], o[7]}, rws, lane * 16 + 1024, ub);
        STAMP(23);
        T5_PTRS
        vmw0<2>();
        w_take<12, 16, 1024>(t0, t1, bw5);
        publish(p.ws, flag_off(l, 3, dt4 * 32 + ft), tag);
        STAMP(12);
      } else {
        IDS
        T5_PTRS
        w_issue2<24, 36, 4096u, 28>(W2_ARGS);
        drain();
        w_take<12, 16, 1024>(t0, t1, bw5);
        lgk0();
      }
      IDS
      T5_PTRS
      const u32 f4 = flag_off(l, 3, dt * 32 + 7 * w + (lane < 56 ? lane >> 3 : 6));
      Poll pq;
      f32x16 init;
      const float bv = blo(bias2);
#pragma unroll
      for (int i = 0; i < 8; ++i) { init[2 * i] = blo(xres[i]) + bv; init[2 * i + 1] = bhi(xres[i]) + bv; }
      asm volatile("" : "+a"(init));
      w_issue2<36, 56 - PD0, 4096u, 28>(W2_ARGS); pq.a = poll_one(p.ws, f4);
      w_issue2<56 - PD0, 56 - PD1, 4096u, 28>(W2_ARGS); pq.b = poll_one(p.ws, f4);
      w_issue2<56 - PD1, 56, 4096u, 28>(W2_ARGS); pq.c = poll_one(p.ws, f4);
      const u32 ub = WS_UF + l * UF_L + (u32)(28 * w) * 1024u;
      const u32 u0 = opq(ub + (u32)(2 * dt + wa) * 114688u), u1 = opq(u0 + 114688u - (u32)wa * 229376u);
      const u32 wis = l == 0 ? WS_WI + (u32)head * 86016u + (u32)w * 21504u : 0x40000000u;
      STAMP(13);
      poll_finish_mixed<PD0 + 2, PD1 + 1, 0>(pq, p.ws, f4, tag);
      STAMP(14);
      v4u wi[21];
      const f32x16 y = gemm_main<28, 16, 1024, false, 12, 6, true>(L, w, lane, rws, u0, u1, t0, t1, bw5, init, pq,
                                                                   [&](auto I) { constexpr int i = I; wi[i] = lda<i>(rws, lane * 16, wis); });
      asm volatile("s_waitcnt vmcnt(0)" : "+v"(wi[0]), "+v"(wi[1]), "+v"(wi[2]), "+v"(wi[3]), "+v"(wi[4]), "+v"(wi[5]), "+v"(wi[6]), "+v"(wi[7]), "+v"(wi[8]), "+v"(wi[9]), "+v"(wi[10]), "+v"(wi[11]));
      STAMP(15);
#pragma unroll
      for (int i = 0; i < 8; ++i) xres[i] = pk(y[2 * i], y[2 * i + 1]);
      v4u xo[2];
#pragma unroll
      for (int s = 0; s < 2; ++s) {
        auto a = __builtin_amdgcn_permlane32_swap(xres[4 * s], xres[4 * s + 2], false, false);
        auto c = __builtin_amdgcn_permlane32_swap(xres[4 * s + 1], xres[4 * s + 3], false, false);
        xo[s] = v4u{a[0], c[0], a[1], c[1]};
      }
      if (l == 0) {
        const u32 xb = WS_XF + (u32)((2 * st + wb) * 28 + 4 * dt) * 1024u + wa * 512;
        st_sc1(xo[0], rws, h * 1024 + r * 16, xb);
        st_sc1(xo[1], rws, h * 1024 + r * 16 + 2048, xb);
        float q0 = 0.f, q1 = 0.f;
#pragma unroll
        for (int i = 0; i < 8; ++i) { q0 = __builtin_amdgcn_fdot2_f32_bf16(asbf2(xres[i]), asbf2(0x3F803F80u), q0, false); q1 = __builtin_amdgcn_fdot2_f32_bf16(asbf2(xres[i]), asbf2(xres[i]), q1, false); }
        q0 = half_add(q0); q1 = half_add(q1);
        __builtin_amdgcn_raw_buffer_store_b32(asu(h ? q1 : q0), rws, r * 8 + h * 4, WS_SQ + (u32)((2 * st + wb) * 14 + 2 * dt + wa) * 256u, 16);
        asm volatile("" ::: "memory");
        sfor<0, 12>([&](auto I) { constexpr int i = I; *(LAS v4u*)(L + w * 21504 + i * 1024 + lane * 16) = wi[i]; });
        publish(p.ws, flag_off(0, 4, st * 8 + dt), tag);
        sfor<12, 21>([&](auto I) { constexpr int i = I; wi[i] = lda<i>(rws, lane * 16, wis); });
        sfor<12, 21>([&](auto I) { constexpr int i = I; vmw<20 - i>(wi[i]); *(LAS v4u*)(L + w * 21504 + i * 1024 + lane * 16) = wi[i]; });
        bar();
      } else {
        const rsrc_t rout = mkrs(p.out, S_ * D_ * 2);
        const u32 oo = (u32)((2 * st + wb) * 32 + r) * 896u + (u32)(dt * 64 + wa * 32 + 8 * h) * 2u;
        __builtin_amdgcn_raw_buffer_store_b128(xo[0], rout, oo, 0, 0);
        __builtin_amdgcn_raw_buffer_store_b128(xo[1], rout, oo + 32, 0, 0);
        if (b == 0 && tid == 0) __hip_atomic_store((GAS u64*)(p.ws + WS_EPOCH), tag, RLX_AGENT);
      }
      STAMP(16);
    } else {
      IDS
      const u32 wi_ = (u32)((bi - 28) * 4 + w);
      const rsrc_t r1 = mkrs(p.w1, S_ * F_ * 2), r2 = mkrs(p.w2, S_ * F_ * 2);
      const u32 v1 = ((lane >> 3) >> 1) * (512u * 3584u) + ((lane >> 3) & 1) * 3584u + ((lane & 7) < 7 ? (lane & 7) : 0) * 128u;
      const u32 v2 = ((lane >> 3) >> 1) * (448u * 4096u) + ((lane >> 3) & 1) * 4096u + (lane & 7) * 128u;
      for (u32 row = 192 + 2 * wi_; row < 512; row += 32) { const u32 d = touch(r1, v1, row * 3584u + (u32)pj * 896u); drain(); touch_done(d); }
      for (u32 row = 192 + 2 * wi_; row < 448; row += 32) { const u32 d = touch(r2, v2, row * 4096u + (u32)pj * 1024u); drain(); touch_done(d); }
      if (l == 0) {
        const Poll pq = wait_flags(p.ws, flag_off(0, 1, (lane >> 2) * 16 + head), tag);
        const u32 wis = WS_WI + (u32)head * 86016u + (u32)w * 21504u;
        v4u wi[21];
        sfor<0, 21>([&](auto I) { constexpr int i = I; wi[i] = lda<i>(rws, lane * 16, wis); });
        sfor<0, 21>([&](auto I) { constexpr int i = I; vmw<20 - i>(wi[i]); *(LAS v4u*)(L + w * 21504 + i * 1024 + lane * 16) = wi[i]; });
        poll_done(pq);
        bar();
        for (u32 i = wi_; i < 42; i += 16) { const u32 d = touch(rws, lane * 128, WS_WI + (u32)((g & 1) * 8) * 86016u + i * 8192u); drain(); touch_done(d); }
        for (u32 row = 2 * wi_; row < 192; row += 32) { const u32 d = touch(r2, v2, row * 4096u + (u32)pj * 1024u); drain(); touch_done(d); }
        for (u32 row = 2 * wi_; row < 192; row += 32) { const u32 d = touch(r1, v1, row * 3584u + (u32)pj * 896u); drain(); touch_done(d); }
      }
    }
  }
  asm volatile(".Lfwd_last:");
}

extern "C" void kernel_launch(void* const* d_in, const int* in_sizes, int n_in, void* d_out, int out_size, void* d_ws, size_t ws_size,
                              hipStream_t stream) {
  Params p;
  p.x = d_in[0]; p.wqkv = d_in[1]; p.wo = d_in[2]; p.w1 = d_in[4]; p.b1 = d_in[5]; p.w2 = d_in[6]; p.b2 = d_in[7];
  p.out = d_out; p.ws = (unsigned char*)d_ws;
  (void)hipFuncSetAttribute((const void*)fused_fwd, hipFuncAttributeMaxDynamicSharedMemorySize, L_END);
  hipLaunchKernelGGL(fused_fwd, dim3(256), dim3(256), L_END, stream, p);
}
```

```cpp
#include <hip/hip_runtime.h>
#include <hip/hip_bf16.h>
#include <type_traits>

typedef unsigned u32;
typedef unsigned long long u64;
typedef u32 v4u __attribute__((ext_vector_type(4)));
typedef u32 v2u __attribute__((ext_vector_type(2)));
typedef float f32x16 __attribute__((ext_vector_type(16)));
typedef float f32x4 __attribute__((ext_vector_type(4)));
typedef float f32x2 __attribute__((ext_vector_type(2)));
typedef __bf16 bf16x8 __attribute__((ext_vector_type(8)));
typedef __bf16 bf16x2 __attribute__((ext_vector_type(2)));
typedef short s16x4 __attribute__((ext_vector_type(4)));
#define LAS __attribute__((address_space(3)))
#define GAS __attribute__((address_space(1)))
#define DI __device__ __forceinline__
#define RLX_AGENT __ATOMIC_RELAXED, __HIP_MEMORY_SCOPE_AGENT
#define SB() __builtin_amdgcn_sched_barrier(0)
typedef __amdgpu_buffer_rsrc_t rsrc_t;
typedef LAS unsigned char* lptr;

#define DBG_STAMP 0
#define DBG_WAVE 0
#define POLL_GAP "3"
#define TOUCH 1
#define QP0 12
#define NB 10
#define CODE_TOUCH 1
#define TLBT 0
#define NOW 0
#define PA0 12
#define PA1 18
#define PB0 12
#define PC0 10
#define PC1 5
#define PD0 8
#define PD1 4
#define TB1 3
#define NODMA 0
#define SAFE_SUM 1.0e18f
#define POL 0
#define MAPX 1
#define SINGLE 0
#define GAP_LO 48u
#define QS 20
#define LA4 6
#define LA5 6
#define LA1 8
#define LA3 6
#define ROT4 0
#define ROT5 0
#define ROT1 (head & 1)
#define WARM2 2

constexpr int S_ = 2048, D_ = 448, F_ = 1792;
constexpr u32 WS_EPOCH = 0, WS_TMO = 128;
constexpr u32 WS_VOTE = 4096;
constexpr u32 WS_PROBE = 512u << 10;
constexpr u32 WS_FLAGS = 8192;
constexpr u32 WS_STATS = 1u << 20, STATS_L = 64 * 4096;
constexpr u32 WS_KV = 2u << 20, KV_L = 4u << 20;
constexpr u32 WS_OF = WS_KV + 2 * KV_L, OF_L = 2u << 20;
constexpr u32 WS_HF = WS_OF + 2 * OF_L, HF_L = 2u << 20;
constexpr u32 WS_UF = WS_HF + 2 * HF_L, UF_L = 2u << 20;
constexpr u32 WS_XF = WS_UF + 2 * UF_L;
constexpr u32 WS_WI = WS_XF + (2u << 20);
constexpr u32 WS_END = 2 * (WS_WI + (2u << 20));
constexpr u32 WS_DBG = WS_END;
constexpr u32 NOWHERE = 0x40000000u;
constexpr u32 L_RING = 0, RING_W = 24576;
constexpr u32 L_INBOX = 98304, INBOX_W = 12288;
constexpr u32 L_WQ = 57344;
constexpr u32 L_WO = 86016;
constexpr u32 L_CSP = 147456;
constexpr u32 L_CSF = L_CSP + 1536;
constexpr u32 L_MR = L_CSF + 2048;
constexpr u32 L_TR = L_MR + 1024;
constexpr u32 L_END = L_TR + 4 * 2304;
constexpr u32 kSpinCap = 1u << 16;
constexpr float EPS = 1e-5f;

struct Params {
  const void* x; const void* wqkv; const void* wo; const void* w1; const void* b1; const void* w2; const void* b2;
  void* out; unsigned char* ws;
};

DI f32x16 mfma(v4u a, v4u b, f32x16 c) {
  return __builtin_amdgcn_mfma_f32_32x32x16_bf16(__builtin_bit_cast(bf16x8, a), __builtin_bit_cast(bf16x8, b), c, 0, 0, 0);
}
DI f32x16 qk1(v4u k0, v4u q0, f32x16 ci) {
  f32x16 c;
  asm volatile("v_mfma_f32_32x32x16_bf16 %0, %1, %2, %3" : "=&v"(c) : "a"(k0), "v"(q0), "v"(ci));
  return c;
}
DI f32x16 qk1z(v4u k0, v4u q0) {
  f32x16 c;
  asm volatile("v_mfma_f32_32x32x16_bf16 %0, %1, %2, 0" : "=&v"(c) : "a"(k0), "v"(q0));
  return c;
}
DI void qk2(f32x16& c, v4u k1, v4u q1) { asm volatile("v_mfma_f32_32x32x16_bf16 %0, %1, %2, %0" : "+v"(c) : "a"(k1), "v"(q1)); }
DI f32x16 qk_own(v4u k0, v4u k1, v4u q0, v4u q1) {
  f32x16 c;
  asm volatile("s_nop 1\n\tv_mfma_f32_32x32x16_bf16 %0, %1, %3, 0\n\tv_mfma_f32_32x32x16_bf16 %0, %2, %4, %0" : "=&v"(c) : "v"(k0), "v"(k1), "v"(q0), "v"(q1));
  return c;
}
DI f32x16 qk_own_c(v4u k0, v4u k1, v4u q0, v4u q1, f32x16 ci) {
  f32x16 c;
  asm volatile("s_nop 1\n\tv_mfma_f32_32x32x16_bf16 %0, %1, %3, %5\n\tv_mfma_f32_32x32x16_bf16 %0, %2, %4, %0" : "=&v"(c) : "v"(k0), "v"(k1), "v"(q0), "v"(q1), "v"(ci));
  return c;
}
DI void tie(f32x16& c) { asm volatile("" : "+v"(c)); }
DI void tie_far(f32x16& c) { asm volatile("s_nop 7\n\ts_nop 4" : "+v"(c)); }
DI void nop2() { asm volatile("s_nop 1"); }
DI u32 pk(float lo, float hi) { f32x2 v = {lo, hi}; return __builtin_bit_cast(u32, __builtin_convertvector(v, bf16x2)); }
DI float asf(u32 v) { return __builtin_bit_cast(float, v); }
DI u32 asu(float v) { return __builtin_bit_cast(u32, v); }
DI bf16x2 asbf2(u32 v) { return __builtin_bit_cast(bf16x2, v); }
DI float blo(u32 v) { return __builtin_bit_cast(float, v << 16); }
DI float bhi(u32 v) { return __builtin_bit_cast(float, v & 0xffff0000u); }
DI void bar() { asm volatile("s_waitcnt lgkmcnt(0)\n\ts_barrier" ::: "memory"); }
DI void drain() { asm volatile("s_waitcnt vmcnt(0)" ::: "memory"); }
DI void lgk0() { asm volatile("s_waitcnt lgkmcnt(0)" ::: "memory"); }
template <u32 SI, u32 LI> DI void dma16(rsrc_t rs, u32 voff, u32 soff, u32 lds) {
  u32 t;
  asm volatile("s_add_u32 m0, %1, %5\n\ts_add_u32 %0, %4, %6\n\tbuffer_load_dwordx4 %2, %3, %0 offen lds"
               : "=&s"(t) : "s"(lds), "v"(voff), "s"(rs), "s"(soff), "i"(LI), "i"(SI) : "memory", "scc");
}
DI void dma_fake(rsrc_t rs, u32 voff, u32 lds) {
  asm volatile("s_mov_b32 m0, %0\n\ts_mov_b64 exec, 1\n\tbuffer_load_dword %1, %2, 0 offen lds\n\ts_mov_b64 exec, -1" :: "s"(lds), "v"(voff), "s"(rs) : "memory");
}
template <int K> DI v4u lda(rsrc_t rs, u32 voff, u32 soff) {
  v4u d; u32 t;
  asm volatile("s_add_u32 %1, %4, %5\n\tbuffer_load_dwordx4 %0, %2, %3, %1 offen offset:%6 sc1"
               : "=v"(d), "=&s"(t) : "v"(voff), "s"(rs), "s"(soff), "i"((K >> 3) * 16384 + ((K >> 2) & 1) * 4096), "i"((K & 3) * 1024) : "scc");
  return d;
}
template <int IMM> DI v4u ldp(rsrc_t rs, u32 voff, u32 soff) {
  v4u d;
  asm volatile("buffer_load_dwordx4 %0, %1, %2, %3 offen offset:%4" : "=v"(d) : "v"(voff), "s"(rs), "s"(soff), "i"(IMM));
  return d;
}
template <int IMM> DI v4u ldg(rsrc_t rs, u32 voff, u32 soff) {
  v4u d;
  asm volatile("buffer_load_dwordx4 %0, %1, %2, %3 offen offset:%4 sc1" : "=v"(d) : "v"(voff), "s"(rs), "s"(soff), "i"(IMM));
  return d;
}
template <int IMM> DI v2u ldp2(rsrc_t rs, u32 voff) {
  v2u d;
  asm volatile("buffer_load_dwordx2 %0, %1, %2, 0 offen offset:%3" : "=v"(d) : "v"(voff), "s"(rs), "i"(IMM));
  return d;
}
DI float mul1(float a, float b) { float d; asm volatile("v_mul_f32 %0, %1, %2" : "=v"(d) : "v"(a), "v"(b)); return d; }
DI void pin7(v2u (&v)[7]) { asm volatile("" : "+v"(v[0]), "+v"(v[1]), "+v"(v[2]), "+v"(v[3]), "+v"(v[4]), "+v"(v[5]), "+v"(v[6])); }
DI void pin4(float (&v)[4]) { asm volatile("" : "+v"(v[0]), "+v"(v[1]), "+v"(v[2]), "+v"(v[3])); }
template <int IMM> DI v2u lda2(rsrc_t rs, u32 voff, u32 soff) {
  v2u d;
  asm volatile("buffer_load_dwordx2 %0, %1, %2, %3 offen offset:%4 sc1" : "=v"(d) : "v"(voff), "s"(rs), "s"(soff), "i"(IMM));
  return d;
}
template <int N> DI void vmw0() { asm volatile("s_waitcnt vmcnt(%0)" ::"i"(N) : "memory"); }
template <int N> DI void vmw(v4u& a) { asm volatile("s_waitcnt vmcnt(%1)" : "+v"(a) : "i"(N)); }
template <int N> DI void vmw(v4u& a, v4u& b, v4u& c, v4u& d) { asm volatile("s_waitcnt vmcnt(%4)" : "+v"(a), "+v"(b), "+v"(c), "+v"(d) : "i"(N)); }
template <int N> DI void vmw(v4u& a, v4u& b) { asm volatile("s_waitcnt vmcnt(%2)" : "+v"(a), "+v"(b) : "i"(N)); }
template <int I, int N, class Fn> DI void sfor(Fn&& f) { if constexpr (I < N) { f(std::integral_constant<int, I>{}); sfor<I + 1, N>(f); } }
DI u32 opq(u32 v) { v = __builtin_amdgcn_readfirstlane(v); asm volatile("" : "+s"(v)); return v; }
DI u32 opqv(u32 v) { asm volatile("" : "+v"(v)); return v; }
DI v2u trd(lptr p) { return __builtin_bit_cast(v2u, __builtin_amdgcn_ds_read_tr16_b64_v4i16((LAS s16x4*)p)); }
DI v4u trd2(lptr p0, lptr p1) { v2u a = trd(p0), b = trd(p1); return v4u{a.x, a.y, b.x, b.y}; }
DI v4u ld_sc1(rsrc_t rs, u32 voff, u32 soff) { return __builtin_amdgcn_raw_buffer_load_b128(rs, voff, soff, 16); }
DI u32 ph(u32 o) { return o + (o & ~8191u); }
DI u32 rd(u32 o, u32 nb) { const u32 q = ph(o); return q | ((((q >> 21) ^ nb) & 1u) << 13); }
DI void st_sc1(v4u v, rsrc_t rs, u32 voff, u32 soff) { __builtin_amdgcn_raw_buffer_store_b128(v, rs, voff, soff, 16); }
DI void st2(v4u v, rsrc_t rs, u32 voff, u32 q) { st_sc1(v, rs, voff, q); st_sc1(v, rs, voff, q + 8192u); }
DI rsrc_t mkrs(const void* p, u32 bytes) {
  u32 fl = 0x00020000;
  asm volatile("" : "+s"(p), "+s"(bytes), "+s"(fl));
  return __builtin_amdgcn_make_buffer_rsrc((void*)p, 0, bytes, fl);
}
DI float half_add(float v) {
  auto r = __builtin_amdgcn_permlane32_swap(asu(v), asu(v), false, false);
  const u32 a = r[0], b = r[1];
  return asf(a) + asf(b);
}
DI float half_max(float v) {
  auto r = __builtin_amdgcn_permlane32_swap(asu(v), asu(v), false, false);
  const u32 a = r[0], b = r[1];
  return fmaxf(asf(a), asf(b));
}
DI bool spin_out(unsigned char* ws, u32& spins, u32 tl) {
  ++spins;
  if ((spins & 63u) == 0u) {
    GAS u32* tmo = (GAS u32*)(ws + WS_TMO);
    if (__hip_atomic_load(tmo, RLX_AGENT) == tl) return true;
    if (spins > kSpinCap) { __hip_atomic_store(tmo, tl, RLX_AGENT); return true; }
  }
  return false;
}
struct Poll { v2u a, b, c; };
#define PL(R, O, B) "global_load_dwordx2 " R ", " O ", " B " sc1\n\t"
DI v2u poll_one(unsigned char* ws, u32 off) {
  v2u r;
  asm volatile(PL("%0", "%1", "%2") : "=v"(r) : "v"(off), "s"(ws) : "memory");
  return r;
}
DI void poll_finish(Poll& q, unsigned char* ws, u32 off, u64 tag) {
  u32 left;
#define PC(R) "s_waitcnt vmcnt(2)\n\tv_cmp_ne_u64 vcc, %6, " R "\n\ts_cbranch_vccz 2f\n\t" PL(R, "%4", "%5")
  asm volatile("s_mov_b32 %3, 0x8000\n1:\n\t" PC("%0") PC("%1") PC("%2") "s_sub_u32 %3, %3, 1\n\ts_cbranch_scc0 1b\n2:"
               : "+v"(q.a), "+v"(q.b), "+v"(q.c), "=&s"(left) : "v"(off), "s"(ws), "s"(tag) : "vcc", "scc", "memory");
#undef PC
  if (left == 0xffffffffu) { drain(); __hip_atomic_store((GAS u32*)(ws + WS_TMO), (u32)tag, RLX_AGENT); }
}
template <int CA, int CB, int CC> DI void poll_finish_mixed(Poll& q, unsigned char* ws, u32 off, u64 tag) {
  u32 left;
#define PE(R, N) "s_waitcnt vmcnt(" N ")\n\tv_cmp_ne_u64 vcc, %6, " R "\n\ts_cbranch_vccz 2f\n\t"
#define PC(R) "s_waitcnt vmcnt(2)\n\tv_cmp_ne_u64 vcc, %6, " R "\n\ts_cbranch_vccz 2f\n\t" PL(R, "%4", "%5")
  asm volatile("s_mov_b32 %3, 0x8000\n\t" PE("%0", "%7") PL("%0", "%4", "%5") PE("%1", "%8") PL("%1", "%4", "%5") PE("%2", "%9") PL("%2", "%4", "%5")
               "1:\n\t" PC("%0") PC("%1") PC("%2") "s_sub_u32 %3, %3, 1\n\ts_cbranch_scc0 1b\n2:"
               : "+v"(q.a), "+v"(q.b), "+v"(q.c), "=&s"(left) : "v"(off), "s"(ws), "s"(tag), "i"(CA), "i"(CB + 1), "i"(CC + 2) : "vcc", "scc", "memory");
#undef PE
#undef PC
  if (left == 0xffffffffu) { drain(); __hip_atomic_store((GAS u32*)(ws + WS_TMO), (u32)tag, RLX_AGENT); }
}
DI Poll wait_flags(unsigned char* ws, u32 off, u64 tag) {
  Poll q;
  q.a = poll_one(ws, off); asm volatile("s_sleep " POLL_GAP);
  q.b = poll_one(ws, off); asm volatile("s_sleep " POLL_GAP);
  q.c = poll_one(ws, off);
  poll_finish(q, ws, off, tag);
  return q;
}
DI void poll_done(const Poll& q) { asm volatile("" ::"v"(q.a), "v"(q.b), "v"(q.c)); }
DI u32 touch(rsrc_t rs, u32 voff, u32 soff) {
  u32 d;
  asm volatile("s_nop 4\n\tbuffer_load_dword %0, %1, %2, %3 offen" : "=v"(d) : "v"(voff), "s"(rs), "s"(soff));
  return d;
}
DI void touch_done(u32 d) { asm volatile("" ::"v"(d)); }
DI void publish(unsigned char* ws, u32 off, u64 tag) {
  asm volatile("s_waitcnt vmcnt(0) lgkmcnt(0)\n\ts_barrier" ::: "memory");
  if (threadIdx.x < 2) __hip_atomic_store((GAS u64*)(ws + (ph(off) + threadIdx.x * 8192u)), tag, RLX_AGENT);
  asm volatile("" ::: "memory");
}
DI u32 flag_off(int l, int type, int idx) { return WS_FLAGS + (u32)(((l * 5 + type) << 8) + idx) * 128u; }

#define STAMP(i) do {} while (0)

DI void red_put(lptr L, const f32x16& a, int q, int i, int w, int lane) {
  *(LAS f32x4*)(L + L_INBOX + (u32)(w ^ q) * INBOX_W + (q - 1) * 4096 + lane * 16 + i * 1024) = f32x4{a[4 * i], a[4 * i + 1], a[4 * i + 2], a[4 * i + 3]};
}
DI f32x16 red_get(lptr L, const f32x16& own, int w, int lane) {
  bar();
  f32x16 r = own;
  lptr src = L + L_INBOX + (u32)w * INBOX_W + lane * 16;
#pragma unroll
  for (int q = 0; q < 3; ++q)
#pragma unroll
    for (int i = 0; i < 4; ++i) {
      const f32x4 v = *(LAS f32x4*)(src + q * 4096 + i * 1024);
      r[4 * i] += v[0]; r[4 * i + 1] += v[1]; r[4 * i + 2] += v[2]; r[4 * i + 3] += v[3];
    }
  return r;
}
template <int P0, int P1, u32 RS> DI void w_issue(rsrc_t rs, u32 voff, u32 soff, u32 ring) {
  sfor<P0, P1>([&](auto I) { constexpr int i = I; dma16<i * 8 * RS, (i % 24) * 1024>(rs, voff, soff, ring); });
}
template <int P0, int P1, u32 RS, int HP> DI void w_issue2(rsrc_t rs, u32 voff, u32 s0, u32 s1, u32 ring) {
  sfor<P0, P1>([&](auto I) { constexpr int i = I; dma16<(i % HP) * 8 * RS, (i % 24) * 1024>(rs, voff, i < HP ? s0 : s1, ring); });
}
template <int K0, int K1, int TSTEP, int NR> DI void w_take(lptr t0, lptr t1, v4u (&bw)[NR][2]) {
#pragma unroll
  for (int kk = K0; kk < K1; ++kk) {
    const int sl = (kk % 12) * 2048;
    bw[kk][0] = trd2(t0 + sl, t0 + sl + TSTEP);
    bw[kk][1] = trd2(t1 + sl, t1 + sl + TSTEP);
  }
}
template <int NR> DI void w_undef(v4u (&bw)[NR][2], int k0, int k1) {
#pragma unroll
  for (int kk = 0; kk < NR; ++kk) if (kk >= k0 && kk < k1) { asm volatile("" : "=v"(bw[kk][0])); asm volatile("" : "=v"(bw[kk][1])); }
}

template <int NK, int NR, int TSTEP, bool WA, int LS, int Z, bool TAIL, class Tail>
DI f32x16 gemm_main(lptr L, int w, int lane, rsrc_t ws, u32 a0, u32 a1, lptr t0, lptr t1, const v4u (&bw)[NR][2], f32x16 init, const Poll& pq, Tail&& tail) {
  constexpr bool RING = NR < NK;
  constexpr int NS = 2 * NK, LAG = 3;
  static_assert(!RING || (NK - NR == 12 && NS - LS >= 30 && NK - Z >= 14), "ring is 12 slots and must be consumed before the tail");
  const u32 voff = lane * 16;
  v4u a[NS], wl[12][2];
  f32x16 acc[4];
#define KOF(n) (!RING ? (n) : (n) < 2 ? (n) : (n) < 14 ? NR + (n) - 2 : (n) - 12)
#define SN(j) ((j) < NS - 2 * Z ? (j) / 2 : (j) < NS - Z ? (j) - (NK - Z) : (j) - NK)
#define SH(j) ((j) < NS - 2 * Z ? (j) & 1 : (j) < NS - Z ? 1 : 0)
  auto ld = [&](auto J) { constexpr int j = J; a[j] = lda<KOF(SN(j))>(ws, voff, SH(j) ? a1 : a0); };
  sfor<0, LS>(ld);
  acc[0] = init;
#pragma unroll
  for (int q = 1; q < 4; ++q)
#pragma unroll
    for (int i = 0; i < 16; ++i) acc[q][i] = 0.f;
  auto mm = [&](auto J, auto WI) {
    constexpr int j = J, wi = WI, n = SN(j), q = WA ? 2 * wi + SH(j) : 2 * SH(j) + wi;
    v4u wv;
    if constexpr (RING && n >= 2 && n < 14) wv = wl[n - 2][wi]; else wv = bw[KOF(n)][wi];
    acc[q] = WA ? mfma(wv, a[j], acc[q]) : mfma(a[j], wv, acc[q]);
  };
  using I0 = std::integral_constant<int, 0>; using I1 = std::integral_constant<int, 1>;
  constexpr int QE = WA ? 1 : 2, QL = WA ? 2 : 1;
  SB();
  sfor<0, NS>([&](auto J) {
    constexpr int j = J, e = j - (NS - Z);
    asm volatile("" ::: "memory");
    vmw<(TAIL || j + LS <= NS) ? LS - 1 : NS - 1 - j>(a[j]);
    asm volatile("" ::: "memory");
    if constexpr (j == 0) poll_done(pq);
    mm(J, I1{});
    SB();
    if constexpr (j + LS < NS) ld(std::integral_constant<int, j + LS>{}); else if constexpr (TAIL) tail(std::integral_constant<int, j + LS - NS>{});
    if constexpr (e >= 1 && e <= 4 && Z >= 5) red_put(L, acc[3], 3, e - 1, w, lane);
    if constexpr (e >= 1 && e <= 2 && Z < 5) { red_put(L, acc[3], 3, 2 * e - 2, w, lane); red_put(L, acc[3], 3, 2 * e - 1, w, lane); }
    SB();
    if constexpr (j < NS - LAG) mm(J, I0{});
    SB();
    if constexpr (RING && j < 24) { constexpr int sl = ((NR + j / 2) % 12) * 2048; lptr t = (j & 1) ? t1 : t0; wl[j / 2][j & 1] = trd2(t + sl, t + sl + TSTEP); }
    if constexpr (e >= 1 && e <= 4 && Z >= 5) red_put(L, acc[QE], QE, e - 1, w, lane);
    if constexpr (e >= 1 && e <= 2 && Z < 5) { red_put(L, acc[QE], QE, 2 * e - 2, w, lane); red_put(L, acc[QE], QE, 2 * e - 1, w, lane); }
    SB();
  });
  sfor<NS - LAG, NS>([&](auto J) { mm(J, I0{}); });
  SB();
#pragma unroll
  for (int i = 0; i < 4; ++i) red_put(L, acc[QL], QL, i, w, lane);
#undef KOF
#undef SN
#undef SH
  return red_get(L, acc[0], w, lane);
}

__global__ __launch_bounds__(256, 1) void fused_fwd(Params p) {
  extern __shared__ __attribute__((aligned(16))) unsigned char smem[];
  lptr L = (lptr)smem;
  const u32 lds0 = (u32)(size_t)L;
#define IDS \
  const int tid = (int)opqv(threadIdx.x), lane = tid & 63, r = lane & 31, h = lane >> 5; \
  const int w = (int)opq(w_), b = (int)opq(blockIdx.x), g = b & 7, bi = b >> 3, pj = g >> 1; \
  const int T = 4 * pj + (bi >> 3), head = (g & 1) * 8 + (bi & 7); \
  const u32 pk_ = opq(tiles_); \
  const int st = pk_ & 31, dt = (pk_ >> 8) & 7, ft = (pk_ >> 16) & 31, dt4 = pk_ >> 24; \
  const int wa = w >> 1, wb = w & 1; \
  const int ch16 = (lane >> 4) & 1, tq = (lane & 15) >> 2, tp = lane & 3; \
  const u32 ringw = L_RING + (u32)w * RING_W; \
  const u32 dmarow = lane >> 3, dmach = ((lane & 7) ^ (((lane >> 4) & 1) << 2)) * 16; \
  const u32 sw = (u32)((tq >> 1) & 1) << 2, cpart = 2 * ch16 + (tp >> 1); \
  (void)r; (void)h; (void)T; (void)head; (void)st; (void)dt; (void)ft; (void)dt4; (void)wa; (void)wb; (void)ch16; (void)tq; (void)tp; (void)ringw; (void)dmarow; (void)dmach; (void)sw; (void)cpart;
  const u32 w_ = __builtin_amdgcn_readfirstlane(threadIdx.x >> 6);
  u32 tiles_;
  {
    const u32 g = blockIdx.x & 7, bi = blockIdx.x >> 3, pj = g >> 1, d3 = (bi * 43) >> 7, m3 = bi - 3 * d3;
    const u32 d7 = (bi * 37) >> 8, m7 = bi - 7 * d7;
    const u32 st = 4 * g + (d7 & 3), dt = m7, ft = 7 * pj + 4 * (g & 1) + (d7 & 3), dt4 = m7; (void)m3;
    tiles_ = opq(st | (dt << 8) | (ft << 16) | (dt4 << 24));
  }
  const bool p35 = (blockIdx.x >> 3) < 28;
  const bool p4 = (blockIdx.x >> 3) < (MAPX ? ((blockIdx.x & 1) ? 21u : 28u) : ((blockIdx.x & 1) ? 24u : 25u));

  unsigned char* const wsa = (unsigned char*)(((size_t)p.ws + 0x1FFFFF) & ~(size_t)0x1FFFFF);
  const rsrc_t rws = mkrs(wsa, WS_END);
  const u32 half_ = (__builtin_amdgcn_s_getreg((3 << 11) | 20) >> 2) & 1u;
  u32 nb = w_ & 1;
  u64 tag = 0;
  u32 xres[8];
  v4u wf[8][2];
#pragma unroll
  for (int i = 0; i < 8; ++i) { xres[i] = 0; asm volatile("" : "=a"(wf[i][0])); asm volatile("" : "=a"(wf[i][1])); }

#pragma unroll 1
  for (int l = 0; l < 2; ++l) {
    STAMP(0);
    const rsrc_t rw1 = mkrs(p.w1, (p4 && !NOW) ? S_ * F_ * 2 : 0);
    const rsrc_t rw2 = mkrs(p.w2, (p35 && !NOW) ? F_ * S_ * 2 : 0);
    v4u bw4[20][2], bw5[16][2];
#define T5_PTRS \
    lptr t0 = L + ringw + (4 * h + tq) * 128 + (((4 * wb + cpart) ^ sw) * 16) + (tp & 1) * 8; \
    lptr t1 = L + ringw + (4 * h + tq) * 128 + (((4 * (wb ^ 1) + cpart) ^ sw) * 16) + (tp & 1) * 8;
#define T4_PTRS \
    lptr n0 = L + ringw + (8 * h + tq) * 128 + (((4 * wa + cpart) ^ sw) * 16) + (tp & 1) * 8; \
    lptr n1 = L + ringw + (8 * h + tq) * 128 + (((4 * (wa ^ 1) + cpart) ^ sw) * 16) + (tp & 1) * 8;
#define W2_ARGS rw2, dmarow * 4096u + dmach + (u32)st * 128u, (u32)(448 * w + 224 * ROT5) * 4096u, (u32)(448 * w + 224 * (ROT5 ^ 1)) * 4096u, lds0 + ringw
#define W1_ARGS rw1, dmarow * 3584u + dmach + (u32)ft * 128u, (u32)(512 * w + 256 * ROT4) * 3584u, (u32)(512 * w + 256 * (ROT4 ^ 1)) * 3584u, lds0 + ringw
    {
    IDS
    const int tb = 4 * T + w;
    const u32 shift = (head & 1) * 8;
    const u32 half = (((tq >> 1) & 1) ^ 1) * 64, col = shift + ch16 * 32 + tp * 8;
    lptr wtk = L + (32 * h + tq) * 128 + half + col, wtv = L + (32 * h + tq) * 128 + (half ^ 64) + col, wtq = L + L_WQ + (32 * h + tq) * 64 + col;
    v4u X[28];
    f32x16 kt, vv, rr, ck_, cv_;
#pragma unroll
    for (int i = 0; i < 16; ++i) { kt[i] = 0.f; vv[i] = 0.f; }
    float mean, rstd, nmr, csv;
    constexpr int LN0 = LA1 + 5;
    static_assert(LN0 + 6 <= 20, "order of the stages");
    auto ln_part = [&](auto I, float sum, float ssq) {
      constexpr int k = I;
      if constexpr (k == LN0) {
        mean = half_add(sum) * (1.f / 448.f);
        rstd = __builtin_amdgcn_rsqf(fmaxf(half_add(ssq) * (1.f / 448.f) - mean * mean, 0.f) + EPS);
        nmr = -mean * rstd;
        *(LAS float*)(L + L_MR + w * 256 + lane * 4) = h ? nmr : rstd;
        csv = *(LAS float*)(L + L_CSF + w * 512 + 256 + r * 4);
      }
      if constexpr (k >= LN0 + 2 && k < LN0 + 6) {
        constexpr int i = k - LN0 - 2;
        const f32x4 cs = *(LAS f32x4*)(L + L_CSF + w * 512 + 128 + (8 * i + 4 * h) * 4);
        const f32x4 r4 = *(LAS f32x4*)(L + L_MR + w * 256 + (8 * i + 4 * h) * 4);
        const f32x4 m4 = *(LAS f32x4*)(L + L_MR + w * 256 + 128 + (8 * i + 4 * h) * 4);
#pragma unroll
        for (int e = 0; e < 4; ++e) { ck_[4 * i + e] = cs[e]; cv_[4 * i + e] = m4[e]; rr[4 * i + e] = r4[e]; }
      }
      if constexpr (k >= 20) {
        constexpr int i = 2 * (k - 20);
        ck_[i] = mul1(ck_[i], nmr); cv_[i] = mul1(cv_[i], csv); ck_[i + 1] = mul1(ck_[i + 1], nmr); cv_[i + 1] = mul1(cv_[i + 1], csv);
      }
    };
    u32 tc2 = 0, tc3 = 0, tc4 = 0, t4v = 0;
    v2u xr2[4] = {v2u{xres[0], xres[1]}, v2u{xres[2], xres[3]}, v2u{xres[4], xres[5]}, v2u{xres[6], xres[7]}};
    v4u wk[28], wv[28], wq[28];
    constexpr int PD = 3;
#define WROW(k) (((k) >> 2) * 64 + ((k) & 3) * 8)
#define WFK(k) trd2(wtk + WROW(k) * 128, wtk + WROW(k) * 128 + 512)
#define WFV(k) trd2(wtv + WROW(k) * 128, wtv + WROW(k) * 128 + 512)
#define WFQ(k) trd2(wtq + WROW(k) * 64, wtq + WROW(k) * 64 + 256)
    auto kv_head = [&]() {
#pragma unroll
      for (int k = 0; k < PD; ++k) { wk[k] = WFK(k); wv[k] = WFV(k); }
      SB();
    };
    auto kv_step = [&](auto I, auto&& mem) {
      constexpr int k = I;
      kt = mfma(wk[k], X[k], kt);
      SB();
      if constexpr (k + PD < 28) wk[k + PD] = WFK(k + PD); else wq[2 * (k + PD - 28)] = WFQ(2 * (k + PD - 28));
      mem();
      SB();
      vv = mfma(X[k], wv[k], vv);
      SB();
      if constexpr (k + PD < 28) wv[k + PD] = WFV(k + PD); else wq[2 * (k + PD - 28) + 1] = WFQ(2 * (k + PD - 28) + 1);
      SB();
    };
    const u32 gw = (u32)(b * 4 + w);
    auto touch_w2a = [&]() { const u32 n = gw * 24 + (lane < 24 ? lane : 0), rr_ = n >> 5;
      return touch(mkrs(p.w2, TOUCH ? S_ * F_ * 2 : 0), ((rr_ / 192) * 448 + rr_ % 192) * 4096u + (n & 31) * 128u, 0); };
    auto touch_w1a = [&]() { const u32 n = gw * 21 + (lane < 21 ? lane : 0), rr_ = n / 28;
      return touch(mkrs(p.w1, TOUCH ? S_ * F_ * 2 : 0), ((rr_ / 192) * 512 + rr_ % 192) * 3584u + (n % 28) * 128u, 0); };
    const v4u ones = {0x3F803F80u, 0x3F803F80u, 0x3F803F80u, 0x3F803F80u};
    if (l == 0) {
      v2u e0;
      asm volatile("global_load_dwordx2 %0, %1, %2 sc1" : "=v"(e0) : "v"(WS_EPOCH), "s"(wsa));
      u32 tcc;
      {
        u64 pc; u32 back, len;
        asm volatile("s_getpc_b64 %0\n.Lfwd_here:\n\ts_mov_b32 %1, .Lfwd_here-_Z9fused_fwd6Params\n\ts_mov_b32 %2, .Lfwd_last-_Z9fused_fwd6Params" : "=s"(pc), "=s"(back), "=s"(len));
        tcc = touch(mkrs((const void*)(pc - back), CODE_TOUCH ? len : 0), lane * 128, (u32)(bi * 4 + w) * 8192u);
      }
      const rsrc_t rx = mkrs(p.x, S_ * D_ * 2), rwq = mkrs(p.wqkv, D_ * 3 * D_ * 2);
      const u32 hoff = (u32)(head * 56) - shift;
      const u32 xv = dmarow * 896u + (lane & 7) * 16u, xs = (u32)(tb * 32) * 896u;
      lptr xb = L + L_WO + w * 15360;
      lptr xw = xb + dmarow * 128, xr = xb + r * 128;
      const u32 xsw = (r >> 1) & 7, xc0 = ((lane & 7) ^ (lane >> 4)) * 16;
      const u32 kvv = dmarow * 2688u + 896u + ((((lane >> 2) ^ (lane >> 4) ^ 1) & 1) * 896u) + (lane & 3) * 16u + hoff;
      const u32 kvs = (u32)(112 * w) * 2688u, kvl0 = lds0 + (u32)(14 * w) * 1024u;
      const rsrc_t rwo = mkrs(p.wo, D_ * D_ * 2);
      const u32 wov = dmarow * 896u + dmach + (u32)dt * 128u, wos = (u32)(112 * w) * 896u, wol = lds0 + L_WO + (u32)w * 15360u;
      const u32 qv = (u32)(lane >> 2) * 2688u + (lane & 3) * 16u + hoff, ql = lds0 + L_WQ + (u32)(7 * w) * 1024u;
      sfor<0, 28>([&](auto I) { constexpr int k = I; X[k] = ldp<(k >> 2) * 128>(rx, xv, xs + (k & 3) * 7168u); });
      sfor<0, 7>([&](auto I) { constexpr int i = I; dma16<i * 8 * 2688, i * 1024>(rwq, kvv, kvs, kvl0); });
      STAMP(24);
      float sq4[4] = {0.f, 0.f, 0.f, 0.f};
      sfor<0, 7>([&](auto I) {
        constexpr int G = I;
        vmw<24 - 4 * G + 7 + 2 * G>(X[4 * G], X[4 * G + 1], X[4 * G + 2], X[4 * G + 3]);
#pragma unroll
        for (int i = 0; i < 4; ++i) *(LAS v4u*)(xw + i * 1024 + (xc0 ^ ((i & 1) * 64))) = X[4 * G + i];
#pragma unroll
        for (int c = 0; c < 4; ++c) X[4 * G + c] = *(LAS v4u*)(xr + (((4 * h + c) ^ xsw) * 16));
        if constexpr (2 * G < 7) dma16<(7 + 2 * G) * 8 * 2688, (7 + 2 * G) * 1024>(rwq, kvv, kvs, kvl0); else dma16<(2 * G - 7) * 16 * 2688, (2 * G - 7) * 1024>(rwq, qv, kvs, ql);
        if constexpr (2 * G + 1 < 7) dma16<(8 + 2 * G) * 8 * 2688, (8 + 2 * G) * 1024>(rwq, kvv, kvs, kvl0); else dma16<(2 * G - 6) * 16 * 2688, (2 * G - 6) * 1024>(rwq, qv, kvs, ql);
#pragma unroll
        for (int i = 0; i < 16; ++i) {
          const bf16x2 xx = asbf2(X[4 * G + (i >> 2)][i & 3]);
          sq4[i & 1] = __builtin_amdgcn_fdot2_f32_bf16(xx, xx, sq4[i & 1], false);
          sq4[2 + (i & 1)] = __builtin_amdgcn_fdot2_f32_bf16(xx, asbf2(0x3F803F80u), sq4[2 + (i & 1)], false);
        }
        pin4(sq4);
        SB();
      });
      STAMP(25);
      f32x16 ck, cv;
#pragma unroll
      for (int i = 0; i < 16; ++i) { ck[i] = 0.f; cv[i] = 0.f; }
      lptr ck0 = L + (112 * w + 8 * h + tq) * 128 + half + col, cv0 = L + (112 * w + 8 * h + tq) * 128 + (half ^ 64) + col;
      vmw0<7>();
      STAMP(26);
      {
        v4u f[14];
#pragma unroll
        for (int i = 0; i < 7; ++i) { f[2 * i] = trd2(ck0 + i * 2048, ck0 + i * 2048 + 512); f[2 * i + 1] = trd2(cv0 + i * 2048, cv0 + i * 2048 + 512); }
        SB();
        sfor<0, 7>([&](auto I) { constexpr int i = I;
          ck = mfma(ones, f[2 * i], ck); SB(); w_issue<2 * i, 2 * i + 1, 896u>(rwo, wov, wos, wol); SB();
          cv = mfma(ones, f[2 * i + 1], cv); SB(); w_issue<2 * i + 1, 2 * i + 2, 896u>(rwo, wov, wos, wol); SB(); });
        w_issue<14, 15, 896u>(rwo, wov, wos, wol);
      }
      {
        lptr cq0 = L + L_WQ + (112 * w + 8 * h + tq) * 64 + col;
        v4u f[7];
        vmw0<15>();
#pragma unroll
        for (int i = 0; i < 7; ++i) f[i] = trd2(cq0 + i * 1024, cq0 + i * 1024 + 256);
        tc2 = touch_w2a(); tc3 = touch_w1a();
        *(LAS float*)(L + L_CSP + w * 384 + 128 + lane * 4) = h ? cv[0] : ck[0];
        SB();
#pragma unroll
        for (int i = 0; i < 16; ++i) ck[i] = 0.f;
#pragma unroll
        for (int i = 0; i < 7; ++i) ck = mfma(ones, f[i], ck);
        touch_done(tcc);
        asm volatile("" : "+v"(e0));
        tag = (((u64)(u32)__builtin_amdgcn_readfirstlane(e0.y) << 32) | (u64)(u32)__builtin_amdgcn_readfirstlane(e0.x)) + 1ull;
        { const u32 n = gw * 35 + (lane < 35 ? lane : 0), rr_ = n / 28; t4v = opqv(((rr_ / 320) * 512 + 192 + rr_ % 320) * 3584u + (n % 28) * 128u); }
        *(LAS float*)(L + L_CSP + w * 384 + r * 4) = ck[0];
      }
      STAMP(27);
      bar();
      kv_head();
      const float su0 = sq4[2] + sq4[3], sq0 = sq4[0] + sq4[1];
      lptr t0 = L + L_WO + w * 15360 + (4 * h + tq) * 128 + (((4 * wa + cpart) ^ sw) * 16) + (tp & 1) * 8;
      lptr t1 = L + L_WO + w * 15360 + (4 * h + tq) * 128 + (((4 * (wa ^ 1) + cpart) ^ sw) * 16) + (tp & 1) * 8;
      const u32 wib = WS_WI + (u32)head * 98304u;
      const u32 xo = (u32)((2 * st + wb) * 32 + r) * 896u + (u32)(dt * 64 + wa * 32 + 4 * h) * 2u;
      float cp[8];
      v4u img[2];
      STAMP(1);
      SB();
      sfor<0, 28>([&](auto I) { constexpr int k = I; kv_step(I, [&]() {
        if constexpr (k < 4) { cp[k] = *(LAS float*)(L + L_CSP + k * 384 + 128 + lane * 4); cp[4 + k] = *(LAS float*)(L + L_CSP + k * 384 + r * 4); }
        if constexpr (k == 4) { img[0] = *(LAS v4u*)(L + tb * 1024 + lane * 16); img[1] = *(LAS v4u*)(L + (tb < 20 ? tb + 64 : tb) * 1024 + lane * 16); }
        if constexpr (k == 5) *(LAS float*)(L + L_CSF + w * 512 + 128 + lane * 4) = (cp[0] + cp[1]) + (cp[2] + cp[3]);
        if constexpr (k == 6) *(LAS float*)(L + L_CSF + w * 512 + r * 4) = (cp[4] + cp[5]) + (cp[6] + cp[7]);
        if constexpr (k == 7) st2(img[0], rws, lane * 16, ph(wib + (u32)(tb + 3 * (tb / 21)) * 1024u));
        if constexpr (k == 8) st2(img[1], rws, lane * 16, tb < 20 ? ph(wib + (u32)(tb + 73) * 1024u) : NOWHERE);
        if constexpr (k == 9) st2(v4u{0u, 0u, 0u, 0u}, rws, head < 7 ? h * (2 * STATS_L) + r * 16 : NOWHERE, ph(WS_STATS + (u32)(tb * 4096 + head * 512)));
        if constexpr (k == 10 || k == 11) { xr2[2 * k - 20] = ldp2<(2 * k - 20) * 16>(rx, xo); xr2[2 * k - 19] = ldp2<(2 * k - 19) * 16>(rx, xo); }
        if constexpr (k == 12) vmw0<12>();
        if constexpr (k >= 12) { constexpr int i = (k - 12) >> 1, ro = (28 * (i >> 1) + 16 * (i & 1)) * 128; lptr t = (k & 1) ? t1 : t0; wf[i][k & 1] = trd2(t + ro, t + ro + 1024); }
        ln_part(I, su0, sq0);
      }); });
      asm volatile("" : "+a"(kt), "+a"(vv));
    } else {
      kv_head();
      const Poll pq = wait_flags(wsa, rd(flag_off(0, 4, (2 * T + (w >> 1)) * 8 + (lane < 56 ? lane >> 3 : 6)), nb), tag);
      STAMP(1);
      const u32 xb = ph(WS_XF + (u32)tb * 32768u);
      const u32 sqv = (u32)h * 2048u + r * 16, sqs = rd(WS_XF + (u32)tb * 32768u + 28672u, nb);
      v4u sp[4];
      sfor<0, LA1>([&](auto I) { constexpr int k = I; X[k] = lda<k>(rws, lane * 16, xb); });
      float su1 = 0.f, sq1 = 0.f;
      sfor<0, 28>([&](auto I) {
        constexpr int k = I;
        constexpr int spy = k < LA1 ? (k < 4 ? k : 4) : (k - LA1 < 4 ? 4 - (k - LA1) : 0);
        vmw<((k + LA1 <= 28) ? LA1 - 1 : 27 - k) + spy>(X[k]);
        if constexpr (k == 0) poll_done(pq);
        if constexpr (k == LA1 + 4) vmw<LA1 - 1>(sp[0], sp[1], sp[2], sp[3]);
        kv_step(I, [&]() {
          if constexpr (k + LA1 < 28) X[k + LA1] = lda<k + LA1>(rws, lane * 16, xb);
          if constexpr (k < 3) sp[k] = ldg<k * 512>(rws, sqv, sqs);
          if constexpr (k == 3) sp[3] = ldg<1536>(rws, h ? NOWHERE : sqv, sqs);
          if constexpr (k == LA1 + 4) {
            su1 = ((asf(sp[0].x) + asf(sp[0].z)) + (asf(sp[1].x) + asf(sp[1].z))) + ((asf(sp[2].x) + asf(sp[2].z)) + (asf(sp[3].x) + asf(sp[3].z)));
            sq1 = ((asf(sp[0].y) + asf(sp[0].w)) + (asf(sp[1].y) + asf(sp[1].w))) + ((asf(sp[2].y) + asf(sp[2].w)) + (asf(sp[3].y) + asf(sp[3].w)));
          }
          ln_part(I, su1, sq1);
        });
      });
      asm volatile("" : "+a"(kt), "+a"(vv));
    }
    STAMP(18);
    const u32 kvbase = WS_KV + l * KV_L + (u32)head * (64u * 4096u);
    lptr kvl = L + L_INBOX + lane * 16;
    v4u K0, K1, V0, V1;
    const bool probe = l == 0 && ((head & 7) == 0 || (head & 7) == 7);
    const u32 vidx = (u32)((T * 4 + (head >> 3) * 2 + (head & 1)) * 4 + w);
    if (probe) {
      drain();
      const u32 q = ph(WS_PROBE + vidx * 256u);
      __builtin_amdgcn_raw_buffer_store_b32(0u, rws, lane * 4, q + (w & 1) * 8192u, 16);
      __builtin_amdgcn_raw_buffer_store_b32(0u, rws, lane * 4, q + ((w & 1) ^ 1) * 8192u, 16);
      asm volatile("" ::: "memory");
    }
    {
      u32 o[8];
#pragma unroll
      for (int i = 0; i < 8; ++i) o[i] = pk(kt[2 * i] * rstd + ck_[2 * i], kt[2 * i + 1] * rstd + ck_[2 * i + 1]);
      if (h) { o[6] = 0u; o[7] = 0u; }
      K0 = v4u{o[0], o[1], o[2], o[3]}; K1 = v4u{o[4], o[5], o[6], o[7]};
      st_sc1(K0, rws, lane * 16, ph(kvbase + tb * 4096));
      st_sc1(K1, rws, lane * 16 + 1024, ph(kvbase + tb * 4096));
#pragma unroll
      for (int i = 0; i < 8; ++i) o[i] = pk(vv[2 * i] * rr[2 * i] + cv_[2 * i], vv[2 * i + 1] * rr[2 * i + 1] + cv_[2 * i + 1]);
      if (r >= 28) {
#pragma unroll
        for (int i = 0; i < 8; ++i) o[i] = 0x3F803F80u;
      }
      V0 = v4u{o[0], o[1], o[2], o[3]}; V1 = v4u{o[4], o[5], o[6], o[7]};
      st_sc1(V0, rws, lane * 16 + 2048, ph(kvbase + tb * 4096));
      st_sc1(V1, rws, lane * 16 + 3072, ph(kvbase + tb * 4096));
    }
    if (probe) {
      u64 t1, t2;
      asm volatile("s_waitcnt vmcnt(5)\n\ts_memtime %0\n\ts_waitcnt vmcnt(4)\n\ts_memtime %1\n\ts_waitcnt lgkmcnt(0)" : "=&s"(t1), "=&s"(t2)::"memory");
      const u32 gap = (u32)t2 - (u32)t1;
      const u32 vote = (gap > GAP_LO && gap < 700u) ? 1u + ((w & 1) ^ half_) : 0u;
      if (lane == 0) __hip_atomic_store((GAS u64*)(wsa + WS_VOTE + vidx * 8u), ((u64)vote << 32) | (u32)tag, RLX_AGENT);
      drain();
    }
    STAMP(19);
    {
      const int tsrc = T - 2 + (w >> 1);
      const u32 f1 = opqv(rd(flag_off(l, 0, (tsrc < 0 ? T : tsrc) * 16 + head), nb));
      const float rs = rstd * (0.18898223650461362f * 1.4426950408889634f), ms = nmr * (0.18898223650461362f * 1.4426950408889634f);
      Poll pq;
      f32x16 qt, cq;
      u32 em[16];
#pragma unroll
      for (int i = 0; i < 16; ++i) qt[i] = 0.f;
      auto q_step = [&](auto I) {
        constexpr int k = I;
        qt = mfma(wq[k], X[k], qt);
        SB();
        if constexpr (k + 2 * PD < 28) wq[k + 2 * PD] = WFQ(k + 2 * PD);
        if constexpr (k < 4) { const f32x4 cs = *(LAS f32x4*)(L + L_CSF + w * 512 + (8 * k + 4 * h) * 4); cq[4 * k] = cs[0]; cq[4 * k + 1] = cs[1]; cq[4 * k + 2] = cs[2]; cq[4 * k + 3] = cs[3]; }
        if constexpr (k >= 4 && k < 20) { constexpr int i = k - 4; em[i] = opqv((u32)((r - ((i & 3) + 8 * (i >> 2)) - 4 * h) >> 31)); }
        if constexpr (k >= 20) { cq[2 * k - 40] = mul1(cq[2 * k - 40], ms); cq[2 * k - 39] = mul1(cq[2 * k - 39], ms); }
        SB();
      };
      SB();
      sfor<0, QS>(q_step);
      asm volatile("" : "+a"(qt));
      publish(wsa, flag_off(l, 0, T * 16 + head), tag);
      touch_done(tc2); touch_done(tc3);
      asm volatile("" : "+v"(xr2[0]), "+v"(xr2[1]), "+v"(xr2[2]), "+v"(xr2[3]));
#pragma unroll
      for (int i = 0; i < 4; ++i) { xres[2 * i] = xr2[i].x; xres[2 * i + 1] = xr2[i].y; }
      STAMP(2);
      {
        lptr own = kvl + (8 + w) * 4096;
        *(LAS v4u*)own = K0; *(LAS v4u*)(own + 1024) = K1; *(LAS v4u*)(own + 2048) = V0; *(LAS v4u*)(own + 3072) = V1;
      }
      SB();
      sfor<QS, 28>(q_step);
      v4u Q0, Q1;
      {
        u32 o[8];
#pragma unroll
        for (int i = 0; i < 8; ++i) o[i] = pk(qt[2 * i] * rs + cq[2 * i], qt[2 * i + 1] * rs + cq[2 * i + 1]);
        if (h) { o[6] = 0x00003F80u; o[7] = 0u; }
        Q0 = v4u{o[0], o[1], o[2], o[3]}; Q1 = v4u{o[4], o[5], o[6], o[7]};
      }
      asm volatile("" : "+v"(Q0), "+v"(Q1));
      pq.a = poll_one(wsa, f1);
      float nm;
      f32x16 cm, c0, o;
      v4u t[8];
      {
        f32x16 s8 = qk_own(K0, K1, Q0, Q1);
        tie_far(s8);
#pragma unroll
        for (int i = 0; i < 16; ++i) s8[i] = asf((em[i] & 0xF149F2CAu) | (~em[i] & asu(s8[i])));
        float m4[4];
#pragma unroll
        for (int i = 0; i < 4; ++i) m4[i] = fmaxf(fmaxf(s8[4 * i], s8[4 * i + 1]), fmaxf(s8[4 * i + 2], s8[4 * i + 3]));
        nm = -half_max(fmaxf(fmaxf(m4[0], m4[1]), fmaxf(m4[2], m4[3])));
        asm volatile("" : "+v"(nm));
        pq.b = poll_one(wsa, f1);
        u32 pp[8];
#pragma unroll
        for (int i = 0; i < 8; ++i) pp[i] = pk(__builtin_amdgcn_exp2f(s8[2 * i] + nm), __builtin_amdgcn_exp2f(s8[2 * i + 1] + nm));
        asm volatile("" : "+v"(pp[0]), "+v"(pp[7]));
        pq.c = poll_one(wsa, f1);
        poll_finish(pq, wsa, f1, tag);
        STAMP(3);
        const u32 so = tsrc >= 0 ? ph(kvbase + (u32)(4 * T - 8 + 2 * w) * 4096u) : NOWHERE;
        t[0] = lda<0>(rws, lane * 16, so); t[1] = lda<1>(rws, lane * 16, so); t[2] = lda<4>(rws, lane * 16, so); t[3] = lda<5>(rws, lane * 16, so);
        t[4] = lda<2>(rws, lane * 16, so); t[5] = lda<3>(rws, lane * 16, so); t[6] = lda<6>(rws, lane * 16, so); t[7] = lda<7>(rws, lane * 16, so);
        SB();
#pragma unroll
        for (int i = 0; i < 16; ++i) o[i] = 0.f;
        o = mfma(V0, v4u{pp[0], pp[1], pp[2], pp[3]}, o);
        o = mfma(V1, v4u{pp[4], pp[5], pp[6], pp[7]}, o);
#pragma unroll
        for (int i = 0; i < 16; ++i) { cm[i] = nm; c0[i] = asf((em[i] & asu(nm)) | (~em[i] & 0xF149F2CAu)); }
        asm volatile("" : "+v"(cm), "+v"(c0), "+a"(o));
      }
      const u32 w2v = opqv(dmarow * 4096u + dmach + (u32)st * 128u), w2s = opq((u32)(448 * w) * 4096u), w2l = opq(lds0 + ringw);
      const rsrc_t rt4 = mkrs(p.w1, (TOUCH && l == 0) ? S_ * F_ * 2 : 0);
      lptr kq = kvl + w * 4096;
      SB();
      {
        lptr dst = kvl + w * 8192;
        vmw<4>(t[0], t[1], t[2], t[3]);
        poll_done(pq);
        if (tsrc < 0 && h) { t[1][2] = 0xF149u; t[3][2] = 0xF149u; }
#pragma unroll
        for (int i = 0; i < 4; ++i) *(LAS v4u*)(dst + (i & 1) * 1024 + (i >> 1) * 4096) = t[i];
        vmw<0>(t[4], t[5], t[6], t[7]);
#pragma unroll
        for (int i = 0; i < 4; ++i) *(LAS v4u*)(dst + 2048 + (i & 1) * 1024 + (i >> 1) * 4096) = t[4 + i];
      }
      bar();
      STAMP(17);
      v4u vt;
      {
        v4u Kr[8][2], Vr[8][2];
        f32x16 s[8];
        u32 pp[8][8];
        float e[16];
#define KLD(j) do { Kr[j][0] = *(LAS v4u*)(kq + (j) * 4096); Kr[j][1] = *(LAS v4u*)(kq + (j) * 4096 + 1024); } while (0)
#define VLD(j) do { Vr[j][0] = *(LAS v4u*)(kq + (j) * 4096 + 2048); Vr[j][1] = *(LAS v4u*)(kq + (j) * 4096 + 3072); } while (0)
#define W2P(i) w_issue<i, (i) + 1, 4096u>(rw2, w2v, w2s, w2l)
        KLD(0); KLD(1); KLD(2); VLD(0);
        tc4 = touch(rt4, t4v, 0);
        vt = ldg<0>(rws, lane * 16 + (w & 1) * 1024, l == 0 ? WS_VOTE : NOWHERE);
        SB();
        s[0] = qk1(Kr[0][0], Q0, c0); qk2(s[0], Kr[0][1], Q1);
        s[1] = qk1(Kr[1][0], Q0, cm); qk2(s[1], Kr[1][1], Q1);
        SB();
        sfor<0, 8>([&](auto I) {
          constexpr int c = I;
          tie(s[c]);
          auto ex = [&](int q) {
#pragma unroll
            for (int i = 0; i < 4; ++i) e[4 * q + i] = __builtin_amdgcn_exp2f(s[c][4 * q + i]);
          };
          auto cv = [&](int q) { pp[c][2 * q] = pk(e[4 * q], e[4 * q + 1]); pp[c][2 * q + 1] = pk(e[4 * q + 2], e[4 * q + 3]); };
          SB(); ex(0); if constexpr (c < 6) W2P(4 * c); if constexpr (c < 5) KLD(c + 3); SB();
          if constexpr (c < 6) s[c + 2] = qk1(Kr[c + 2][0], Q0, cm);
          SB(); ex(1); cv(0); if constexpr (c < 6) W2P(4 * c + 1); if constexpr (c < 7) VLD(c + 1); SB();
          if constexpr (c < 6) qk2(s[c + 2], Kr[c + 2][1], Q1);
          SB(); cv(1); ex(2); if constexpr (c < 6) W2P(4 * c + 2); SB();
          o = mfma(Vr[c][0], v4u{pp[c][0], pp[c][1], pp[c][2], pp[c][3]}, o);
          SB(); ex(3); cv(2); cv(3); if constexpr (c < 6) W2P(4 * c + 3); SB();
          o = mfma(Vr[c][1], v4u{pp[c][4], pp[c][5], pp[c][6], pp[c][7]}, o);
        });
      }
      float lsum = half_add(h ? o[12] : 0.f);
      if (!__all(lsum < SAFE_SUM)) {
        float mx = -nm;
#pragma unroll 1
        for (int pass = 0; pass < 2; ++pass) {
#pragma unroll
          for (int i = 0; i < 16; ++i) o[i] = 0.f;
#pragma unroll 1
          for (int j = 0; j < 9; ++j) {
            f32x16 sj = qk_own(*(LAS v4u*)(kq + j * 4096), *(LAS v4u*)(kq + j * 4096 + 1024), Q0, Q1);
            tie_far(sj);
            const u32 keep = (j == 0 || j == 8) ? 0u : 0xffffffffu, flip = j == 8 ? 0xffffffffu : 0u;
            u32 pp[8];
#pragma unroll
            for (int i = 0; i < 16; ++i) { const u32 m = (em[i] ^ flip) | keep; sj[i] = asf((m & asu(sj[i])) | (~m & 0xF149F2CAu)); if (pass == 0) mx = fmaxf(mx, sj[i]); }
#pragma unroll
            for (int i = 0; i < 8; ++i) pp[i] = pk(__builtin_amdgcn_exp2f(sj[2 * i] - mx), __builtin_amdgcn_exp2f(sj[2 * i + 1] - mx));
            o = mfma(*(LAS v4u*)(kq + j * 4096 + 2048), v4u{pp[0], pp[1], pp[2], pp[3]}, o);
            o = mfma(*(LAS v4u*)(kq + j * 4096 + 3072), v4u{pp[4], pp[5], pp[6], pp[7]}, o);
          }
          mx = half_max(mx);
        }
        lsum = half_add(h ? o[12] : 0.f);
      }
      const float inv = __builtin_amdgcn_rcpf(lsum);
      u32 oo[8];
#pragma unroll
      for (int i = 0; i < 8; ++i) oo[i] = pk(o[2 * i] * inv, o[2 * i + 1] * inv);
      if (h) { oo[6] = 0u; oo[7] = 0u; }
      const u32 ob = ph(WS_OF + l * OF_L + (u32)(tb * 16 + head) * 2048u);
      st_sc1(v4u{oo[0], oo[1], oo[2], oo[3]}, rws, lane * 16, ob);
      st_sc1(v4u{oo[4], oo[5], oo[6], oo[7]}, rws, lane * 16 + 1024, ob);
      {
        T5_PTRS
        vmw0<2>();
        w_take<0, 12, 1024>(t0, t1, bw5);
        vmw<2>(vt);
        if (l == 0) {
          const u32 tl = (u32)tag;
          const int n0 = __popcll(__ballot(vt.x == tl && vt.y == 1u)) + __popcll(__ballot(vt.z == tl && vt.w == 1u));
          const int n1 = __popcll(__ballot(vt.x == tl && vt.y == 2u)) + __popcll(__ballot(vt.z == tl && vt.w == 2u));
          if (n0 > 2 * n1 + 3) nb = half_; else if (n1 > 2 * n0 + 3) nb = half_ ^ 1u;
        }
      }
      publish(wsa, flag_off(l, 1, T * 16 + head), tag);
      touch_done(tc4);
      STAMP(4);
    }
    }
    if (p35) {
      v2u bias1[4];
      u32 bias2;
      {
      IDS
      {
        const rsrc_t rb1 = mkrs(p.b1, F_ * 2);
        const u32 bo = (u32)(ft * 64 + wa * 32 + 4 * h) * 2u;
        asm volatile("s_nop 4\n\tbuffer_load_ushort %0, %1, %2, 0 offen" : "=v"(bias2) : "v"((u32)((2 * st + wb) * 32 + r) * 2u), "s"(mkrs(p.b2, S_ * 2)));
        sfor<0, 4>([&](auto I) { constexpr int i = I; bias1[i] = ldp2<16 * i>(rb1, bo); });
      }
      const u32 f2 = rd(flag_off(l, 1, (st >> 1) * 16 + 4 * w + (lane >> 4)), nb);
      Poll pq;
      f32x16 init;
#pragma unroll
      for (int i = 0; i < 8; ++i) { init[2 * i] = blo(xres[i]); init[2 * i + 1] = bhi(xres[i]); }
      asm volatile("" : "+a"(init));
      w_issue2<0, PA0, 3584u, 32>(W1_ARGS); pq.a = poll_one(wsa, f2);
      w_issue2<PA0, PA1, 3584u, 32>(W1_ARGS); pq.b = poll_one(wsa, f2);
      w_issue2<PA1, 24, 3584u, 32>(W1_ARGS); pq.c = poll_one(wsa, f2);
      const u32 o0 = opq(ph(WS_OF + l * OF_L + (u32)st * 65536u + (u32)wb * 32768u + (u32)w * 8192u));
      const u32 o1 = opq(o0 ^ 65536u);
      poll_finish_mixed<24 - PA0 + 2, 24 - PA1 + 1, 0>(pq, wsa, f2, tag);
      asm volatile("" : "+v"(bias1[0]), "+v"(bias1[1]), "+v"(bias1[2]), "+v"(bias1[3]), "+v"(bias2));
      STAMP(5);
      const f32x16 y = gemm_main<8, 8, 0, true, 12, 4, false>(L, w, lane, rws, o0, o1, L, L, wf, init, pq, [&](auto) {});
      STAMP(21);
      float s1 = 0.f, s2 = 0.f;
#pragma unroll
      for (int i = 0; i < 8; ++i) {
        xres[i] = pk(y[2 * i], y[2 * i + 1]);
        s1 = __builtin_amdgcn_fdot2_f32_bf16(asbf2(xres[i]), asbf2(0x3F803F80u), s1, false);
        s2 = __builtin_amdgcn_fdot2_f32_bf16(asbf2(xres[i]), asbf2(xres[i]), s2, false);
      }
      s1 = half_add(s1); s2 = half_add(s2);
      const u32 sb = rd(WS_STATS + l * STATS_L + (u32)(2 * st + wb) * 4096u, nb);
      __builtin_amdgcn_raw_buffer_store_b64(v2u{asu(s1), asu(s2) | 1u}, rws, r * 16 + h * 8192, (sb & ~8192u) + (u32)(dt * 512 + wa * 8), 16);
      SB();
      STAMP(6);
      T4_PTRS
      w_take<0, NB, 512>(n0, n1, bw4);
      f32x16 xv;
#pragma unroll
      for (int i = 0; i < 8; ++i) { xv[2 * i] = blo(xres[i]); xv[2 * i + 1] = bhi(xres[i]); }
      asm volatile("s_waitcnt lgkmcnt(0)" : "+v"(xv)::"memory");
      float t1s, t2s;
      {
        v4u gr[4];
        const u32 gv = (u32)h * 2048u + r * 16, gv3 = h ? NOWHERE : gv;
        w_issue2<24, 24 + PB0, 3584u, 32>(W1_ARGS);
        gr[0] = ldg<0>(rws, gv, sb); gr[1] = ldg<512>(rws, gv, sb); gr[2] = ldg<1024>(rws, gv, sb); gr[3] = ldg<1536>(rws, gv3, sb);
        w_issue2<24 + PB0, 24 + 2 * NB, 3584u, 32>(W1_ARGS);
        vmw<2 * NB - PB0>(gr[0], gr[1], gr[2], gr[3]);
        for (u32 spins = 0;;) {
          const u32 ok = (gr[0].y & gr[0].w & gr[1].y) & (gr[1].w & gr[2].y & gr[2].w) & ((gr[3].y & gr[3].w) | (u32)h);
          if (__all(ok & 1u)) break;
          if (spin_out(wsa, spins, (u32)tag)) break;
          asm volatile("" ::: "memory");
          gr[0] = ld_sc1(rws, gv, sb); gr[1] = ld_sc1(rws, gv + 512, sb); gr[2] = ld_sc1(rws, gv + 1024, sb); gr[3] = ld_sc1(rws, gv3 + 1536, sb);
        }
        t1s = ((asf(gr[0].x) + asf(gr[0].z)) + (asf(gr[1].x) + asf(gr[1].z))) + ((asf(gr[2].x) + asf(gr[2].z)) + (asf(gr[3].x) + asf(gr[3].z)));
        t2s = ((asf(gr[0].y) + asf(gr[0].w)) + (asf(gr[1].y) + asf(gr[1].w))) + ((asf(gr[2].y) + asf(gr[2].w)) + (asf(gr[3].y) + asf(gr[3].w)));
      }
      STAMP(7);
      t1s = half_add(t1s); t2s = half_add(t2s);
      const float m1 = t1s * (1.f / 448.f);
      const float var = fmaxf(t2s * (1.f / 448.f) - m1 * m1, 0.f);
      const float r1 = __builtin_amdgcn_rsqf(var + EPS);
      const float r2 = __builtin_amdgcn_rsqf(var * r1 * r1 + EPS);
      const float rr = r1 * r2, mm = m1 * rr;
      lptr tw = L + L_TR + w * 2304;
#pragma unroll
      for (int i = 0; i < 4; ++i)
        *(LAS v2u*)(tw + r * 72 + i * 16 + h * 8) = v2u{pk(xv[4 * i] * rr - mm, xv[4 * i + 1] * rr - mm), pk(xv[4 * i + 2] * rr - mm, xv[4 * i + 3] * rr - mm)};
      asm volatile("" ::: "memory");
      lptr tr = tw + (8 * h + tq) * 72 + ch16 * 32 + tp * 8;
      const u32 hb = ph(WS_HF + l * HF_L + (u32)((2 * dt + wa) * 128 + (2 * st + wb) * 2) * 1024u);
      st2(trd2(tr, tr + 4 * 72), rws, lane * 16, hb);
      st2(trd2(tr + 16 * 72, tr + 20 * 72), rws, lane * 16 + 1024, hb);
      vmw0<4>();
      w_take<NB, 20, 512>(n0, n1, bw4);
      publish(wsa, flag_off(l, 2, dt * 32 + st), tag);
      STAMP(8);
      }
      if (p4) {
        IDS
        T4_PTRS
        const u32 f3 = rd(flag_off(l, 2, dt4 * 32 + 8 * w + (lane >> 3)), nb);
        Poll pq;
        f32x16 init;
#pragma unroll
        for (int i = 0; i < 8; ++i) { const u32 bb = (i & 1) ? bias1[i >> 1].y : bias1[i >> 1].x; init[2 * i] = blo(bb); init[2 * i + 1] = bhi(bb); }
        asm volatile("" : "+a"(init));
        w_issue2<24 + 2 * NB, 64 - PC0, 3584u, 32>(W1_ARGS); pq.a = poll_one(wsa, f3);
        w_issue2<64 - PC0, 64 - PC1, 3584u, 32>(W1_ARGS); pq.b = poll_one(wsa, f3);
        w_issue2<64 - PC1, 64, 3584u, 32>(W1_ARGS); pq.c = poll_one(wsa, f3);
        const u32 hb = WS_HF + l * HF_L + (u32)(32 * w) * 1024u;
        const u32 h0 = opq(rd(hb + (u32)(2 * dt4 + wb) * 131072u, nb)), h1 = opq(h0 ^ 262144u);
        STAMP(9);
        poll_finish_mixed<PC0 + 2, PC1 + 1, 0>(pq, wsa, f3, tag);
        STAMP(10);
        const f32x16 y = gemm_main<32, 20, 512, true, 12, 6, true>(L, w, lane, rws, h0, h1, n0, n1, bw4, init, pq,
                                                                   [&](auto I) { constexpr int i = I; w_issue2<24 + i, 25 + i, 4096u, 28>(W2_ARGS); });
        STAMP(11);
        u32 o[8];
#pragma unroll
        for (int i = 0; i < 8; ++i) {
          const f32x2 u = {y[2 * i], y[2 * i + 1]};
          const f32x2 z = u * (u * u * -0.1029432397f - 2.302208198f);
          const f32x2 d = f32x2{__builtin_amdgcn_exp2f(z[0]), __builtin_amdgcn_exp2f(z[1])} + 1.f;
          const f32x2 gv = u * f32x2{__builtin_amdgcn_rcpf(d[0]), __builtin_amdgcn_rcpf(d[1])};
          o[i] = pk(gv[0], gv[1]);
        }
        const u32 ub = ph(WS_UF + l * UF_L + (u32)((2 * dt4 + wb) * 128 + 4 * ft + 4 * pj + 2 * wa) * 1024u);
        st2(v4u{o[0], o[1], o[2], o[3]}, rws, lane * 16, ub);
        st2(v4u{o[4], o[5], o[6], o[7]}, rws, lane * 16 + 1024, ub);
        STAMP(23);
        T5_PTRS
        vmw0<4>();
        w_take<12, 16, 1024>(t0, t1, bw5);
        publish(wsa, flag_off(l, 3, dt4 * 32 + ft), tag);
        STAMP(12);
      } else {
        IDS
        T5_PTRS
        w_issue2<24, 36, 4096u, 28>(W2_ARGS);
        drain();
        w_take<12, 16, 1024>(t0, t1, bw5);
        lgk0();
      }
      IDS
      T5_PTRS
      const u32 f4 = rd(flag_off(l, 3, dt * 32 + 7 * w + (lane < 56 ? lane >> 3 : 6)), nb);
      Poll pq;
      f32x16 init;
      const float bv = blo(bias2);
#pragma unroll
      for (int i = 0; i < 8; ++i) { init[2 * i] = blo(xres[i]) + bv; init[2 * i + 1] = bhi(xres[i]) + bv; }
      asm volatile("" : "+a"(init));
      w_issue2<36, 56 - PD0, 4096u, 28>(W2_ARGS); pq.a = poll_one(wsa, f4);
      w_issue2<56 - PD0, 56 - PD1, 4096u, 28>(W2_ARGS); pq.b = poll_one(wsa, f4);
      w_issue2<56 - PD1, 56, 4096u, 28>(W2_ARGS); pq.c = poll_one(wsa, f4);
      const u32 ub = WS_UF + l * UF_L + (u32)(32 * w) * 1024u;
      const u32 u0 = opq(rd(ub + (u32)(2 * dt + wa) * 131072u, nb)), u1 = opq(u0 ^ 262144u);
      const u32 wis = l == 0 ? rd(WS_WI + (u32)(head * 4 + w) * 24576u, nb) : NOWHERE;
      STAMP(13);
      poll_finish_mixed<PD0 + 2, PD1 + 1, 0>(pq, wsa, f4, tag);
      STAMP(14);
      v4u wi[21];
      const f32x16 y = gemm_main<28, 16, 1024, false, 12, 6, true>(L, w, lane, rws, u0, u1, t0, t1, bw5, init, pq,
                                                                   [&](auto I) { constexpr int i = I; wi[i] = lda<i>(rws, lane * 16, wis); });
      asm volatile("s_waitcnt vmcnt(0)" : "+v"(wi[0]), "+v"(wi[1]), "+v"(wi[2]), "+v"(wi[3]), "+v"(wi[4]), "+v"(wi[5]), "+v"(wi[6]), "+v"(wi[7]), "+v"(wi[8]), "+v"(wi[9]), "+v"(wi[10]), "+v"(wi[11]));
      STAMP(15);
#pragma unroll
      for (int i = 0; i < 8; ++i) xres[i] = pk(y[2 * i], y[2 * i + 1]);
      v4u xo[2];
#pragma unroll
      for (int s = 0; s < 2; ++s) {
        auto a = __builtin_amdgcn_permlane32_swap(xres[4 * s], xres[4 * s + 2], false, false);
        auto c = __builtin_amdgcn_permlane32_swap(xres[4 * s + 1], xres[4 * s + 3], false, false);
        xo[s] = v4u{a[0], c[0], a[1], c[1]};
      }
      if (l == 0) {
        const u32 xb = ph(WS_XF + (u32)((2 * st + wb) * 32 + 4 * dt) * 1024u + wa * 512);
        st_sc1(xo[0], rws, h * 1024 + r * 16, xb);
        st_sc1(xo[1], rws, h * 1024 + r * 16 + 2048, xb);
        float q0 = 0.f, q1 = 0.f;
#pragma unroll
        for (int i = 0; i < 8; ++i) { q0 = __builtin_amdgcn_fdot2_f32_bf16(asbf2(xres[i]), asbf2(0x3F803F80u), q0, false); q1 = __builtin_amdgcn_fdot2_f32_bf16(asbf2(xres[i]), asbf2(xres[i]), q1, false); }
        q0 = half_add(q0); q1 = half_add(q1);
        __builtin_amdgcn_raw_buffer_store_b64(v2u{asu(q0), asu(q1)}, rws, r * 16 + h * 8192, ph(WS_XF + (u32)((2 * st + wb) * 32 + 28) * 1024u + (u32)(dt * 512 + wa * 8)), 16);
        asm volatile("" ::: "memory");
        sfor<0, 12>([&](auto I) { constexpr int i = I; *(LAS v4u*)(L + w * 21504 + i * 1024 + lane * 16) = wi[i]; });
        publish(wsa, flag_off(0, 4, st * 8 + dt), tag);
        sfor<12, 21>([&](auto I) { constexpr int i = I; wi[i] = lda<i>(rws, lane * 16, wis); });
        sfor<12, 21>([&](auto I) { constexpr int i = I; vmw<20 - i>(wi[i]); *(LAS v4u*)(L + w * 21504 + i * 1024 + lane * 16) = wi[i]; });
        bar();
      } else {
        const rsrc_t rout = mkrs(p.out, S_ * D_ * 2);
        const u32 oo = (u32)((2 * st + wb) * 32 + r) * 896u + (u32)(dt * 64 + wa * 32 + 8 * h) * 2u;
        __builtin_amdgcn_raw_buffer_store_b128(xo[0], rout, oo, 0, 0);
        __builtin_amdgcn_raw_buffer_store_b128(xo[1], rout, oo + 32, 0, 0);
        if (b == 0 && tid == 0) __hip_atomic_store((GAS u64*)(wsa + WS_EPOCH), tag, RLX_AGENT);
      }
      STAMP(16);
    } else {
      IDS
      const u32 wi_ = (u32)((bi - 28) * 4 + w);
      const rsrc_t r1 = mkrs(p.w1, S_ * F_ * 2), r2 = mkrs(p.w2, S_ * F_ * 2);
      const u32 v1 = (lane >> 4) * (512u * 3584u) + ((lane >> 2) & 3) * 3584u + (lane & 3) * 128u, c1 = (u32)(7 * pj + 4 * (g & 1)) * 128u;
      const u32 v2 = (lane >> 4) * (448u * 4096u) + ((lane >> 2) & 3) * 4096u + (lane & 3) * 128u, c2 = (u32)g * 512u;
      constexpr u32 RW = 4;
      for (u32 row = 192 + RW * wi_; row < 512; row += 16 * RW) { const u32 d = touch(r1, v1, row * 3584u + c1); drain(); touch_done(d); }
      for (u32 row = 192 + RW * wi_; row < 448; row += 16 * RW) { const u32 d = touch(r2, v2, row * 4096u + c2); drain(); touch_done(d); }
      if (l == 0) {
        const Poll pq = wait_flags(wsa, rd(flag_off(0, 1, (lane >> 2) * 16 + head), nb), tag);
        const u32 wis = rd(WS_WI + (u32)(head * 4 + w) * 24576u, nb);
        v4u wi[21];
        sfor<0, 21>([&](auto I) { constexpr int i = I; wi[i] = lda<i>(rws, lane * 16, wis); });
        sfor<0, 21>([&](auto I) { constexpr int i = I; vmw<20 - i>(wi[i]); *(LAS v4u*)(L + w * 21504 + i * 1024 + lane * 16) = wi[i]; });
        poll_done(pq);
        bar();
        for (u32 i = wi_; i < 48; i += 16) { const u32 d = touch(rws, lane * 128, rd(WS_WI + (u32)((g & 1) * 8) * 98304u + i * 8192u, nb)); drain(); touch_done(d); }
        for (u32 row = RW * wi_; row < 192; row += 16 * RW) { const u32 d = touch(r2, v2, row * 4096u + c2); drain(); touch_done(d); }
        for (u32 row = RW * wi_; row < 192; row += 16 * RW) { const u32 d = touch(r1, v1, row * 3584u + c1); drain(); touch_done(d); }
      }
    }
  }
  asm volatile(".Lfwd_last:");
}

extern "C" void kernel_launch(void* const* d_in, const int* in_sizes, int n_in, void* d_out, int out_size, void* d_ws, size_t ws_size,
                              hipStream_t stream) {
  Params p;
  p.x = d_in[0]; p.wqkv = d_in[1]; p.wo = d_in[2]; p.w1 = d_in[4]; p.b1 = d_in[5]; p.w2 = d_in[6]; p.b2 = d_in[7];
  p.out = d_out; p.ws = (unsigned char*)d_ws;
  (void)hipFuncSetAttribute((const void*)fused_fwd, hipFuncAttributeMaxDynamicSharedMemorySize, L_END);
  hipLaunchKernelGGL(fused_fwd, dim3(256), dim3(256), L_END, stream, p);
}
```

```cpp
#define DBG_STAMP 1
#define DIAG_A 32
#define DIAG_B 48
#define DIAG_SRC dbgC
#define DIAG_MUL 10
#define DIAG_DIV 24
#define DIAG_BLK 16
#include <hip/hip_runtime.h>
#include <hip/hip_bf16.h>
#include <type_traits>

typedef unsigned u32;
typedef unsigned long long u64;
typedef u32 v4u __attribute__((ext_vector_type(4)));
typedef u32 v2u __attribute__((ext_vector_type(2)));
typedef float f32x16 __attribute__((ext_vector_type(16)));
typedef float f32x4 __attribute__((ext_vector_type(4)));
typedef float f32x2 __attribute__((ext_vector_type(2)));
typedef __bf16 bf16x8 __attribute__((ext_vector_type(8)));
typedef __bf16 bf16x2 __attribute__((ext_vector_type(2)));
typedef short s16x4 __attribute__((ext_vector_type(4)));
#define LAS __attribute__((address_space(3)))
#define GAS __attribute__((address_space(1)))
#define DI __device__ __forceinline__
#define RLX_AGENT __ATOMIC_RELAXED, __HIP_MEMORY_SCOPE_AGENT
#define SB() __builtin_amdgcn_sched_barrier(0)
typedef __amdgpu_buffer_rsrc_t rsrc_t;
typedef LAS unsigned char* lptr;

#define DBG_WAVE 0
#define POLL_GAP "3"
#define TOUCH 1
#define QP0 12
#define NB 10
#define CODE_TOUCH 1
#define TLBT 0
#define LA4 6
#define LA5 6
#define LA1 12
#define LA3 6
#define ROT4 0
#define ROT5 0
#define ROT1 (head & 1)
#define WARM2 1

constexpr int S_ = 2048, D_ = 448, F_ = 1792;
constexpr u32 WS_EPOCH = 0, WS_TMO = 128;
constexpr u32 WS_FLAGS = 4096;
constexpr u32 WS_STATS = WS_FLAGS + 2 * 5 * 256 * 128;
constexpr u32 STATS_L = 64 * 14 * 512;
constexpr u32 WS_KV = 2u << 20, KV_L = 4u << 20;
constexpr u32 WS_OF = WS_KV + 2 * KV_L, OF_L = 2u << 20;
constexpr u32 WS_HF = WS_OF + 2 * OF_L, HF_L = 2u << 20;
constexpr u32 WS_UF = WS_HF + 2 * HF_L, UF_L = 2u << 20;
constexpr u32 WS_XF = WS_UF + 2 * UF_L;
constexpr u32 WS_SQ = WS_XF + 64 * 28 * 1024;
constexpr u32 WS_WI = WS_XF + (2u << 20);
constexpr u32 WS_END = WS_WI + (2u << 20);
constexpr u32 WS_DBG = WS_END;
static_assert(WS_STATS + 2 * STATS_L <= WS_KV, "ws map");
constexpr u32 L_RING = 0, RING_W = 24576;
constexpr u32 L_INBOX = 98304, INBOX_W = 12288;
constexpr u32 L_WQ = 57344;
constexpr u32 L_WO = 86016;
constexpr u32 L_CSP = 147456;
constexpr u32 L_CSF = L_CSP + 1536;
constexpr u32 L_MR = L_CSF + 2048;
constexpr u32 L_TR = L_MR + 1024;
constexpr u32 L_END = L_TR + 4 * 2304;
constexpr u32 kSpinCap = 1u << 16;
constexpr float EPS = 1e-5f;

struct Params {
  const void* x; const void* wqkv; const void* wo; const void* w1; const void* b1; const void* w2; const void* b2;
  void* out; unsigned char* ws;
};

DI f32x16 mfma(v4u a, v4u b, f32x16 c) {
  return __builtin_amdgcn_mfma_f32_32x32x16_bf16(__builtin_bit_cast(bf16x8, a), __builtin_bit_cast(bf16x8, b), c, 0, 0, 0);
}
DI f32x16 qk1(v4u k0, v4u q0, f32x16 ci) {
  f32x16 c;
  asm volatile("v_mfma_f32_32x32x16_bf16 %0, %1, %2, %3" : "=&v"(c) : "a"(k0), "v"(q0), "v"(ci));
  return c;
}
DI f32x16 qk1z(v4u k0, v4u q0) {
  f32x16 c;
  asm volatile("v_mfma_f32_32x32x16_bf16 %0, %1, %2, 0" : "=&v"(c) : "a"(k0), "v"(q0));
  return c;
}
DI void qk2(f32x16& c, v4u k1, v4u q1) { asm volatile("v_mfma_f32_32x32x16_bf16 %0, %1, %2, %0" : "+v"(c) : "a"(k1), "v"(q1)); }
DI f32x16 qk_own(v4u k0, v4u k1, v4u q0, v4u q1) {
  f32x16 c;
  asm volatile("s_nop 1\n\tv_mfma_f32_32x32x16_bf16 %0, %1, %3, 0\n\tv_mfma_f32_32x32x16_bf16 %0, %2, %4, %0" : "=&v"(c) : "v"(k0), "v"(k1), "v"(q0), "v"(q1));
  return c;
}
DI f32x16 qk_own_c(v4u k0, v4u k1, v4u q0, v4u q1, f32x16 ci) {
  f32x16 c;
  asm volatile("s_nop 1\n\tv_mfma_f32_32x32x16_bf16 %0, %1, %3, %5\n\tv_mfma_f32_32x32x16_bf16 %0, %2, %4, %0" : "=&v"(c) : "v"(k0), "v"(k1), "v"(q0), "v"(q1), "v"(ci));
  return c;
}
DI void tie(f32x16& c) { asm volatile("" : "+v"(c)); }
DI void tie_far(f32x16& c) { asm volatile("s_nop 7\n\ts_nop 4" : "+v"(c)); }
DI void nop2() { asm volatile("s_nop 1"); }
DI u32 pk(float lo, float hi) { f32x2 v = {lo, hi}; return __builtin_bit_cast(u32, __builtin_convertvector(v, bf16x2)); }
DI float asf(u32 v) { return __builtin_bit_cast(float, v); }
DI u32 asu(float v) { return __builtin_bit_cast(u32, v); }
DI bf16x2 asbf2(u32 v) { return __builtin_bit_cast(bf16x2, v); }
DI float blo(u32 v) { return __builtin_bit_cast(float, v << 16); }
DI float bhi(u32 v) { return __builtin_bit_cast(float, v & 0xffff0000u); }
DI void bar() { asm volatile("s_waitcnt lgkmcnt(0)\n\ts_barrier" ::: "memory"); }
DI void drain() { asm volatile("s_waitcnt vmcnt(0)" ::: "memory"); }
DI void lgk0() { asm volatile("s_waitcnt lgkmcnt(0)" ::: "memory"); }
template <u32 SI, u32 LI> DI void dma16(rsrc_t rs, u32 voff, u32 soff, u32 lds) {
  u32 t;
  asm volatile("s_add_u32 m0, %1, %5\n\ts_add_u32 %0, %4, %6\n\tbuffer_load_dwordx4 %2, %3, %0 offen lds"
               : "=&s"(t) : "s"(lds), "v"(voff), "s"(rs), "s"(soff), "i"(LI), "i"(SI) : "memory", "scc");
}
template <int K> DI v4u lda(rsrc_t rs, u32 voff, u32 soff) {
  v4u d; u32 t;
  asm volatile("s_add_u32 %1, %4, %5\n\tbuffer_load_dwordx4 %0, %2, %3, %1 offen offset:%6 sc1"
               : "=v"(d), "=&s"(t) : "v"(voff), "s"(rs), "s"(soff), "i"((K >> 2) * 4096), "i"((K & 3) * 1024) : "scc");
  return d;
}
template <int IMM> DI v4u ldp(rsrc_t rs, u32 voff, u32 soff) {
  v4u d;
  asm volatile("buffer_load_dwordx4 %0, %1, %2, %3 offen offset:%4" : "=v"(d) : "v"(voff), "s"(rs), "s"(soff), "i"(IMM));
  return d;
}
template <int IMM> DI v2u ldp2(rsrc_t rs, u32 voff) {
  v2u d;
  asm volatile("buffer_load_dwordx2 %0, %1, %2, 0 offen offset:%3" : "=v"(d) : "v"(voff), "s"(rs), "i"(IMM));
  return d;
}
DI void pin4(float (&v)[4]) { asm volatile("" : "+v"(v[0]), "+v"(v[1]), "+v"(v[2]), "+v"(v[3])); }
template <int IMM> DI u32 lda1(rsrc_t rs, u32 voff, u32 soff) {
  u32 d;
  asm volatile("buffer_load_dword %0, %1, %2, %3 offen offset:%4 sc1" : "=v"(d) : "v"(voff), "s"(rs), "s"(soff), "i"(IMM));
  return d;
}
template <int N> DI void vmw0() { asm volatile("s_waitcnt vmcnt(%0)" ::"i"(N) : "memory"); }
template <int N> DI void vmw(v4u& a) { asm volatile("s_waitcnt vmcnt(%1)" : "+v"(a) : "i"(N)); }
template <int N> DI void vmw(v4u& a, v4u& b, v4u& c, v4u& d) { asm volatile("s_waitcnt vmcnt(%4)" : "+v"(a), "+v"(b), "+v"(c), "+v"(d) : "i"(N)); }
template <int N> DI void vmw(v4u& a, v4u& b) { asm volatile("s_waitcnt vmcnt(%2)" : "+v"(a), "+v"(b) : "i"(N)); }
template <int I, int N, class Fn> DI void sfor(Fn&& f) { if constexpr (I < N) { f(std::integral_constant<int, I>{}); sfor<I + 1, N>(f); } }
DI u32 opq(u32 v) { asm volatile("" : "+s"(v)); return v; }
DI u32 opqv(u32 v) { asm volatile("" : "+v"(v)); return v; }
DI v2u trd(lptr p) { return __builtin_bit_cast(v2u, __builtin_amdgcn_ds_read_tr16_b64_v4i16((LAS s16x4*)p)); }
DI v4u trd2(lptr p0, lptr p1) { v2u a = trd(p0), b = trd(p1); return v4u{a.x, a.y, b.x, b.y}; }
DI v4u ld_sc1(rsrc_t rs, u32 voff, u32 soff) { return __builtin_amdgcn_raw_buffer_load_b128(rs, voff, soff, 16); }
DI void st_sc1(v4u v, rsrc_t rs, u32 voff, u32 soff) { __builtin_amdgcn_raw_buffer_store_b128(v, rs, voff, soff, 16); }
DI rsrc_t mkrs(const void* p, u32 bytes) {
  u32 fl = 0x00020000;
  asm volatile("" : "+s"(p), "+s"(bytes), "+s"(fl));
  return __builtin_amdgcn_make_buffer_rsrc((void*)p, 0, bytes, fl);
}
DI float half_add(float v) {
  auto r = __builtin_amdgcn_permlane32_swap(asu(v), asu(v), false, false);
  const u32 a = r[0], b = r[1];
  return asf(a) + asf(b);
}
DI float half_max(float v) {
  auto r = __builtin_amdgcn_permlane32_swap(asu(v), asu(v), false, false);
  const u32 a = r[0], b = r[1];
  return fmaxf(asf(a), asf(b));
}
DI bool spin_out(unsigned char* ws, u32& spins, u32 tl) {
  ++spins;
  if ((spins & 63u) == 0u) {
    GAS u32* tmo = (GAS u32*)(ws + WS_TMO);
    if (__hip_atomic_load(tmo, RLX_AGENT) == tl) return true;
    if (spins > kSpinCap) { __hip_atomic_store(tmo, tl, RLX_AGENT); return true; }
  }
  return false;
}
struct Poll { v2u a, b, c; };
#define PL(R, O, B) "global_load_dwordx2 " R ", " O ", " B " sc1\n\t"
DI v2u poll_one(unsigned char* ws, u32 off) {
  v2u r;
  asm volatile(PL("%0", "%1", "%2") : "=v"(r) : "v"(off), "s"(ws) : "memory");
  return r;
}
DI void poll_finish(Poll& q, unsigned char* ws, u32 off, u64 tag) {
  u32 left;
#define PC(R) "s_waitcnt vmcnt(2)\n\tv_cmp_ne_u64 vcc, %6, " R "\n\ts_cbranch_vccz 2f\n\t" PL(R, "%4", "%5")
  asm volatile("s_mov_b32 %3, 0x8000\n1:\n\t" PC("%0") PC("%1") PC("%2") "s_sub_u32 %3, %3, 1\n\ts_cbranch_scc0 1b\n2:"
               : "+v"(q.a), "+v"(q.b), "+v"(q.c), "=&s"(left) : "v"(off), "s"(ws), "s"(tag) : "vcc", "scc", "memory");
#undef PC
  if (left == 0xffffffffu) { drain(); __hip_atomic_store((GAS u32*)(ws + WS_TMO), (u32)tag, RLX_AGENT); }
}
DI Poll wait_flags(unsigned char* ws, u32 off, u64 tag) {
  Poll q;
  q.a = poll_one(ws, off); asm volatile("s_sleep " POLL_GAP);
  q.b = poll_one(ws, off); asm volatile("s_sleep " POLL_GAP);
  q.c = poll_one(ws, off);
  poll_finish(q, ws, off, tag);
  return q;
}
DI void poll_done(const Poll& q) { asm volatile("" ::"v"(q.a), "v"(q.b), "v"(q.c)); }
DI u32 touch(rsrc_t rs, u32 voff, u32 soff) {
  u32 d;
  asm volatile("s_nop 4\n\tbuffer_load_dword %0, %1, %2, %3 offen" : "=v"(d) : "v"(voff), "s"(rs), "s"(soff));
  return d;
}
DI void touch_done(u32 d) { asm volatile("" ::"v"(d)); }
DI void publish(unsigned char* ws, u32 off, u64 tag) {
  asm volatile("s_waitcnt vmcnt(0) lgkmcnt(0)\n\ts_barrier" ::: "memory");
  if (threadIdx.x == 0) __hip_atomic_store((GAS u64*)(ws + off), tag, RLX_AGENT);
  asm volatile("" ::: "memory");
}
DI u32 flag_off(int l, int type, int idx) { return WS_FLAGS + (u32)(((l * 5 + type) << 8) + idx) * 128u; }

#define STAMP(i) do { u64 t0_, t1_; asm volatile("s_memrealtime %0\n\ts_memtime %1\n\ts_waitcnt lgkmcnt(0)" : "=s"(t0_), "=s"(t1_)); \
  const u32 sl_ = l * 32 + (i); asm volatile("s_mov_b32 m0, %4\n\tv_writelane_b32 %0, %2, m0\n\tv_writelane_b32 %1, %3, m0" : "+v"(dbgR), "+v"(dbgC) : "s"((u32)t0_), "s"((u32)t1_), "s"(sl_)); } while (0)

DI f32x16 reduce4(lptr L, const f32x16 (&acc)[4], int w, int lane) {
#pragma unroll
  for (int q = 1; q < 4; ++q) {
    lptr dst = L + L_INBOX + (u32)(w ^ q) * INBOX_W + (q - 1) * 4096 + lane * 16;
#pragma unroll
    for (int i = 0; i < 4; ++i)
      *(LAS f32x4*)(dst + i * 1024) = f32x4{acc[q][4 * i], acc[q][4 * i + 1], acc[q][4 * i + 2], acc[q][4 * i + 3]};
  }
  bar();
  f32x16 r = acc[0];
  lptr src = L + L_INBOX + (u32)w * INBOX_W + lane * 16;
#pragma unroll
  for (int q = 0; q < 3; ++q)
#pragma unroll
    for (int i = 0; i < 4; ++i) {
      const f32x4 v = *(LAS f32x4*)(src + q * 4096 + i * 1024);
      r[4 * i] += v[0]; r[4 * i + 1] += v[1]; r[4 * i + 2] += v[2]; r[4 * i + 3] += v[3];
    }
  return r;
}

template <int P0, int P1, u32 RS> DI void w_issue(rsrc_t rs, u32 voff, u32 soff, u32 ring) {
  sfor<P0, P1>([&](auto I) { constexpr int i = I; dma16<i * 8 * RS, (i % 24) * 1024>(rs, voff, soff, ring); });
}
template <int P0, int P1, u32 RS, int HP> DI void w_issue2(rsrc_t rs, u32 voff, u32 s0, u32 s1, u32 ring) {
  sfor<P0, P1>([&](auto I) { constexpr int i = I; dma16<(i % HP) * 8 * RS, (i % 24) * 1024>(rs, voff, i < HP ? s0 : s1, ring); });
}
template <int K0, int K1, int TSTEP, int NR> DI void w_take(lptr t0, lptr t1, v4u (&bw)[NR][2]) {
#pragma unroll
  for (int kk = K0; kk < K1; ++kk) {
    const int sl = (kk % 12) * 2048;
    bw[kk][0] = trd2(t0 + sl, t0 + sl + TSTEP);
    bw[kk][1] = trd2(t1 + sl, t1 + sl + TSTEP);
  }
}
template <int NR> DI void w_undef(v4u (&bw)[NR][2], int k0, int k1) {
#pragma unroll
  for (int kk = 0; kk < NR; ++kk) if (kk >= k0 && kk < k1) { asm volatile("" : "=v"(bw[kk][0])); asm volatile("" : "=v"(bw[kk][1])); }
}

template <int NK, int NR, int TSTEP, bool WA, int LA, class Tail>
DI void gemm_main(rsrc_t ws, u32 voff, u32 a0, u32 a1, u32 rot, lptr t0, lptr t1, const v4u (&bw)[NR][2], f32x16 (&acc)[4], Tail&& tail) {
  static_assert(NK - NR == 12 && NK - LA >= 13, "ring is 12 slots and must be consumed before the tail");
  v4u a[NK][2];
  constexpr int HK = NK / 2;
  const u32 b0[2] = {a0 + rot * (HK * 1024), a0 + (rot ^ 1u) * (HK * 1024)}, b1[2] = {a1 + rot * (HK * 1024), a1 + (rot ^ 1u) * (HK * 1024)};
  sfor<0, LA>([&](auto I) { constexpr int n = I, k = (n + NR) % NK; a[n][0] = lda<k % HK>(ws, voff, b0[k / HK]); a[n][1] = lda<k % HK>(ws, voff, b1[k / HK]); });
  v4u wl[12][2];
  wl[0][0] = trd2(t0 + (NR % 12) * 2048, t0 + (NR % 12) * 2048 + TSTEP); wl[0][1] = trd2(t1 + (NR % 12) * 2048, t1 + (NR % 12) * 2048 + TSTEP);
#pragma unroll
  for (int q = 0; q < 4; ++q)
#pragma unroll
    for (int i = 0; i < 16; ++i) acc[q][i] = 0.f;
  wl[1][0] = trd2(t0 + ((NR + 1) % 12) * 2048, t0 + ((NR + 1) % 12) * 2048 + TSTEP); wl[1][1] = trd2(t1 + ((NR + 1) % 12) * 2048, t1 + ((NR + 1) % 12) * 2048 + TSTEP);
  SB();
  sfor<0, NK>([&](auto I) {
    constexpr int n = I, kn = (n + LA + NR) % NK, sl = ((NR + n + 2) % 12) * 2048;
    vmw<2 * LA - 2>(a[n][0], a[n][1]);
    v4u w0, w1;
    if constexpr (n < 12) { w0 = wl[n][0]; w1 = wl[n][1]; } else { w0 = bw[n - 12][0]; w1 = bw[n - 12][1]; }
    acc[1] = WA ? mfma(w0, a[n][1], acc[1]) : mfma(a[n][0], w1, acc[1]);
    SB();
    if constexpr (n + LA < NK) a[n + LA][0] = lda<kn % HK>(ws, voff, b0[kn / HK]); else tail(std::integral_constant<int, 2 * (n + LA - NK)>{});
    SB();
    acc[2] = WA ? mfma(w1, a[n][0], acc[2]) : mfma(a[n][1], w0, acc[2]);
    SB();
    if constexpr (n + 2 < 12) wl[n + 2][0] = trd2(t0 + sl, t0 + sl + TSTEP);
    SB();
    acc[3] = WA ? mfma(w1, a[n][1], acc[3]) : mfma(a[n][1], w1, acc[3]);
    SB();
    if constexpr (n + LA < NK) a[n + LA][1] = lda<kn % HK>(ws, voff, b1[kn / HK]); else tail(std::integral_constant<int, 2 * (n + LA - NK) + 1>{});
    SB();
    acc[0] = WA ? mfma(w0, a[n][0], acc[0]) : mfma(a[n][0], w0, acc[0]);
    SB();
    if constexpr (n + 2 < 12) wl[n + 2][1] = trd2(t1 + sl, t1 + sl + TSTEP);
    SB();
  });
}

__global__ __launch_bounds__(256, 1) void fused_fwd(Params p) {
  extern __shared__ __attribute__((aligned(16))) unsigned char smem[];
  lptr L = (lptr)smem;
  const u32 lds0 = (u32)(size_t)L;
#define IDS \
  const int tid = (int)opqv(threadIdx.x), lane = tid & 63, r = lane & 31, h = lane >> 5; \
  const int w = (int)opq(w_), b = (int)opq(blockIdx.x), g = b & 7, bi = b >> 3, pj = g >> 1; \
  const int T = 2 * g + (bi >> 4), head = bi & 15; \
  const int d3 = (bi * 43) >> 7, m3 = bi - 3 * d3; \
  const int st = 8 * pj + ((g & 1) ? d3 : (bi >> 2)), dt = (g & 1) ? 4 + m3 : (bi & 3); \
  const int ft = 7 * pj + ((g & 1) ? (bi >> 2) : d3), dt4 = (g & 1) ? (bi & 3) : 4 + m3; \
  const int wa = w >> 1, wb = w & 1; \
  const int ch16 = (lane >> 4) & 1, tq = (lane & 15) >> 2, tp = lane & 3; \
  const u32 ringw = L_RING + (u32)w * RING_W; \
  const u32 dmarow = lane >> 3, dmach = ((lane & 7) ^ (((lane >> 4) & 1) << 2)) * 16; \
  const u32 sw = (u32)((tq >> 1) & 1) << 2, cpart = 2 * ch16 + (tp >> 1); \
  (void)r; (void)h; (void)T; (void)head; (void)st; (void)dt; (void)ft; (void)dt4; (void)wa; (void)wb; (void)ch16; (void)tq; (void)tp; (void)ringw; (void)dmarow; (void)dmach; (void)sw; (void)cpart;
  const u32 w_ = __builtin_amdgcn_readfirstlane(threadIdx.x >> 6);
  const bool p35 = (blockIdx.x & 1) == 0 || (blockIdx.x >> 3) < 24;
  const bool p4 = (blockIdx.x >> 3) < ((blockIdx.x & 1) ? 28u : 21u);

  const rsrc_t rws = mkrs(p.ws, WS_END);
  u64 tag = 0;
  u32 dbgR = 0, dbgC = 0;
  u32 xres[8];
  v4u wf[8][2];
#pragma unroll
  for (int i = 0; i < 8; ++i) { xres[i] = 0; asm volatile("" : "=a"(wf[i][0])); asm volatile("" : "=a"(wf[i][1])); }

#pragma unroll 1
  for (int l = 0; l < 2; ++l) {
    STAMP(0);
    const rsrc_t rw1 = mkrs(p.w1, p4 ? S_ * F_ * 2 : 0);
    const rsrc_t rw2 = mkrs(p.w2, p35 ? F_ * S_ * 2 : 0);
#define W2_ARGS rw2, dmarow * 4096u + dmach + (u32)st * 128u, (u32)(448 * w + 224 * ROT5) * 4096u, (u32)(448 * w + 224 * (ROT5 ^ 1)) * 4096u, lds0 + ringw
#define W1_ARGS rw1, dmarow * 3584u + dmach + (u32)ft * 128u, (u32)(512 * w + 256 * ROT4) * 3584u, (u32)(512 * w + 256 * (ROT4 ^ 1)) * 3584u, lds0 + ringw
    {
    IDS
    const int tb = 4 * T + w;
    const u32 shift = (head & 1) * 8;
    const u32 half = (((tq >> 1) & 1) ^ 1) * 64, col = shift + ch16 * 32 + tp * 8;
    lptr wtk = L + (32 * h + tq) * 128 + half + col, wtv = L + (32 * h + tq) * 128 + (half ^ 64) + col, wtq = L + L_WQ + (32 * h + tq) * 64 + col;
    v4u X[28];
    f32x16 kt, vv;
#pragma unroll
    for (int i = 0; i < 16; ++i) { kt[i] = 0.f; vv[i] = 0.f; }
    float ssq;
    u32 tc2 = 0, tc3 = 0, tc4 = 0, tc5 = 0, tc6 = 0;
    v4u wk[28], wv[28], wq[28];
    constexpr int PD = 3;
#define WROW(k) (((k) >> 2) * 64 + ((k) & 3) * 8)
#define WFK(k) trd2(wtk + WROW(k) * 128, wtk + WROW(k) * 128 + 512)
#define WFV(k) trd2(wtv + WROW(k) * 128, wtv + WROW(k) * 128 + 512)
#define WFQ(k) trd2(wtq + WROW(k) * 64, wtq + WROW(k) * 64 + 256)
    auto kv_head = [&]() {
#pragma unroll
      for (int k = 0; k < PD; ++k) { wk[k] = WFK(k); wv[k] = WFV(k); }
      SB();
    };
    auto kv_step = [&](auto I, auto&& mem) {
      constexpr int k = I;
      kt = mfma(wk[k], X[k], kt);
      SB();
      if constexpr (k + PD < 28) wk[k + PD] = WFK(k + PD);
      mem();
      SB();
      vv = mfma(X[k], wv[k], vv);
      SB();
      if constexpr (k + PD < 28) wv[k + PD] = WFV(k + PD);
      SB();
    };
    const v4u ones = {0x3F803F80u, 0x3F803F80u, 0x3F803F80u, 0x3F803F80u};
    if (l == 0) {
      v2u e0;
      asm volatile("global_load_dwordx2 %0, %1, %2 sc1" : "=v"(e0) : "v"(WS_EPOCH), "s"(p.ws));
      u32 tcc;
      {
        u64 pc; u32 back, len;
        asm volatile("s_getpc_b64 %0\n.Lfwd_here:\n\ts_mov_b32 %1, .Lfwd_here-_Z9fused_fwd6Params\n\ts_mov_b32 %2, .Lfwd_last-_Z9fused_fwd6Params" : "=s"(pc), "=s"(back), "=s"(len));
        tcc = touch(mkrs((const void*)(pc - back), CODE_TOUCH ? len : 0), lane * 128, (u32)(bi * 4 + w) * 8192u);
      }
      const rsrc_t rx = mkrs(p.x, S_ * D_ * 2), rwq = mkrs(p.wqkv, D_ * 3 * D_ * 2);
      const u32 hoff = (u32)(head * 56) - shift;
      const u32 xv = dmarow * 896u + (lane & 7) * 16u, xs = (u32)(tb * 32) * 896u;
      lptr xb = L + L_WO + w * 15360;
      lptr xw = xb + dmarow * 128, xr = xb + r * 128;
      const u32 xsw = (r >> 1) & 7, xc0 = ((lane & 7) ^ (lane >> 4)) * 16;
      const u32 kvv = dmarow * 2688u + 896u + ((((lane >> 2) ^ (lane >> 4) ^ 1) & 1) * 896u) + (lane & 3) * 16u + hoff;
      const u32 kvs = (u32)(112 * w) * 2688u, kvl0 = lds0 + (u32)(14 * w) * 1024u;
      const rsrc_t rwo = mkrs(p.wo, D_ * D_ * 2);
      const u32 wov = dmarow * 896u + dmach + (u32)dt * 128u, wos = (u32)(112 * w) * 896u, wol = lds0 + L_WO + (u32)w * 15360u;
      const u32 qv = (u32)(lane >> 2) * 2688u + (lane & 3) * 16u + hoff, ql = lds0 + L_WQ + (u32)(7 * w) * 1024u;
      sfor<0, 28>([&](auto I) { constexpr int k = I; X[k] = ldp<(k >> 2) * 128>(rx, xv, xs + (k & 3) * 7168u); });
      sfor<0, 7>([&](auto I) { constexpr int i = I; dma16<i * 8 * 2688, i * 1024>(rwq, kvv, kvs, kvl0); });
      STAMP(24);
      float sq4[4] = {0.f, 0.f, 0.f, 0.f};
      sfor<0, 7>([&](auto I) {
        constexpr int G = I;
        vmw<24 - 4 * G + 7 + 2 * G>(X[4 * G], X[4 * G + 1], X[4 * G + 2], X[4 * G + 3]);
#pragma unroll
        for (int i = 0; i < 4; ++i) *(LAS v4u*)(xw + i * 1024 + (xc0 ^ ((i & 1) * 64))) = X[4 * G + i];
#pragma unroll
        for (int c = 0; c < 4; ++c) X[4 * G + c] = *(LAS v4u*)(xr + (((4 * h + c) ^ xsw) * 16));
        if constexpr (2 * G < 7) dma16<(7 + 2 * G) * 8 * 2688, (7 + 2 * G) * 1024>(rwq, kvv, kvs, kvl0); else dma16<(2 * G - 7) * 16 * 2688, (2 * G - 7) * 1024>(rwq, qv, kvs, ql);
        if constexpr (2 * G + 1 < 7) dma16<(8 + 2 * G) * 8 * 2688, (8 + 2 * G) * 1024>(rwq, kvv, kvs, kvl0); else dma16<(2 * G - 6) * 16 * 2688, (2 * G - 6) * 1024>(rwq, qv, kvs, ql);
#pragma unroll
        for (int i = 0; i < 16; ++i) { const bf16x2 xx = asbf2(X[4 * G + (i >> 2)][i & 3]); sq4[i & 3] = __builtin_amdgcn_fdot2_f32_bf16(xx, xx, sq4[i & 3], false); }
        pin4(sq4);
        SB();
      });
      ssq = (sq4[0] + sq4[1]) + (sq4[2] + sq4[3]);
      STAMP(25);
      f32x16 ck, cv;
#pragma unroll
      for (int i = 0; i < 16; ++i) { ck[i] = 0.f; cv[i] = 0.f; }
      lptr ck0 = L + (112 * w + 8 * h + tq) * 128 + half + col, cv0 = L + (112 * w + 8 * h + tq) * 128 + (half ^ 64) + col;
      vmw0<7>();
      STAMP(26);
      {
        v4u f[14];
#pragma unroll
        for (int i = 0; i < 7; ++i) { f[2 * i] = trd2(ck0 + i * 2048, ck0 + i * 2048 + 512); f[2 * i + 1] = trd2(cv0 + i * 2048, cv0 + i * 2048 + 512); }
        SB();
        sfor<0, 7>([&](auto I) { constexpr int i = I;
          ck = mfma(ones, f[2 * i], ck); SB(); w_issue<2 * i, 2 * i + 1, 896u>(rwo, wov, wos, wol); SB();
          cv = mfma(ones, f[2 * i + 1], cv); SB(); w_issue<2 * i + 1, 2 * i + 2, 896u>(rwo, wov, wos, wol); SB(); });
        w_issue<14, 15, 896u>(rwo, wov, wos, wol);
      }
      {
        const u32 gw = (u32)(b * 4 + w);
        const u32 l2 = gw * 24 + (lane < 24 ? lane : 0), r2 = l2 >> 5, l1 = gw * 21 + (lane < 21 ? lane : 0), r1 = l1 / 28;
        tc2 = touch(mkrs(p.w2, TOUCH ? S_ * F_ * 2 : 0), ((r2 / 192) * 448 + r2 % 192) * 4096u + (l2 & 31) * 128u, 0);
        tc3 = touch(mkrs(p.w1, TOUCH ? S_ * F_ * 2 : 0), ((r1 / 192) * 512 + r1 % 192) * 3584u + (l1 % 28) * 128u, 0);
      }
      STAMP(27);
      touch_done(tcc);
      asm volatile("" : "+v"(e0));
      tag = (((u64)(u32)__builtin_amdgcn_readfirstlane(e0.y) << 32) | (u64)(u32)__builtin_amdgcn_readfirstlane(e0.x)) + 1ull;
      *(LAS float*)(L + L_CSP + w * 384 + 128 + lane * 4) = h ? cv[0] : ck[0];
      bar();
      {
        lptr c28 = L + (112 * w + lane) * 128 + (((lane >> 1) & 1) ^ 1) * 64 + shift + 56;
        *(LAS unsigned short*)c28 = 0x3F80;
        if (lane < 48) *(LAS unsigned short*)(c28 + 64 * 128) = 0x3F80;
        float a0 = 0.f;
#pragma unroll
        for (int v = 0; v < 4; ++v) a0 += *(LAS float*)(L + L_CSP + v * 384 + 128 + lane * 4);
        *(LAS float*)(L + L_CSF + w * 512 + 128 + lane * 4) = a0;
      }
      bar();
      kv_head();
      STAMP(1);
      SB();
      sfor<0, 28>([&](auto I) { kv_step(I, []() {}); });
      asm volatile("" : "+a"(kt), "+a"(vv));
    } else {
      kv_head();
      const Poll pq = wait_flags(p.ws, flag_off(0, 4, (2 * T + (w >> 1)) * 8 + (lane < 7 ? lane : 0)), tag);
      STAMP(1);
      const u32 xb = WS_XF + (u32)(tb * 28) * 1024u;
      const u32 sqv = (u32)(7 * h) * 128u + r * 4, sqs = WS_SQ + (u32)tb * 1792u;
      u32 sp[7];
      sfor<0, LA1>([&](auto I) { constexpr int k = I; X[k] = lda<k>(rws, lane * 16, xb); });
      sfor<0, 28>([&](auto I) {
        constexpr int k = I;
        vmw<(k + LA1 <= 35) ? LA1 - 1 : 34 - k>(X[k]);
        if constexpr (k == 0) poll_done(pq);
        kv_step(I, [&]() {
          if constexpr (k + LA1 < 28) X[k + LA1] = lda<k + LA1>(rws, lane * 16, xb);
          else if constexpr (k + LA1 < 35) sp[k + LA1 - 28] = lda1<(k + LA1 - 28) * 128>(rws, sqv, sqs);
        });
      });
      asm volatile("s_waitcnt vmcnt(0)" : "+v"(sp[0]), "+v"(sp[1]), "+v"(sp[2]), "+v"(sp[3]), "+v"(sp[4]), "+v"(sp[5]), "+v"(sp[6]));
      asm volatile("" : "+a"(kt), "+a"(vv));
      ssq = ((asf(sp[0]) + asf(sp[1])) + (asf(sp[2]) + asf(sp[3]))) + ((asf(sp[4]) + asf(sp[5])) + asf(sp[6]));
    }
    STAMP(18);
    float mean, rstd;
    {
      const float sum = half_add(h ? kt[12] : 0.f);
      const float sq = half_add(ssq);
      mean = sum * (1.f / 448.f);
      const float var = fmaxf(sq * (1.f / 448.f) - mean * mean, 0.f);
      rstd = __builtin_amdgcn_rsqf(var + EPS);
    }
    const float mr = mean * rstd;
    *(LAS float*)(L + L_MR + w * 256 + h * 128 + r * 4) = h ? mr : rstd;
    const u32 kvbase = WS_KV + l * KV_L + (u32)head * (64u * 4096u);
    lptr kvl = L + L_INBOX + lane * 16;
    v4u K0, K1, V0, V1;
    {
      u32 o[8];
#pragma unroll
      for (int i = 0; i < 4; ++i) {
        const f32x4 cs = *(LAS f32x4*)(L + L_CSF + w * 512 + 128 + (8 * i + 4 * h) * 4);
        o[2 * i] = pk(kt[4 * i] * rstd - mr * cs[0], kt[4 * i + 1] * rstd - mr * cs[1]);
        o[2 * i + 1] = pk(kt[4 * i + 2] * rstd - mr * cs[2], kt[4 * i + 3] * rstd - mr * cs[3]);
      }
      if (h) { o[6] = 0u; o[7] = 0u; }
      K0 = v4u{o[0], o[1], o[2], o[3]}; K1 = v4u{o[4], o[5], o[6], o[7]};
      st_sc1(K0, rws, lane * 16, kvbase + tb * 4096);
      st_sc1(K1, rws, lane * 16 + 1024, kvbase + tb * 4096);
      const float csv = *(LAS float*)(L + L_CSF + w * 512 + 256 + r * 4);
#pragma unroll
      for (int i = 0; i < 4; ++i) {
        const f32x4 rr = *(LAS f32x4*)(L + L_MR + w * 256 + (8 * i + 4 * h) * 4);
        const f32x4 mm = *(LAS f32x4*)(L + L_MR + w * 256 + 128 + (8 * i + 4 * h) * 4);
        o[2 * i] = pk(vv[4 * i] * rr[0] - mm[0] * csv, vv[4 * i + 1] * rr[1] - mm[1] * csv);
        o[2 * i + 1] = pk(vv[4 * i + 2] * rr[2] - mm[2] * csv, vv[4 * i + 3] * rr[3] - mm[3] * csv);
      }
      if (r >= 28) {
#pragma unroll
        for (int i = 0; i < 8; ++i) o[i] = 0x3F803F80u;
      }
      V0 = v4u{o[0], o[1], o[2], o[3]}; V1 = v4u{o[4], o[5], o[6], o[7]};
      st_sc1(V0, rws, lane * 16 + 2048, kvbase + tb * 4096);
      st_sc1(V1, rws, lane * 16 + 3072, kvbase + tb * 4096);
    }
    STAMP(19);
    if (l == 0) {
      vmw0<TLBT ? 8 : 6>();
      lptr t0 = L + L_WO + w * 15360 + (4 * h + tq) * 128 + (((4 * wa + cpart) ^ sw) * 16) + (tp & 1) * 8;
      lptr t1 = L + L_WO + w * 15360 + (4 * h + tq) * 128 + (((4 * (wa ^ 1) + cpart) ^ sw) * 16) + (tp & 1) * 8;
      lptr cq0 = L + L_WQ + (112 * w + 8 * h + tq) * 64 + col;
      v4u f[7];
#pragma unroll
      for (int i = 0; i < 7; ++i) f[i] = trd2(cq0 + i * 1024, cq0 + i * 1024 + 256);
#pragma unroll
      for (int k = 0; k < 8; ++k) {
        const int ro = (28 * (k >> 1) + 16 * (k & 1)) * 128;
        wf[k][0] = trd2(t0 + ro, t0 + ro + 1024); wf[k][1] = trd2(t1 + ro, t1 + ro + 1024);
      }
      SB();
      f32x16 cq;
#pragma unroll
      for (int i = 0; i < 16; ++i) cq[i] = 0.f;
#pragma unroll
      for (int i = 0; i < 7; ++i) cq = mfma(ones, f[i], cq);
      *(LAS float*)(L + L_CSP + w * 384 + r * 4) = cq[0];
      st_sc1(v4u{0u, 0u, 0u, 0u}, rws, head < 14 ? WS_STATS + h * STATS_L + (u32)((tb * 14 + head) * 512 + r * 16) : 0x40000000u, 0);
    }
    publish(p.ws, flag_off(l, 0, T * 16 + head), tag);
    touch_done(tc2); touch_done(tc3); touch_done(tc5); touch_done(tc6);
    STAMP(2);
#pragma unroll
    for (int k = 0; k < 2 * PD; ++k) wq[k] = WFQ(k);
    {
      lptr own = kvl + (8 + w) * 4096;
      *(LAS v4u*)own = K0; *(LAS v4u*)(own + 1024) = K1; *(LAS v4u*)(own + 2048) = V0; *(LAS v4u*)(own + 3072) = V1;
    }
    if (l == 0) {
      float a0 = 0.f;
#pragma unroll
      for (int v = 0; v < 4; ++v) a0 += *(LAS float*)(L + L_CSP + v * 384 + r * 4);
      *(LAS float*)(L + L_CSF + w * 512 + r * 4) = a0;
      const u32 wib = WS_WI + (u32)head * 86016u + (u32)tb * 1024u;
      st_sc1(*(LAS v4u*)(L + tb * 1024 + lane * 16), rws, lane * 16, wib);
      st_sc1(*(LAS v4u*)(L + (tb < 20 ? tb + 64 : tb) * 1024 + lane * 16), rws, lane * 16, tb < 20 ? wib + 65536u : 0x40000000u);
      const u32 xo = (u32)((2 * st + wb) * 32 + r) * 896u + (u32)(dt * 64 + wa * 32 + 4 * h) * 2u;
      const rsrc_t rx = mkrs(p.x, S_ * D_ * 2);
#pragma unroll
      for (int i = 0; i < 4; ++i) {
        const v2u v = __builtin_amdgcn_raw_buffer_load_b64(rx, xo + i * 16, 0, 0);
        xres[2 * i] = v.x; xres[2 * i + 1] = v.y;
      }
    }
    {
      const int tsrc = T - 2 + (w >> 1);
      const u32 f1 = opqv(flag_off(l, 0, (tsrc < 0 ? T : tsrc) * 16 + head));
      Poll pq;
      f32x16 qt;
#pragma unroll
      for (int i = 0; i < 16; ++i) qt[i] = 0.f;
      SB();
      sfor<0, 28>([&](auto I) {
        constexpr int k = I;
        qt = mfma(wq[k], X[k], qt);
        SB();
        if constexpr (k + 2 * PD < 28) wq[k + 2 * PD] = WFQ(k + 2 * PD);
        if constexpr (k == QP0) pq.a = poll_one(p.ws, f1);
        if constexpr (k == QP0 + 6) pq.b = poll_one(p.ws, f1);
        if constexpr (k == QP0 + 12) pq.c = poll_one(p.ws, f1);
        SB();
      });
      asm volatile("" : "+a"(qt));
      poll_finish(pq, p.ws, f1, tag);
      STAMP(3);
      const u32 so = tsrc >= 0 ? kvbase + (u32)(4 * T - 8 + 2 * w) * 4096u : 0x40000000u;
      v4u t[8];
      t[0] = lda<0>(rws, lane * 16, so); t[1] = lda<1>(rws, lane * 16, so); t[2] = lda<4>(rws, lane * 16, so); t[3] = lda<5>(rws, lane * 16, so);
      t[4] = lda<2>(rws, lane * 16, so); t[5] = lda<3>(rws, lane * 16, so); t[6] = lda<6>(rws, lane * 16, so); t[7] = lda<7>(rws, lane * 16, so);
      SB();
      v4u Q0, Q1;
      {
        const float sc = 0.18898223650461362f * 1.4426950408889634f;
        const float rs = rstd * sc, ms = mr * sc;
        u32 o[8];
#pragma unroll
        for (int i = 0; i < 4; ++i) {
          const f32x4 cs = *(LAS f32x4*)(L + L_CSF + w * 512 + (8 * i + 4 * h) * 4);
          o[2 * i] = pk(qt[4 * i] * rs - ms * cs[0], qt[4 * i + 1] * rs - ms * cs[1]);
          o[2 * i + 1] = pk(qt[4 * i + 2] * rs - ms * cs[2], qt[4 * i + 3] * rs - ms * cs[3]);
        }
        if (h) { o[6] = 0x00003F80u; o[7] = 0u; }
        Q0 = v4u{o[0], o[1], o[2], o[3]}; Q1 = v4u{o[4], o[5], o[6], o[7]};
      }
      f32x16 s8 = qk_own(K0, K1, Q0, Q1);
      u32 em[16], pm[8];
      f32x16 c0;
#pragma unroll
      for (int i = 0; i < 16; ++i) { const int key = (i & 3) + 8 * (i >> 2) + 4 * h; em[i] = key > r ? 0xffffffffu : 0u; c0[i] = asf(~em[i] & 0xF149F2CAu); }
#pragma unroll
      for (int i = 0; i < 8; ++i) pm[i] = (em[2 * i] & 0x0000ffffu) | (em[2 * i + 1] & 0xffff0000u);
#pragma unroll
      for (int i = 0; i < 16; ++i) asm volatile("" : "+v"(em[i]));
#pragma unroll
      for (int i = 0; i < 8; ++i) asm volatile("" : "+v"(pm[i]));
      tie_far(s8);
      float m4[4];
#pragma unroll
      for (int i = 0; i < 16; ++i) s8[i] = asf((em[i] & 0xF149F2CAu) | (~em[i] & asu(s8[i])));
#pragma unroll
      for (int i = 0; i < 4; ++i) m4[i] = fmaxf(fmaxf(s8[4 * i], s8[4 * i + 1]), fmaxf(s8[4 * i + 2], s8[4 * i + 3]));
      asm volatile("" : "+v"(c0), "+v"(m4[0]), "+v"(m4[1]), "+v"(m4[2]), "+v"(m4[3]));
      SB();
      {
        lptr dst = kvl + w * 8192;
        vmw<4>(t[0], t[1], t[2], t[3]);
        poll_done(pq);
        if (tsrc < 0 && h) { t[1][2] = 0xF149u; t[3][2] = 0xF149u; }
#pragma unroll
        for (int i = 0; i < 4; ++i) *(LAS v4u*)(dst + (i & 1) * 1024 + (i >> 1) * 4096) = t[i];
        vmw<0>(t[4], t[5], t[6], t[7]);
#pragma unroll
        for (int i = 0; i < 4; ++i) *(LAS v4u*)(dst + 2048 + (i & 1) * 1024 + (i >> 1) * 4096) = t[4 + i];
      }
      bar();
      {
        const u32 l1 = (u32)(b * 4 + w) * 35 + (lane < 35 ? lane : 0), r1 = l1 / 28;
        tc4 = touch(mkrs(p.w1, (TOUCH && l == 0) ? S_ * F_ * 2 : 0), ((r1 / 320) * 512 + 192 + r1 % 320) * 3584u + (l1 % 28) * 128u, 0);
      }
      STAMP(17);
      lptr kq = kvl + w * 4096;
      v4u Kr[8][2], Vr[8][2];
#pragma unroll
      for (int j = 0; j < 4; ++j) { Kr[j][0] = *(LAS v4u*)(kq + j * 4096); Kr[j][1] = *(LAS v4u*)(kq + j * 4096 + 1024); }
      SB();
      {
        f32x16 s[8];
        auto mx = [&](const f32x16& t, int half) {
#pragma unroll
          for (int i = 0; i < 4; ++i) m4[i] = fmaxf(fmaxf(m4[i], t[8 * half + 2 * i]), t[8 * half + 2 * i + 1]);
        };
        sfor<0, 8>([&](auto I) {
          constexpr int n = I;
          if constexpr (n == 0) s[0] = qk1(Kr[0][0], Q0, c0); else s[n] = qk1z(Kr[n][0], Q0);
          SB();
          if constexpr (n < 4) Kr[n + 4][0] = *(LAS v4u*)(kq + (n + 4) * 4096);
          Vr[n][0] = *(LAS v4u*)(kq + n * 4096 + 2048);
          if constexpr (n >= 2) mx(s[n - 2], 1);
          SB();
          qk2(s[n], Kr[n][1], Q1);
          SB();
          if constexpr (n < 4) Kr[n + 4][1] = *(LAS v4u*)(kq + (n + 4) * 4096 + 1024);
          Vr[n][1] = *(LAS v4u*)(kq + n * 4096 + 3072);
          if constexpr (n >= 1) { tie(s[n - 1]); mx(s[n - 1], 0); }
          SB();
        });
        mx(s[6], 1);
        SB();
        asm volatile("s_nop 7" : "+v"(s[7]));
        mx(s[7], 0); mx(s[7], 1);
      }
      const float nm = -half_max(fmaxf(fmaxf(m4[0], m4[1]), fmaxf(m4[2], m4[3])));
      f32x16 cm, o;
#pragma unroll
      for (int i = 0; i < 16; ++i) { cm[i] = nm; o[i] = 0.f; }
      {
        f32x16 s[9];
        u32 pp[9][8];
        float e[16];
        asm volatile("" : "+v"(cm));
        SB(); nop2();
        s[1] = qk1(Kr[1][0], Q0, cm); qk2(s[1], Kr[1][1], Q1);
        s8 = qk_own_c(K0, K1, Q0, Q1, cm);
        SB();
        sfor<1, 9>([&](auto I) {
          constexpr int c = I, nx = (c + 1) & 7;
          tie(s[c]);
          if constexpr (c == 8) {
            tie(s8);
#pragma unroll
            for (int i = 0; i < 16; ++i) s[8][i] = asf((asu(s[8][i]) & em[i]) | (asu(s8[i]) & ~em[i]));
          }
          auto ex = [&](int q) {
#pragma unroll
            for (int i = 0; i < 4; ++i) e[4 * q + i] = __builtin_amdgcn_exp2f(s[c][4 * q + i]);
          };
          auto cv = [&](int q) { pp[c][2 * q] = pk(e[4 * q], e[4 * q + 1]); pp[c][2 * q + 1] = pk(e[4 * q + 2], e[4 * q + 3]); };
          if constexpr (c < 8) s[c + 1] = qk1(Kr[nx][0], Q0, cm);
          SB(); if constexpr (c < 7) w_issue2<4 * c - 4, 4 * c - 3, 4096u, 28>(W2_ARGS); ex(0); SB();
          if constexpr (c < 8) qk2(s[c + 1], Kr[nx][1], Q1);
          SB(); if constexpr (c < 7) w_issue2<4 * c - 3, 4 * c - 2, 4096u, 28>(W2_ARGS); ex(1); cv(0); SB();
          if constexpr (c > 1) o = mfma(Vr[c - 1][1], v4u{pp[c - 1][4], pp[c - 1][5], pp[c - 1][6], pp[c - 1][7]}, o);
          SB(); if constexpr (c < 7) w_issue2<4 * c - 2, 4 * c - 1, 4096u, 28>(W2_ARGS); ex(2); cv(1); SB();
          if constexpr (c < 8) o = mfma(Vr[c][0], v4u{pp[c][0], pp[c][1], pp[c][2], pp[c][3]}, o);
          else {
            o = mfma(Vr[0][0], v4u{pp[8][0] & pm[0], pp[8][1] & pm[1], pp[8][2] & pm[2], pp[8][3] & pm[3]}, o);
            o = mfma(V0, v4u{pp[8][0] & ~pm[0], pp[8][1] & ~pm[1], pp[8][2] & ~pm[2], pp[8][3] & ~pm[3]}, o);
          }
          SB(); if constexpr (c < 7) w_issue2<4 * c - 1, 4 * c, 4096u, 28>(W2_ARGS); ex(3); cv(2); cv(3); SB();
        });
        o = mfma(Vr[0][1], v4u{pp[8][4] & pm[4], pp[8][5] & pm[5], pp[8][6] & pm[6], pp[8][7] & pm[7]}, o);
        o = mfma(V1, v4u{pp[8][4] & ~pm[4], pp[8][5] & ~pm[5], pp[8][6] & ~pm[6], pp[8][7] & ~pm[7]}, o);
      }
      const float inv = __builtin_amdgcn_rcpf(half_add(h ? o[12] : 0.f));
      u32 oo[8];
#pragma unroll
      for (int i = 0; i < 8; ++i) oo[i] = pk(o[2 * i] * inv, o[2 * i + 1] * inv);
      if (h) { oo[6] = 0u; oo[7] = 0u; }
      const u32 ob = WS_OF + l * OF_L + (u32)(tb * 16 + head) * 2048u;
      st_sc1(v4u{oo[0], oo[1], oo[2], oo[3]}, rws, lane * 16, ob);
      st_sc1(v4u{oo[4], oo[5], oo[6], oo[7]}, rws, lane * 16 + 1024, ob);
      publish(p.ws, flag_off(l, 1, T * 16 + head), tag);
      touch_done(tc4);
      STAMP(4);
    }
    }
    v4u bw4[20][2], bw5[16][2];
#define T5_PTRS \
    lptr t0 = L + ringw + (4 * h + tq) * 128 + (((4 * wb + cpart) ^ sw) * 16) + (tp & 1) * 8; \
    lptr t1 = L + ringw + (4 * h + tq) * 128 + (((4 * (wb ^ 1) + cpart) ^ sw) * 16) + (tp & 1) * 8;
#define T4_PTRS \
    lptr n0 = L + ringw + (8 * h + tq) * 128 + (((4 * wa + cpart) ^ sw) * 16) + (tp & 1) * 8; \
    lptr n1 = L + ringw + (8 * h + tq) * 128 + (((4 * (wa ^ 1) + cpart) ^ sw) * 16) + (tp & 1) * 8;
    {
      IDS
      T5_PTRS
      w_take<0, 12, 1024>(t0, t1, bw5);
      lgk0();
      w_issue2<0, 24, 3584u, 32>(W1_ARGS);
    }
    if (p35) {
      IDS
      const Poll pq = wait_flags(p.ws, flag_off(l, 1, (st >> 1) * 16 + 4 * w + (lane & 3)), tag);
      STAMP(5);
      const u32 o0 = WS_OF + l * OF_L + (u32)((2 * st + wb) * 16 + 4 * w) * 2048u;
      const u32 o1 = WS_OF + l * OF_L + (u32)((2 * st + (wb ^ 1)) * 16 + 4 * w) * 2048u;
      v4u of[8][2];
      sfor<0, LA3>([&](auto I) { constexpr int k = I; of[k][0] = lda<k>(rws, lane * 16, o0); of[k][1] = lda<k>(rws, lane * 16, o1); });
      f32x16 acc[4];
#pragma unroll
      for (int q = 0; q < 4; ++q)
#pragma unroll
        for (int i = 0; i < 16; ++i) acc[q][i] = 0.f;
      sfor<0, 8>([&](auto I) {
        constexpr int k = I;
        vmw<(k + LA3 <= 8) ? 2 * LA3 - 2 : 14 - 2 * k>(of[k][0], of[k][1]);
        if constexpr (k == 0) poll_done(pq);
        acc[1] = mfma(wf[k][0], of[k][1], acc[1]);
        SB();
        if constexpr (k + LA3 < 8) of[k + LA3][0] = lda<k + LA3>(rws, lane * 16, o0);
        SB();
        acc[2] = mfma(wf[k][1], of[k][0], acc[2]);
        acc[3] = mfma(wf[k][1], of[k][1], acc[3]);
        SB();
        if constexpr (k + LA3 < 8) of[k + LA3][1] = lda<k + LA3>(rws, lane * 16, o1);
        SB();
        acc[0] = mfma(wf[k][0], of[k][0], acc[0]);
        if constexpr (k < 7) SB();
      });
      const f32x16 y = reduce4(L, acc, w, lane);
      float xv[16], s1 = 0.f, s2 = 0.f;
#pragma unroll
      for (int i = 0; i < 8; ++i) {
        xres[i] = pk(blo(xres[i]) + y[2 * i], bhi(xres[i]) + y[2 * i + 1]);
        xv[2 * i] = blo(xres[i]); xv[2 * i + 1] = bhi(xres[i]);
        s1 += xv[2 * i] + xv[2 * i + 1];
        s2 += xv[2 * i] * xv[2 * i] + xv[2 * i + 1] * xv[2 * i + 1];
      }
      s1 = half_add(s1); s2 = half_add(s2);
      const u32 tg = (u32)tag | 1u;
      const u32 sb = WS_STATS + l * STATS_L + (u32)((2 * st + wb) * 14) * 512u;
      if (h == 0) st_sc1(v4u{asu(s1), tg, asu(s2), tg}, rws, r * 16, sb + (u32)(2 * dt + wa) * 512u);
      STAMP(6);
      {
        T4_PTRS
        w_take<0, NB, 512>(n0, n1, bw4);
        lgk0();
        w_issue2<24, 24 + 2 * NB, 3584u, 32>(W1_ARGS);
      }
      float t1s, t2s;
      for (u32 spins = 0;;) {
        v4u gr[7];
#pragma unroll
        for (int i = 0; i < 7; ++i) gr[i] = ld_sc1(rws, (u32)(7 * h) * 512u + r * 16 + i * 512, sb);
        bool ok = true; t1s = 0.f; t2s = 0.f;
#pragma unroll
        for (int i = 0; i < 7; ++i) { ok &= (gr[i].y == tg) & (gr[i].w == tg); t1s += asf(gr[i].x); t2s += asf(gr[i].z); }
        if (__all(ok)) break;
        asm volatile("" ::: "memory");
        if (spin_out(p.ws, spins, (u32)tag)) break;
      }
      STAMP(7);
      t1s = half_add(t1s); t2s = half_add(t2s);
      const float m1 = t1s * (1.f / 448.f);
      const float var = fmaxf(t2s * (1.f / 448.f) - m1 * m1, 0.f);
      const float r1 = __builtin_amdgcn_rsqf(var + EPS);
      const float r2 = __builtin_amdgcn_rsqf(var * r1 * r1 + EPS);
      const float rr = r1 * r2, mm = m1 * rr;
      lptr tw = L + L_TR + w * 2304;
#pragma unroll
      for (int i = 0; i < 4; ++i)
        *(LAS v2u*)(tw + r * 72 + i * 16 + h * 8) = v2u{pk(xv[4 * i] * rr - mm, xv[4 * i + 1] * rr - mm), pk(xv[4 * i + 2] * rr - mm, xv[4 * i + 3] * rr - mm)};
      lgk0();
      lptr tr = tw + (8 * h + tq) * 72 + ch16 * 32 + tp * 8;
      const u32 hb = WS_HF + l * HF_L + (u32)((2 * dt + wa) * 128 + (2 * st + wb) * 2) * 1024u;
      st_sc1(trd2(tr, tr + 4 * 72), rws, lane * 16, hb);
      st_sc1(trd2(tr + 16 * 72, tr + 20 * 72), rws, lane * 16 + 1024, hb);
      publish(p.ws, flag_off(l, 2, dt * 32 + st), tag);
      STAMP(8);
    } else {
      IDS
      T4_PTRS
      drain();
      w_take<0, NB, 512>(n0, n1, bw4);
      lgk0();
      w_issue2<24, 24 + 2 * NB, 3584u, 32>(W1_ARGS);
      drain();
    }
    if (p4) {
      IDS
      T4_PTRS
      v2u bias[4];
      {
        const rsrc_t rb1 = mkrs(p.b1, F_ * 2);
        const u32 bo = (u32)(ft * 64 + wa * 32 + 4 * h) * 2u;
        sfor<0, 4>([&](auto I) { constexpr int i = I; bias[i] = ldp2<16 * i>(rb1, bo); });
      }
      w_take<NB, 20, 512>(n0, n1, bw4);
      lgk0();
      w_issue2<24 + 2 * NB, 64, 3584u, 32>(W1_ARGS);
      STAMP(9);
      const Poll pq = wait_flags(p.ws, flag_off(l, 2, dt4 * 32 + 8 * w + (lane & 7)), tag);
      asm volatile("" : "+v"(bias[0]), "+v"(bias[1]), "+v"(bias[2]), "+v"(bias[3]));
      STAMP(10);
      f32x16 acc[4];
      const u32 hb = WS_HF + l * HF_L + (u32)(32 * w) * 1024u;
      gemm_main<32, 20, 512, true, LA4>(rws, lane * 16, hb + (u32)(2 * dt4 + wb) * 131072u, hb + (u32)(2 * dt4 + (wb ^ 1)) * 131072u, ROT4, n0, n1, bw4, acc,
                                        [&](auto I) { constexpr int i = I; w_issue2<24 + i, 25 + i, 4096u, 28>(W2_ARGS); });
      poll_done(pq);
      STAMP(11);
      const f32x16 y = reduce4(L, acc, w, lane);
      u32 o[8];
#pragma unroll
      for (int i = 0; i < 8; ++i) {
        const u32 bb = (i & 1) ? bias[i >> 1].y : bias[i >> 1].x;
        float gv[2];
#pragma unroll
        for (int e = 0; e < 2; ++e) {
          const float u = y[2 * i + e] + (e ? bhi(bb) : blo(bb));
          const float z = u * (-2.302208198f - 0.1029432397f * u * u);
          gv[e] = u * __builtin_amdgcn_rcpf(1.f + __builtin_amdgcn_exp2f(z));
        }
        o[i] = pk(gv[0], gv[1]);
      }
      const u32 ub = WS_UF + l * UF_L + (u32)((2 * dt4 + wb) * 112 + 4 * ft + 2 * wa) * 1024u;
      st_sc1(v4u{o[0], o[1], o[2], o[3]}, rws, lane * 16, ub);
      st_sc1(v4u{o[4], o[5], o[6], o[7]}, rws, lane * 16 + 1024, ub);
      publish(p.ws, flag_off(l, 3, dt4 * 32 + ft), tag);
      STAMP(12);
    } else {
      IDS
      {
        const rsrc_t rwf = mkrs(p.w2, F_ * S_ * 2);
        const int nb = (g & 1) ? 4 : 11, ib = bi - ((g & 1) ? 28 : 21);
        for (int row = ib * 4 + w; row < F_; row += nb * 4 * 8) {
          sfor<0, 8>([&](auto I) { constexpr int i = I; dma16<0, i * 1024>(rwf, lane * 16 + pj * 1024, (u32)(row + i * nb * 4) * 4096u, lds0 + ringw); });
          asm volatile("s_waitcnt vmcnt(16)" ::: "memory");
        }
        drain();
      }
      w_issue2<24, 24 + 2 * LA4, 4096u, 28>(W2_ARGS);
      drain();
    }
    if (p35) {
      IDS
      T5_PTRS
      u32 bias;
      asm volatile("s_nop 4\n\tbuffer_load_ushort %0, %1, %2, 0 offen" : "=v"(bias) : "v"((u32)((2 * st + wb) * 32 + r) * 2u), "s"(mkrs(p.b2, S_ * 2)));
      w_issue2<24 + 2 * LA4, 48, 4096u, 28>(W2_ARGS);
      w_take<12, 16, 1024>(t0, t1, bw5);
      lgk0();
      w_issue2<48, 56, 4096u, 28>(W2_ARGS);
      STAMP(13);
      const Poll pq = wait_flags(p.ws, flag_off(l, 3, dt * 32 + 7 * w + (lane < 7 ? lane : 0)), tag);
      asm volatile("" : "+v"(bias));
      STAMP(14);
      const u32 wis = l == 0 ? WS_WI + (u32)head * 86016u + (u32)w * 21504u : 0x40000000u;
      v4u wi[21];
      f32x16 acc[4];
      const u32 ub = WS_UF + l * UF_L + (u32)(28 * w) * 1024u;
      gemm_main<28, 16, 1024, false, LA5>(rws, lane * 16, ub + (u32)(2 * dt + wa) * 114688u, ub + (u32)(2 * dt + (wa ^ 1)) * 114688u, ROT5, t0, t1, bw5, acc,
                                          [&](auto I) { constexpr int i = I; wi[i] = lda<i>(rws, lane * 16, wis); });
      poll_done(pq);
      STAMP(15);
      const f32x16 y = reduce4(L, acc, w, lane);
      const float bv = blo(bias);
#pragma unroll
      for (int i = 0; i < 8; ++i) xres[i] = pk(blo(xres[i]) + (y[2 * i] + bv), bhi(xres[i]) + (y[2 * i + 1] + bv));
      v4u xo[2];
#pragma unroll
      for (int s = 0; s < 2; ++s) {
        auto a = __builtin_amdgcn_permlane32_swap(xres[4 * s], xres[4 * s + 2], false, false);
        auto c = __builtin_amdgcn_permlane32_swap(xres[4 * s + 1], xres[4 * s + 3], false, false);
        xo[s] = v4u{a[0], c[0], a[1], c[1]};
      }
      if (l == 0) {
        const u32 xb = WS_XF + (u32)((2 * st + wb) * 28 + 4 * dt) * 1024u + wa * 512;
        st_sc1(xo[0], rws, h * 1024 + r * 16, xb);
        st_sc1(xo[1], rws, h * 1024 + r * 16 + 2048, xb);
        float q0 = 0.f, q1 = 0.f;
#pragma unroll
        for (int i = 0; i < 8; i += 2) { q0 = __builtin_amdgcn_fdot2_f32_bf16(asbf2(xres[i]), asbf2(xres[i]), q0, false); q1 = __builtin_amdgcn_fdot2_f32_bf16(asbf2(xres[i + 1]), asbf2(xres[i + 1]), q1, false); }
        const float q = half_add(q0 + q1);
        if (h == 0) __builtin_amdgcn_raw_buffer_store_b32(asu(q), rws, r * 4, WS_SQ + (u32)((2 * st + wb) * 14 + 2 * dt + wa) * 128u, 16);
        publish(p.ws, flag_off(0, 4, st * 8 + dt), tag);
        sfor<2 * LA5, 21>([&](auto I) { constexpr int i = I; wi[i] = lda<i>(rws, lane * 16, wis); });
        sfor<0, 21>([&](auto I) { constexpr int i = I; vmw<(i < 2 * LA5) ? 21 - 2 * LA5 : 20 - i>(wi[i]); *(LAS v4u*)(L + w * 21504 + i * 1024 + lane * 16) = wi[i]; });
        bar();
      } else {
        sfor<0, 2 * LA5>([&](auto I) { constexpr int i = I; vmw<0>(wi[i]); });
        const rsrc_t rout = mkrs(p.out, S_ * D_ * 2);
        const u32 oo = (u32)((2 * st + wb) * 32 + r) * 896u + (u32)(dt * 64 + wa * 32 + 8 * h) * 2u;
        __builtin_amdgcn_raw_buffer_store_b128(xo[0], rout, oo, 0, 0);
        __builtin_amdgcn_raw_buffer_store_b128(xo[1], rout, oo + 32, 0, 0);
        if (b == 0 && tid == 0) __hip_atomic_store((GAS u64*)(p.ws + WS_EPOCH), tag, RLX_AGENT);
      }
      STAMP(16);
    } else if (l == 0) {
      IDS
      const Poll pq = wait_flags(p.ws, flag_off(0, 1, (lane & 15) * 16 + head), tag);
      const u32 wis = WS_WI + (u32)head * 86016u + (u32)w * 21504u;
      v4u wi[21];
      bar();
      sfor<0, 21>([&](auto I) { constexpr int i = I; wi[i] = lda<i>(rws, lane * 16, wis); });
      sfor<0, 21>([&](auto I) { constexpr int i = I; vmw<20 - i>(wi[i]); *(LAS v4u*)(L + w * 21504 + i * 1024 + lane * 16) = wi[i]; });
      poll_done(pq);
      bar();
    }
  }
  if (blockIdx.x == DIAG_BLK) {
    const u32 ta = __builtin_amdgcn_readlane(DIAG_SRC, DIAG_A), tb_ = __builtin_amdgcn_readlane(DIAG_SRC, DIAG_B);
    const u32 t0 = (u32)__builtin_amdgcn_s_memrealtime();
    const u32 d = (tb_ - ta) * DIAG_MUL / DIAG_DIV;
    if (d < 200000u) while ((u32)__builtin_amdgcn_s_memrealtime() - t0 < d) __builtin_amdgcn_s_sleep(1);
  }
  asm volatile(".Lfwd_last:");
}

extern "C" void kernel_launch(void* const* d_in, const int* in_sizes, int n_in, void* d_out, int out_size, void* d_ws, size_t ws_size,
                              hipStream_t stream) {
  Params p;
  p.x = d_in[0]; p.wqkv = d_in[1]; p.wo = d_in[2]; p.w1 = d_in[4]; p.b1 = d_in[5]; p.w2 = d_in[6]; p.b2 = d_in[7];
  p.out = d_out; p.ws = (unsigned char*)d_ws;
  (void)hipFuncSetAttribute((const void*)fused_fwd, hipFuncAttributeMaxDynamicSharedMemorySize, L_END);
  hipLaunchKernelGGL(fused_fwd, dim3(256), dim3(256), L_END, stream, p);
}
```

```cpp
#include <hip/hip_runtime.h>
#include <hip/hip_bf16.h>
#include <type_traits>

typedef unsigned u32;
typedef unsigned long long u64;
typedef u32 v4u __attribute__((ext_vector_type(4)));
typedef u32 v2u __attribute__((ext_vector_type(2)));
typedef float f32x16 __attribute__((ext_vector_type(16)));
typedef float f32x4 __attribute__((ext_vector_type(4)));
typedef float f32x2 __attribute__((ext_vector_type(2)));
typedef __bf16 bf16x8 __attribute__((ext_vector_type(8)));
typedef __bf16 bf16x2 __attribute__((ext_vector_type(2)));
typedef short s16x4 __attribute__((ext_vector_type(4)));
#define LAS __attribute__((address_space(3)))
#define GAS __attribute__((address_space(1)))
#define DI __device__ __forceinline__
#define RLX_AGENT __ATOMIC_RELAXED, __HIP_MEMORY_SCOPE_AGENT
#define SB() __builtin_amdgcn_sched_barrier(0)
typedef __amdgpu_buffer_rsrc_t rsrc_t;
typedef LAS unsigned char* lptr;

constexpr int AX = 8, PA0 = 8, PA1 = 12, PB0 = 12, PBN = 20, PC0 = 6, PC1 = 3, PD0 = 8, PD1 = 4, PX = 4;
constexpr int NB = 12, QS = 12, QP0 = 21, XG = 6, LA1 = 8;

constexpr int S_ = 2048, D_ = 448, F_ = 1792;
constexpr u32 WS_EPOCH = 0, WS_TMO = 128;
constexpr u32 WS_VOTE = 4096;
constexpr u32 WS_PROBE = 512u << 10;
constexpr u32 WS_FLAGS = 8192;
constexpr u32 WS_STATS = 1u << 20, STATS_L = 64 * 4096;
constexpr u32 WS_KV = 2u << 20, KV_L = 4u << 20;
constexpr u32 WS_OF = WS_KV + 2 * KV_L, OF_L = 2u << 20;
constexpr u32 WS_HF = WS_OF + 2 * OF_L, HF_L = 2u << 20;
constexpr u32 WS_UF = WS_HF + 2 * HF_L, UF_L = 2u << 20;
constexpr u32 WS_XF = WS_UF + 2 * UF_L;
constexpr u32 WS_WI = WS_XF + (2u << 20);
constexpr u32 WS_END = 2 * (WS_WI + (2u << 20));
constexpr u32 NOWHERE = 0x40000000u;
constexpr u32 L_RING = 0, RING_W = 24576;
constexpr u32 L_INBOX = 98304, INBOX_W = 12288;
constexpr u32 L_WQ = 57344;
constexpr u32 L_WO = 86016;
constexpr u32 L_CSP = 147456;
constexpr u32 L_CSF = L_CSP + 1536;
constexpr u32 L_MR = L_CSF + 2048;
constexpr u32 L_TR = L_MR + 1024;
constexpr u32 L_NB = L_TR + 4 * 2304;
constexpr u32 L_END = L_NB + 16;
constexpr u32 kSpinCap = 1u << 18;
constexpr float EPS = 1e-5f;

struct Params {
  const void* x; const void* wqkv; const void* wo; const void* w1; const void* b1; const void* w2; const void* b2;
  void* out; unsigned char* ws;
};

DI f32x16 mfma(v4u a, v4u b, f32x16 c) {
  return __builtin_amdgcn_mfma_f32_32x32x16_bf16(__builtin_bit_cast(bf16x8, a), __builtin_bit_cast(bf16x8, b), c, 0, 0, 0);
}
DI f32x16 qk1(v4u k0, v4u q0, f32x16 ci) {
  f32x16 c;
  asm volatile("v_mfma_f32_32x32x16_bf16 %0, %1, %2, %3" : "=&v"(c) : "a"(k0), "v"(q0), "v"(ci));
  return c;
}
DI void qk2(f32x16& c, v4u k1, v4u q1) { asm volatile("v_mfma_f32_32x32x16_bf16 %0, %1, %2, %0" : "+v"(c) : "a"(k1), "v"(q1)); }
DI f32x16 qk_own(v4u k0, v4u k1, v4u q0, v4u q1) {
  f32x16 c;
  asm volatile("s_nop 1\n\tv_mfma_f32_32x32x16_bf16 %0, %1, %3, 0\n\tv_mfma_f32_32x32x16_bf16 %0, %2, %4, %0" : "=&v"(c) : "v"(k0), "v"(k1), "v"(q0), "v"(q1));
  return c;
}
DI void tie(f32x16& c) { asm volatile("" : "+v"(c)); }
DI void tie_far(f32x16& c) { asm volatile("s_nop 7\n\ts_nop 4" : "+v"(c)); }
DI u32 pk(float lo, float hi) { f32x2 v = {lo, hi}; return __builtin_bit_cast(u32, __builtin_convertvector(v, bf16x2)); }
DI float asf(u32 v) { return __builtin_bit_cast(float, v); }
DI u32 asu(float v) { return __builtin_bit_cast(u32, v); }
DI bf16x2 asbf2(u32 v) { return __builtin_bit_cast(bf16x2, v); }
DI float blo(u32 v) { return __builtin_bit_cast(float, v << 16); }
DI float bhi(u32 v) { return __builtin_bit_cast(float, v & 0xffff0000u); }
DI void bar() { asm volatile("s_waitcnt lgkmcnt(0)\n\ts_barrier" ::: "memory"); }
DI void drain() { asm volatile("s_waitcnt vmcnt(0)" ::: "memory"); }
DI void lgk0() { asm volatile("s_waitcnt lgkmcnt(0)" ::: "memory"); }
template <u32 SI, u32 LI> DI void dma16(rsrc_t rs, u32 voff, u32 soff, u32 lds) {
  u32 t;
  asm volatile("s_add_u32 m0, %1, %5\n\ts_add_u32 %0, %4, %6\n\tbuffer_load_dwordx4 %2, %3, %0 offen lds"
               : "=&s"(t) : "s"(lds), "v"(voff), "s"(rs), "s"(soff), "i"(LI), "i"(SI) : "memory", "scc");
}
template <int K> DI v4u lda(rsrc_t rs, u32 voff, u32 soff) {
  v4u d; u32 t;
  asm volatile("s_xor_b32 %1, %4, %5\n\tbuffer_load_dwordx4 %0, %2, %3, %1 offen offset:%6 sc1"
               : "=v"(d), "=&s"(t) : "v"(voff), "s"(rs), "s"(soff), "i"((K >> 3) * 16384 + ((K >> 2) & 1) * 4096), "i"((K & 3) * 1024) : "scc");
  return d;
}
template <int IMM> DI v4u ldp(rsrc_t rs, u32 voff, u32 soff) {
  v4u d;
  asm volatile("buffer_load_dwordx4 %0, %1, %2, %3 offen offset:%4" : "=v"(d) : "v"(voff), "s"(rs), "s"(soff), "i"(IMM));
  return d;
}
template <u32 SI> DI v4u ldr(rsrc_t rs, u32 voff, u32 soff) {
  v4u d; u32 t;
  asm volatile("s_add_u32 %1, %4, %5\n\tbuffer_load_dwordx4 %0, %2, %3, %1 offen" : "=v"(d), "=&s"(t) : "v"(voff), "s"(rs), "s"(soff), "i"(SI) : "scc");
  return d;
}
template <int IMM> DI v4u ldg(rsrc_t rs, u32 voff, u32 soff) {
  v4u d;
  asm volatile("buffer_load_dwordx4 %0, %1, %2, %3 offen offset:%4 sc1" : "=v"(d) : "v"(voff), "s"(rs), "s"(soff), "i"(IMM));
  return d;
}
template <int IMM> DI v2u ldp2(rsrc_t rs, u32 voff) {
  v2u d;
  asm volatile("buffer_load_dwordx2 %0, %1, %2, 0 offen offset:%3" : "=v"(d) : "v"(voff), "s"(rs), "i"(IMM));
  return d;
}
DI u32 ldu16(rsrc_t rs, u32 voff) {
  u32 d;
  asm volatile("buffer_load_ushort %0, %1, %2, 0 offen" : "=v"(d) : "v"(voff), "s"(rs));
  return d;
}
DI float mul1(float a, float b) { float d; asm volatile("v_mul_f32 %0, %1, %2" : "=v"(d) : "v"(a), "v"(b)); return d; }
DI void pin4(float (&v)[4]) { asm volatile("" : "+v"(v[0]), "+v"(v[1]), "+v"(v[2]), "+v"(v[3])); }
template <int N> DI void vmw0() { asm volatile("s_waitcnt vmcnt(%0)" ::"i"(N) : "memory"); }
template <int N> DI void vmw(v4u& a) { asm volatile("s_waitcnt vmcnt(%1)" : "+v"(a) : "i"(N)); }
template <int N> DI void vmw(v4u& a, v4u& b, v4u& c, v4u& d) { asm volatile("s_waitcnt vmcnt(%4)" : "+v"(a), "+v"(b), "+v"(c), "+v"(d) : "i"(N)); }
template <int I, int N, class Fn> DI void sfor(Fn&& f) { if constexpr (I < N) { f(std::integral_constant<int, I>{}); sfor<I + 1, N>(f); } }
DI u32 opq(u32 v) { v = __builtin_amdgcn_readfirstlane(v); asm volatile("" : "+s"(v)); return v; }
DI u32 opqv(u32 v) { asm volatile("" : "+v"(v)); return v; }
DI v2u trd(lptr p) { return __builtin_bit_cast(v2u, __builtin_amdgcn_ds_read_tr16_b64_v4i16((LAS s16x4*)p)); }
DI v4u trd2(lptr p0, lptr p1) { v2u a = trd(p0), b = trd(p1); return v4u{a.x, a.y, b.x, b.y}; }
DI v4u ld_sc1(rsrc_t rs, u32 voff, u32 soff) { return __builtin_amdgcn_raw_buffer_load_b128(rs, voff, soff, 16); }
DI u32 ph(u32 o) { return o + (o & ~8191u); }
DI u32 swz(u32 i) { return ((i & 1u) << 12) | ((i & 2u) << 13); }
DI u32 rd(u32 o, u32 nb) { const u32 q = ph(o); return q | ((((q >> 21) ^ nb) & 1u) << 13); }
DI void st_sc1(v4u v, rsrc_t rs, u32 voff, u32 soff) { __builtin_amdgcn_raw_buffer_store_b128(v, rs, voff, soff, 16); }
DI void st2(v4u v, rsrc_t rs, u32 voff, u32 q) { st_sc1(v, rs, voff, q); st_sc1(v, rs, voff, q ^ 8192u); }
DI rsrc_t mkrs(const void* p, u32 bytes) {
  u32 fl = 0x00020000;
  asm volatile("" : "+s"(p), "+s"(bytes), "+s"(fl));
  return __builtin_amdgcn_make_buffer_rsrc((void*)p, 0, bytes, fl);
}
DI float half_add(float v) {
  auto r = __builtin_amdgcn_permlane32_swap(asu(v), asu(v), false, false);
  const u32 a = r[0], b = r[1];
  return asf(a) + asf(b);
}
DI float half_max(float v) {
  auto r = __builtin_amdgcn_permlane32_swap(asu(v), asu(v), false, false);
  const u32 a = r[0], b = r[1];
  return fmaxf(asf(a), asf(b));
}
DI bool spin_out(unsigned char* ws, u32& spins, u32 tl) {
  ++spins;
  if ((spins & 63u) == 0u) {
    GAS u32* tmo = (GAS u32*)(ws + WS_TMO);
    if (__hip_atomic_load(tmo, RLX_AGENT) == tl) return true;
    if (spins > kSpinCap) { __hip_atomic_store(tmo, tl, RLX_AGENT); return true; }
  }
  return false;
}
struct Poll { v2u a, b, c; };
#define PL(R, O, B) "global_load_dwordx2 " R ", " O ", " B " sc1\n\t"
DI v2u poll_one(unsigned char* ws, u32 off) {
  v2u r;
  asm volatile(PL("%0", "%1", "%2") : "=v"(r) : "v"(off), "s"(ws) : "memory");
  return r;
}
template <int CA, int CB, int CC> DI u64 poll_finish_mixed(Poll& q, unsigned char* ws, u32 off, u64 tag) {
  u32 left; u64 side;
#define PE(R, N) "s_waitcnt vmcnt(" N ")\n\tv_cmp_eq_u64 vcc, %7, " R "\n\tv_cmp_eq_u64 %4, %8, " R "\n\ts_or_b64 vcc, vcc, %4\n\ts_cmp_eq_u64 vcc, -1\n\ts_cbranch_scc1 2f\n\t" PL(R, "%5", "%6")
  asm volatile("s_mov_b32 %3, 0x20000\n\t" PE("%0", "%9") PE("%1", "%10") PE("%2", "%11")
               "1:\n\t" PE("%0", "2") PE("%1", "2") PE("%2", "2") "s_sub_u32 %3, %3, 1\n\ts_cbranch_scc0 1b\n2:"
               : "+v"(q.a), "+v"(q.b), "+v"(q.c), "=&s"(left), "=&s"(side) : "v"(off), "s"(ws), "s"(tag), "s"(tag + 1), "i"(CA), "i"(CB + 1), "i"(CC + 2) : "vcc", "scc", "memory");
#undef PE
  if (__builtin_amdgcn_readfirstlane(left) == -1) { drain(); __hip_atomic_store((GAS u32*)(ws + WS_TMO), (u32)tag, RLX_AGENT); }
  return ((u64)(u32)__builtin_amdgcn_readfirstlane((u32)(side >> 32)) << 32) | (u32)__builtin_amdgcn_readfirstlane((u32)side);
}
DI u64 poll_finish(Poll& q, unsigned char* ws, u32 off, u64 tag) { return poll_finish_mixed<2, 1, 0>(q, ws, off, tag); }
DI Poll wait_flags(unsigned char* ws, u32 off, u64 tag) {
  Poll q;
  q.a = poll_one(ws, off); asm volatile("s_sleep 3");
  q.b = poll_one(ws, off); asm volatile("s_sleep 3");
  q.c = poll_one(ws, off);
  (void)poll_finish(q, ws, off, tag);
  return q;
}
DI u32 sbit(u64 side, int lane) { return (u32)((side >> lane) & 1ull) << 13; }
DI void poll_done(const Poll& q) { asm volatile("" ::"v"(q.a), "v"(q.b), "v"(q.c)); }
DI u32 touch(rsrc_t rs, u32 voff, u32 soff) {
  u32 d;
  asm volatile("s_nop 4\n\tbuffer_load_dword %0, %1, %2, %3 offen" : "=v"(d) : "v"(voff), "s"(rs), "s"(soff));
  return d;
}
DI u32 touch_ws(rsrc_t rs, u32 voff, u32 soff) {
  u32 d;
  asm volatile("s_nop 4\n\tbuffer_load_dword %0, %1, %2, %3 offen sc1" : "=v"(d) : "v"(voff), "s"(rs), "s"(soff));
  return d;
}
DI void touch_lds(rsrc_t rs, u32 voff, u32 soff, u32 lds) {
  asm volatile("s_mov_b32 m0, %0\n\ts_nop 0\n\tbuffer_load_dword %1, %2, %3 offen lds" ::"s"(lds), "v"(voff), "s"(rs), "s"(soff) : "memory");
}
DI void touch_done(u32 d) { asm volatile("" ::"v"(d)); }
DI void publish(unsigned char* ws, u32 off, u64 val, int w) {
  const u32 fo = ph(off) + (threadIdx.x & 1) * 8192u;
  asm volatile("s_cmp_lg_u32 %3, 0\n\ts_waitcnt vmcnt(0) lgkmcnt(0)\n\ts_barrier\n\ts_cbranch_scc1 3f\n\ts_mov_b64 exec, 3\n\t"
               "global_store_dwordx2 %0, %1, %2 sc1\n\ts_mov_b64 exec, -1\n3:" ::"v"(fo), "v"(val), "s"(ws), "s"(w) : "scc", "memory");
}
DI u32 flag_off(int l, int type, int idx) { return WS_FLAGS + (u32)(((l * 5 + type) << 8) + idx) * 128u; }


DI void red_put(lptr L, const f32x16& a, int q, int i, int w, int lane) {
  *(LAS f32x4*)(L + L_INBOX + (u32)(w ^ q) * INBOX_W + (q - 1) * 4096 + lane * 16 + i * 1024) = f32x4{a[4 * i], a[4 * i + 1], a[4 * i + 2], a[4 * i + 3]};
}
DI f32x16 red_get(lptr L, const f32x16& own, int w, int lane) {
  bar();
  f32x16 r = own;
  lptr src = L + L_INBOX + (u32)w * INBOX_W + lane * 16;
#pragma unroll
  for (int q = 0; q < 3; ++q)
#pragma unroll
    for (int i = 0; i < 4; ++i) {
      const f32x4 v = *(LAS f32x4*)(src + q * 4096 + i * 1024);
      r[4 * i] += v[0]; r[4 * i + 1] += v[1]; r[4 * i + 2] += v[2]; r[4 * i + 3] += v[3];
    }
  return r;
}
template <int P0, int P1, u32 RS> DI void w_issue(rsrc_t rs, u32 voff, u32 soff, u32 ring) {
  sfor<P0, P1>([&](auto I) { constexpr int i = I; dma16<i * 8 * RS, (i % 24) * 1024>(rs, voff, soff, ring); });
}
template <int K0, int K1, int TSTEP, int NR> DI void w_take(lptr t0, lptr t1, v4u (&bw)[NR][2]) {
#pragma unroll
  for (int kk = K0; kk < K1; ++kk) {
    const int sl = (kk % 12) * 2048;
    bw[kk][0] = trd2(t0 + sl, t0 + sl + TSTEP);
    bw[kk][1] = trd2(t1 + sl, t1 + sl + TSTEP);
  }
}
template <int NK, int NR, int TSTEP, bool WA, int LS, int Z, bool TAIL, class Tail, class Post>
DI f32x16 gemm_main(lptr L, int w, int lane, u32 voff, rsrc_t ws, u32 a0, u32 a1, u64 side, lptr t0, lptr t1, const v4u (&bw)[NR][2], f32x16 init, const Poll& pq, Tail&& tail, Post&& post) {
  constexpr bool RING = NR < NK;
  constexpr int NS = 2 * NK, LAG = 3;
  static_assert(!RING || (NK - NR == 12 && NS - LS >= 30 && NK - Z >= 14), "ring is 12 slots and must be consumed before the tail");
  v4u a[NS], wl[12][2];
  f32x16 acc[4];
#define KOF(n) (!RING ? (n) : (n) < 2 ? (n) : (n) < 14 ? NR + (n) - 2 : (n) - 12)
#define SN(j) ((j) < NS - 2 * Z ? (j) / 2 : (j) < NS - Z ? (j) - (NK - Z) : (j) - NK)
#define SH(j) ((j) < NS - 2 * Z ? (j) & 1 : (j) < NS - Z ? 1 : 0)
  auto ld = [&](auto J) { constexpr int j = J; a[j] = lda<KOF(SN(j))>(ws, voff, (SH(j) ? a1 : a0) | (RING ? 0u : sbit(side, 16 * (SN(j) >> 1)))); };
  sfor<0, LS>(ld);
  acc[0] = init;
#pragma unroll
  for (int q = 1; q < 4; ++q)
#pragma unroll
    for (int i = 0; i < 16; ++i) acc[q][i] = 0.f;
  auto mm = [&](auto J, auto WI) {
    constexpr int j = J, wi = WI, n = SN(j), q = WA ? 2 * wi + SH(j) : 2 * SH(j) + wi;
    v4u wv;
    if constexpr (RING && n >= 2 && n < 14) wv = wl[n - 2][wi]; else wv = bw[KOF(n)][wi];
    acc[q] = WA ? mfma(wv, a[j], acc[q]) : mfma(a[j], wv, acc[q]);
  };
  using I0 = std::integral_constant<int, 0>; using I1 = std::integral_constant<int, 1>;
  constexpr int QE = WA ? 1 : 2, QL = WA ? 2 : 1;
  SB();
  sfor<0, NS>([&](auto J) {
    constexpr int j = J, e = j - (NS - Z);
    asm volatile("" ::: "memory");
    vmw<(TAIL || j + LS <= NS) ? LS - 1 : NS - 1 - j>(a[j]);
    asm volatile("" ::: "memory");
    if constexpr (j == 0) { poll_done(pq); post(0); }
    mm(J, I1{});
    SB();
    if constexpr (j + LS < NS) ld(std::integral_constant<int, j + LS>{}); else if constexpr (TAIL) tail(std::integral_constant<int, j + LS - NS>{});
    if constexpr (e >= 1 && e <= 4 && Z >= 5) red_put(L, acc[3], 3, e - 1, w, lane);
    if constexpr (e >= 1 && e <= 2 && Z < 5) { red_put(L, acc[3], 3, 2 * e - 2, w, lane); red_put(L, acc[3], 3, 2 * e - 1, w, lane); }
    SB();
    if constexpr (j < NS - LAG) mm(J, I0{});
    SB();
    if constexpr (RING && j < 24) { constexpr int sl = ((NR + j / 2) % 12) * 2048; lptr t = (j & 1) ? t1 : t0; wl[j / 2][j & 1] = trd2(t + sl, t + sl + TSTEP); }
    if constexpr (e >= 1 && e <= 4 && Z >= 5) red_put(L, acc[QE], QE, e - 1, w, lane);
    if constexpr (e >= 1 && e <= 2 && Z < 5) { red_put(L, acc[QE], QE, 2 * e - 2, w, lane); red_put(L, acc[QE], QE, 2 * e - 1, w, lane); }
    SB();
  });
  sfor<NS - LAG, NS>([&](auto J) { mm(J, I0{}); });
  SB();
#pragma unroll
  for (int i = 0; i < 4; ++i) red_put(L, acc[QL], QL, i, w, lane);
  post(1);
#undef KOF
#undef SN
#undef SH
  return red_get(L, acc[0], w, lane);
}

__global__ __launch_bounds__(256, 1) void fused_fwd(Params p) {
  extern __shared__ __attribute__((aligned(16))) unsigned char smem[];
  lptr L = (lptr)smem;
  const u32 lds0 = (u32)(size_t)L;
  {
    u64 pc; u32 back, len;
    asm volatile("s_getpc_b64 %0\n.Lfwd_here:\n\ts_mov_b32 %1, .Lfwd_here-_Z9fused_fwd6Params\n\ts_mov_b32 %2, .Lfwd_last-_Z9fused_fwd6Params" : "=s"(pc), "=s"(back), "=s"(len));
    touch_lds(mkrs((const void*)(pc - back), len), threadIdx.x * 128, (blockIdx.x >> 3) * 32768u, lds0 + L_TR);
    {
      const u32 bb = blockIdx.x;
      touch_lds(bb < 112 ? mkrs(p.x, S_ * D_ * 2) : bb < 186 ? mkrs(p.wqkv, D_ * 3 * D_ * 2) : mkrs(p.wo, D_ * D_ * 2), (threadIdx.x & 32) ? NOWHERE : ((threadIdx.x >> 6) * 32 + (threadIdx.x & 31)) * 128,
                (bb < 112 ? bb : bb < 186 ? bb - 112 : bb - 186) * 16384u, lds0 + L_TR);
    }
    SB();
  }
#define IDS \
  const int tid = (int)opqv(threadIdx.x), lane = tid & 63, r = lane & 31, h = lane >> 5; \
  const int w = (int)opq(w_), b = (int)opq(blockIdx.x), g = b & 7, bi = b >> 3, pj = g >> 1; \
  const int T = 4 * pj + (bi >> 3), head = (g & 1) * 8 + (bi & 7); \
  const u32 pk_ = opq(tiles_); \
  const int st = pk_ & 31, dt = (pk_ >> 8) & 7, ft = (pk_ >> 16) & 31, dt4 = pk_ >> 24; \
  const int wa = w >> 1, wb = w & 1; \
  const int ch16 = (lane >> 4) & 1, tq = (lane & 15) >> 2, tp = lane & 3; \
  const u32 ringw = L_RING + (u32)w * RING_W; \
  const u32 dmarow = lane >> 3, dmach = ((lane & 7) ^ (((lane >> 4) & 1) << 2)) * 16; \
  const u32 sw = (u32)((tq >> 1) & 1) << 2, cpart = 2 * ch16 + (tp >> 1); \
  (void)r; (void)h; (void)T; (void)head; (void)st; (void)dt; (void)ft; (void)dt4; (void)wa; (void)wb; (void)ch16; (void)tq; (void)tp; (void)ringw; (void)dmarow; (void)dmach; (void)sw; (void)cpart;
  const u32 w_ = __builtin_amdgcn_readfirstlane(threadIdx.x >> 6);
  u32 tiles_;
  {
    const u32 g = blockIdx.x & 7, bi = blockIdx.x >> 3, d7 = (bi * 37) >> 8, m7 = bi - 7 * d7;
    const u32 st = 4 * g + (d7 & 3), dt = m7, ft = 7 * (g >> 1) + 4 * (g & 1) + (d7 & 3), dt4 = m7;
    tiles_ = opq(st | (dt << 8) | (ft << 16) | (dt4 << 24));
  }
  const bool p35 = (blockIdx.x >> 3) < 28;
  const bool p4 = (blockIdx.x >> 3) < ((blockIdx.x & 1) ? 21u : 28u);

  unsigned char* const wsa = (unsigned char*)(((size_t)p.ws + 0x1FFFFF) & ~(size_t)0x1FFFFF);
  const rsrc_t rws = mkrs(wsa, WS_END);
  const u32 half_ = (__builtin_amdgcn_s_getreg((3 << 11) | 20) >> 2) & 1u;
  u32 nb = (blockIdx.x >> 3) & 1;
  u64 tag = 0;
  u32 xres[8];
  v4u bw4[20][2];
#pragma unroll
  for (int i = 0; i < NB; ++i) { asm volatile("" : "=a"(bw4[i][0])); asm volatile("" : "=a"(bw4[i][1])); }
  v4u wf[8][2];
#pragma unroll
  for (int i = 0; i < 8; ++i) { xres[i] = 0; asm volatile("" : "=a"(wf[i][0])); asm volatile("" : "=a"(wf[i][1])); }

#pragma unroll 1
  for (int l = 0; l < 2; ++l) {
    const rsrc_t rw1 = mkrs(p.w1, p4 ? S_ * F_ * 2 : 0);
    const rsrc_t rw2 = mkrs(p.w2, p35 ? F_ * S_ * 2 : 0);
    v4u bw5[16][2];
#define T5_PTRS \
    lptr t0 = L + ringw + (4 * h + tq) * 128 + (((4 * wb + cpart) ^ sw) * 16) + (tp & 1) * 8; \
    lptr t1 = L + ringw + (4 * h + tq) * 128 + (((4 * (wb ^ 1) + cpart) ^ sw) * 16) + (tp & 1) * 8;
#define T4_PTRS \
    lptr n0 = L + ringw + (8 * h + tq) * 128 + (((4 * wa + cpart) ^ sw) * 16) + (tp & 1) * 8; \
    lptr n1 = L + ringw + (8 * h + tq) * 128 + (((4 * (wa ^ 1) + cpart) ^ sw) * 16) + (tp & 1) * 8;
#define W2_ARGS rw2, dmarow * 4096u + dmach + (u32)st * 128u, (u32)(448 * w) * 4096u, lds0 + ringw
    {
    IDS
    const int tb = 4 * T + w;
    const u32 shift = (head & 1) * 8;
    const u32 half = (((tq >> 1) & 1) ^ 1) * 64, col = shift + ch16 * 32 + tp * 8;
    lptr wtk = L + (32 * h + tq) * 128 + half + col, wtv = L + (32 * h + tq) * 128 + (half ^ 64) + col, wtq = L + L_WQ + (32 * h + tq) * 64 + col;
    v4u X[28];
    f32x16 kt, vv, rr, ck_, cv_;
#pragma unroll
    for (int i = 0; i < 16; ++i) { kt[i] = 0.f; vv[i] = 0.f; }
    float mean, rstd, nmr, csv;
    constexpr int LN0 = 13;
    static_assert(LN0 + 6 <= 20 && LN0 - 2 + LA1 < 28, "order of the stages");
    auto ln_part = [&](auto I, float sum, float ssq) {
      constexpr int k = I;
      if constexpr (k == LN0) {
        mean = half_add(sum) * (1.f / 448.f);
        rstd = __builtin_amdgcn_rsqf(fmaxf(half_add(ssq) * (1.f / 448.f) - mean * mean, 0.f) + EPS);
        nmr = -mean * rstd;
        *(LAS float*)(L + L_MR + w * 256 + lane * 4) = h ? nmr : rstd;
        csv = *(LAS float*)(L + L_CSF + w * 512 + 256 + r * 4);
      }
      if constexpr (k >= LN0 + 2 && k < LN0 + 6) {
        constexpr int i = k - LN0 - 2;
        const f32x4 cs = *(LAS f32x4*)(L + L_CSF + w * 512 + 128 + (8 * i + 4 * h) * 4);
        const f32x4 r4 = *(LAS f32x4*)(L + L_MR + w * 256 + (8 * i + 4 * h) * 4);
        const f32x4 m4 = *(LAS f32x4*)(L + L_MR + w * 256 + 128 + (8 * i + 4 * h) * 4);
#pragma unroll
        for (int e = 0; e < 4; ++e) { ck_[4 * i + e] = cs[e]; cv_[4 * i + e] = m4[e]; rr[4 * i + e] = r4[e]; }
      }
      if constexpr (k >= 20) {
        constexpr int i = 2 * (k - 20);
        ck_[i] = mul1(ck_[i], nmr); cv_[i] = mul1(cv_[i], csv); ck_[i + 1] = mul1(ck_[i + 1], nmr); cv_[i + 1] = mul1(cv_[i + 1], csv);
      }
    };
    u32 tc2 = 0, tc3 = 0, tc5 = 0;
    v2u xr2[4] = {v2u{xres[0], xres[1]}, v2u{xres[2], xres[3]}, v2u{xres[4], xres[5]}, v2u{xres[6], xres[7]}};
    v4u wk[28], wv[28], wq[28];
    constexpr int PD = 3;
#define WROW(k) (((k) >> 2) * 64 + ((k) & 3) * 8)
#define WFK(k) trd2(wtk + WROW(k) * 128, wtk + WROW(k) * 128 + 512)
#define WFV(k) trd2(wtv + WROW(k) * 128, wtv + WROW(k) * 128 + 512)
#define WFQ(k) trd2(wtq + WROW(k) * 64, wtq + WROW(k) * 64 + 256)
    lptr wfr = L + lane * 16;
    auto kv_head = [&](auto FR) {
#pragma unroll
      for (int k = 0; k < PD; ++k) {
        if constexpr (FR) { wk[k] = *(LAS v4u*)(wfr + k * 2048); wv[k] = *(LAS v4u*)(wfr + k * 2048 + 1024); } else { wk[k] = WFK(k); wv[k] = WFV(k); }
      }
      SB();
    };
    auto kv_step = [&](auto I, auto FR, auto&& mem) {
      constexpr int k = I;
      kt = mfma(wk[k], X[k], kt);
      SB();
      if constexpr (k + PD < 28 && FR) wk[k + PD] = *(LAS v4u*)(wfr + (k + PD) * 2048); else if constexpr (k + PD < 28) wk[k + PD] = WFK(k + PD); else wq[2 * (k + PD - 28)] = WFQ(2 * (k + PD - 28));
      mem();
      SB();
      vv = mfma(X[k], wv[k], vv);
      SB();
      if constexpr (k + PD < 28 && FR) wv[k + PD] = *(LAS v4u*)(wfr + (k + PD) * 2048 + 1024); else if constexpr (k + PD < 28) wv[k + PD] = WFV(k + PD); else wq[2 * (k + PD - 28) + 1] = WFQ(2 * (k + PD - 28) + 1);
      SB();
    };
    const u32 kvbase = WS_KV + l * KV_L + (u32)head * (64u * 4096u);
    const bool probe = l == 0 && ((bi & 7) == 0 || (bi & 7) == 7);
    const u32 vidx = (u32)((T * 4 + (g & 1) * 2 + (bi & 1)) * 4 + w);
    u32 kvq, ob;
    auto out_addr = [&]() { kvq = opqv(rd(kvbase + tb * 4096, nb) + lane * 16); ob = opqv(rd(WS_OF + l * OF_L + (u32)(tb * 16 + head) * 2048u, nb) + lane * 16); };
    const v4u ones = {0x3F803F80u, 0x3F803F80u, 0x3F803F80u, 0x3F803F80u};
    if (__builtin_expect(l == 0, 1)) {
      v2u e0;
      asm volatile("global_load_dwordx2 %0, %1, %2 sc1" : "=v"(e0) : "v"(WS_EPOCH), "s"(wsa));
      const rsrc_t rx = mkrs(p.x, S_ * D_ * 2), rwq = mkrs(p.wqkv, D_ * 3 * D_ * 2);
      const u32 hoff = (u32)(head * 56) - shift;
      const u32 xv = dmarow * 896u + (lane & 7) * 16u, xs = (u32)(tb * 32) * 896u;
      lptr xb = L + L_WO + w * 15360;
      lptr xw = xb + dmarow * 128, xr = xb + r * 128;
      const u32 xsw = (r >> 1) & 7, xc0 = ((lane & 7) ^ (lane >> 4)) * 16;
      const u32 kvv = dmarow * 2688u + 896u + ((((lane >> 2) ^ (lane >> 4) ^ 1) & 1) * 896u) + (lane & 3) * 16u + hoff;
      const u32 kvs = (u32)(112 * w) * 2688u, kvl0 = lds0 + (u32)(14 * w) * 1024u;
      const rsrc_t rwo = mkrs(p.wo, D_ * D_ * 2);
      const u32 wov = dmarow * 896u + dmach + (u32)dt * 128u, wos = (u32)(112 * w) * 896u, wol = lds0 + L_WO + (u32)w * 15360u;
      const u32 qv = (u32)(lane >> 2) * 2688u + (lane & 3) * 16u + hoff, ql = lds0 + L_WQ + (u32)(7 * w) * 1024u;
      auto x_ld = [&](auto I) { constexpr int k = I; X[k] = ldp<(k >> 2) * 128>(rx, xv, xs + (k & 3) * 7168u); };
      const u32 ph4 = (u32)(bi >> 3), ph4o = (u32)(st & 3);
      auto w_ld = [&](auto I) { constexpr int i = I;
        if constexpr (i < 14) { u32 n = i + ((ph4 * 7) >> 1); n = n >= 14 ? n - 14 : n; dma16<0, 0>(rwq, kvv, kvs + n * (8 * 2688), kvl0 + n * 1024); }
        else { u32 n = i - 14 + ((ph4 * 7) >> 2); n = n >= 7 ? n - 7 : n; dma16<0, 0>(rwq, qv, kvs + n * (16 * 2688), ql + n * 1024); } };
      auto o_ld = [&](int i) { u32 n = i + ph4o * 4; n = n >= 15 ? n - 15 : n; dma16<0, 0>(rwo, wov, wos + n * (8 * 896), wol + n * 1024); };
      float sq4[4] = {0.f, 0.f, 0.f, 0.f};
      auto x_sq = [&](auto I) { constexpr int G = I;
#pragma unroll
        for (int i = 0; i < 16; ++i) {
          const bf16x2 xx = asbf2(X[4 * G + (i >> 2)][i & 3]);
          sq4[i & 1] = __builtin_amdgcn_fdot2_f32_bf16(xx, xx, sq4[i & 1], false);
          sq4[2 + (i & 1)] = __builtin_amdgcn_fdot2_f32_bf16(xx, asbf2(0x3F803F80u), sq4[2 + (i & 1)], false);
        }
        pin4(sq4); };
      SB();
      sfor<0, 4 * XG>(x_ld);
      SB();
      u32 t2v, t3v, t5v, xo, clv;
      {
        const u32 wid = (u32)(bi * 4 + w), row = (wid & 31) * 6 + (lane >> 2);
        t2v = opqv(lane < 24 ? ((wid >> 5) * 448 + row) * 4096u + (u32)g * 512u + (lane & 3) * 128u : NOWHERE);
        t3v = opqv(lane < 24 ? ((wid >> 5) * 512 + row) * 3584u + (u32)(7 * pj + 4 * (g & 1) + (lane & 3)) * 128u : NOWHERE);
      }
      {
        const int ty = lane >> 2, idx = ty < 2 ? T * 16 + head : ty == 2 ? dt * 32 + st : ty == 3 ? dt4 * 32 + ft : st * 8 + dt;
        const u32 fo = ph(flag_off((lane >> 1) & 1, ty, idx)) + (lane & 1) * 8192u;
        const u32 qo = (ph(WS_XF + (u32)((2 * st + wb) * 32 + 28) * 1024u + (u32)dt * 512u) ^ swz(2 * st + wb)) + ((lane >> 1) & 3) * 128u + (lane & 1) * 8192u;
        t5v = opqv(lane < 18 ? fo : (lane >> 3) == 4 ? qo : NOWHERE);
      }
      xo = opqv((u32)((2 * st + wb) * 32 + r) * 896u + (u32)(dt * 64 + wa * 32 + 4 * h) * 2u);
      clv = opqv(head < 7 ? h * (2 * STATS_L) + r * 16 + ph(WS_STATS + (u32)(tb * 4096 + head * 512)) : NOWHERE);
      const u32 wib = WS_WI + (u32)head * 131072u;
      const u32 wi0 = opq(ph(wib + (u32)(tb + 11 * (tb / 21)) * 1024u)), wi1 = opq(tb < 20 ? ph(wib + (u32)(tb + 97) * 1024u) : NOWHERE);
      out_addr();
      sfor<0, 7>([&](auto I) {
        constexpr int G = I;
        constexpr int last = G - 1 + XG < 6 ? G - 1 + XG : 6, since = G < XG ? G : XG;
        vmw<4 * (last - G) + 3 * since>(X[4 * G], X[4 * G + 1], X[4 * G + 2], X[4 * G + 3]);
#pragma unroll
        for (int i = 0; i < 4; ++i) *(LAS v4u*)(xw + (G % 3) * 4096 + i * 1024 + (xc0 ^ ((i & 1) * 64))) = X[4 * G + i];
#pragma unroll
        for (int c = 0; c < 4; ++c) X[4 * G + c] = *(LAS v4u*)(xr + (G % 3) * 4096 + (((4 * h + c) ^ xsw) * 16));
        SB();
        if constexpr (G > 0) x_sq(std::integral_constant<int, G - 1>{});
        SB();
        if constexpr (G + XG < 7) sfor<4 * (G + XG), 4 * (G + XG) + 4>(x_ld);
        sfor<3 * G, 3 * G + 3>(w_ld);
        SB();
      });
      x_sq(std::integral_constant<int, 6>{});
      f32x16 ck, cv;
#pragma unroll
      for (int i = 0; i < 16; ++i) { ck[i] = 0.f; cv[i] = 0.f; }
      lptr ck0 = L + (112 * w + 8 * h + tq) * 128 + half + col, cv0 = L + (112 * w + 8 * h + tq) * 128 + (half ^ 64) + col;
      vmw0<7>();
      {
        v4u f[14];
#pragma unroll
        for (int i = 0; i < 7; ++i) { f[2 * i] = trd2(ck0 + i * 2048, ck0 + i * 2048 + 512); f[2 * i + 1] = trd2(cv0 + i * 2048, cv0 + i * 2048 + 512); }
        SB();
        sfor<0, 7>([&](auto I) { constexpr int i = I;
          ck = mfma(ones, f[2 * i], ck); SB(); o_ld(2 * i); SB();
          cv = mfma(ones, f[2 * i + 1], cv); SB(); o_ld(2 * i + 1); SB(); });
        o_ld(14);
      }
      {
        lptr cq0 = L + L_WQ + (112 * w + 8 * h + tq) * 64 + col;
        v4u f[7];
        vmw0<15>();
#pragma unroll
        for (int i = 0; i < 7; ++i) f[i] = trd2(cq0 + i * 1024, cq0 + i * 1024 + 256);
        tc2 = touch(mkrs(p.w2, S_ * F_ * 2), t2v, 0); tc3 = touch(mkrs(p.w1, S_ * F_ * 2), t3v, 0);
        *(LAS float*)(L + L_CSP + w * 384 + 128 + lane * 4) = h ? cv[0] : ck[0];
        SB();
#pragma unroll
        for (int i = 0; i < 16; ++i) ck[i] = 0.f;
#pragma unroll
        for (int i = 0; i < 7; ++i) ck = mfma(ones, f[i], ck);
        asm volatile("" : "+v"(e0));
        tag = (((u64)(u32)__builtin_amdgcn_readfirstlane(e0.y) << 32) | (u64)(u32)__builtin_amdgcn_readfirstlane(e0.x | 1u)) + 1ull;
        *(LAS float*)(L + L_CSP + w * 384 + r * 4) = ck[0];
      }
      bar();
      kv_head(std::false_type{});
      const float su0 = sq4[2] + sq4[3], sq0 = sq4[0] + sq4[1];
      lptr t0 = L + L_WO + w * 15360 + (4 * h + tq) * 128 + (((4 * wa + cpart) ^ sw) * 16) + (tp & 1) * 8;
      lptr t1 = L + L_WO + w * 15360 + (4 * h + tq) * 128 + (((4 * (wa ^ 1) + cpart) ^ sw) * 16) + (tp & 1) * 8;
      float cp[8];
      v4u img[2];
      SB();
      sfor<0, 28>([&](auto I) { constexpr int k = I; kv_step(I, std::false_type{}, [&]() {
        if constexpr (k < 4) { cp[k] = *(LAS float*)(L + L_CSP + k * 384 + 128 + lane * 4); cp[4 + k] = *(LAS float*)(L + L_CSP + k * 384 + r * 4); }
        if constexpr (k == 4) {
          lptr fp = ((tb & 1) ? wtv : wtk) + WROW(tb >> 1) * 128;
          const v4u fr = trd2(fp, fp + 512), rw = *(LAS v4u*)(L + tb * 1024 + lane * 16);
          img[0] = tb < 56 ? fr : rw; img[1] = *(LAS v4u*)(L + (tb < 20 ? tb + 64 : tb) * 1024 + lane * 16); }
        if constexpr (k == 5) *(LAS float*)(L + L_CSF + w * 512 + 128 + lane * 4) = (cp[0] + cp[1]) + (cp[2] + cp[3]);
        if constexpr (k == 6) *(LAS float*)(L + L_CSF + w * 512 + r * 4) = (cp[4] + cp[5]) + (cp[6] + cp[7]);
        if constexpr (k == 7) st2(img[0], rws, lane * 16, wi0);
        if constexpr (k == 8) st2(img[1], rws, lane * 16, wi1);
        if constexpr (k == 9) st2(v4u{0u, 0u, 0u, 0u}, rws, clv, 0);
        if constexpr (k == 10 || k == 11) { xr2[2 * k - 20] = ldp2<(2 * k - 20) * 16>(rx, xo); xr2[2 * k - 19] = ldp2<(2 * k - 19) * 16>(rx, xo); }
        if constexpr (k == 3) tc5 = touch_ws(rws, t5v, 0);
        if constexpr (k == 12) vmw0<13>();
        if constexpr (k >= 12) { constexpr int i = (k - 12) >> 1, ro = (28 * (i >> 1) + 16 * (i & 1)) * 128; lptr t = (k & 1) ? t1 : t0; wf[i][k & 1] = trd2(t + ro, t + ro + 1024); }
        ln_part(I, su0, sq0);
      }); });
      asm volatile("" : "+a"(kt), "+a"(vv));
      if (probe) {
        drain();
        const u32 q = ph(WS_PROBE + vidx * 256u);
        __builtin_amdgcn_raw_buffer_store_b32(0u, rws, lane * 4, q + (w & 1) * 8192u, 16);
        __builtin_amdgcn_raw_buffer_store_b32(0u, rws, lane * 4, q + ((w & 1) ^ 1) * 8192u, 16);
        asm volatile("" ::: "memory");
      }
    } else {
      const u32 f5 = rd(flag_off(0, 4, (2 * T + (w >> 1)) * 8 + (lane < 56 ? lane >> 3 : 6)), nb);
      Poll pq;
      pq.a = poll_one(wsa, f5);
      bar();
      pq.b = poll_one(wsa, f5);
      kv_head(std::true_type{});
      const u32 xb = opq(ph(WS_XF + (u32)tb * 32768u) ^ swz(tb));
      const u32 sqv = opqv((u32)h * 2048u + r * 16), sqs = opq(rd(WS_XF + (u32)tb * 32768u + 28672u, nb) ^ swz(tb)), l16 = opqv(lane * 16);
      pq.c = poll_one(wsa, f5);
      const u64 sd5 = poll_finish(pq, wsa, f5, tag);
      v4u sp[4];
      sfor<0, LA1>([&](auto I) { constexpr int k = I; X[k] = lda<k>(rws, l16, xb | sbit(sd5, 8 * (k >> 2))); });
      out_addr();
      float su1 = 0.f, sq1 = 0.f;
      sfor<0, 28>([&](auto I) {
        constexpr int k = I;
        constexpr int spy = k < LA1 ? (k < 4 ? k : 4) : (k - LA1 < 4 ? 4 - (k - LA1) : 0);
        vmw<((k + LA1 <= 28) ? LA1 - 1 : 27 - k) + spy>(X[k]);
        if constexpr (k == 0) poll_done(pq);
        if constexpr (k == LN0 - 1) vmw<LN0 - 5>(sp[0], sp[1], sp[2], sp[3]);
        kv_step(I, std::true_type{}, [&]() {
          if constexpr (k + LA1 < 28) X[k + LA1] = lda<k + LA1>(rws, l16, xb | sbit(sd5, 8 * ((k + LA1) >> 2)));
          if constexpr (k < 3) sp[k] = ldg<k * 512>(rws, sqv, sqs);
          if constexpr (k == 3) sp[3] = ldg<1536>(rws, h ? NOWHERE : sqv, sqs);
          if constexpr (k == LN0 - 1) {
            su1 = ((asf(sp[0].x) + asf(sp[0].z)) + (asf(sp[1].x) + asf(sp[1].z))) + ((asf(sp[2].x) + asf(sp[2].z)) + (asf(sp[3].x) + asf(sp[3].z)));
            sq1 = ((asf(sp[0].y) + asf(sp[0].w)) + (asf(sp[1].y) + asf(sp[1].w))) + ((asf(sp[2].y) + asf(sp[2].w)) + (asf(sp[3].y) + asf(sp[3].w)));
          }
          ln_part(I, su1, sq1);
        });
      });
      asm volatile("" : "+a"(kt), "+a"(vv));
    }
    lptr kvl = L + L_INBOX + lane * 16;
    v4u K0, K1, V0, V1;
    {
      u32 o[8];
#pragma unroll
      for (int i = 0; i < 8; ++i) o[i] = pk(kt[2 * i] * rstd + ck_[2 * i], kt[2 * i + 1] * rstd + ck_[2 * i + 1]);
      if (h) { o[6] = 0u; o[7] = 0u; }
      K0 = v4u{o[0], o[1], o[2], o[3]}; K1 = v4u{o[4], o[5], o[6], o[7]};
      st_sc1(K0, rws, kvq, 0);
      st_sc1(K1, rws, kvq + 1024, 0);
#pragma unroll
      for (int i = 0; i < 8; ++i) o[i] = pk(vv[2 * i] * rr[2 * i] + cv_[2 * i], vv[2 * i + 1] * rr[2 * i + 1] + cv_[2 * i + 1]);
      if (r >= 28) {
#pragma unroll
        for (int i = 0; i < 8; ++i) o[i] = 0x3F803F80u;
      }
      V0 = v4u{o[0], o[1], o[2], o[3]}; V1 = v4u{o[4], o[5], o[6], o[7]};
      st_sc1(V0, rws, kvq + 2048, 0);
      st_sc1(V1, rws, kvq + 3072, 0);
    }
    if (probe) {
      u64 t1, t2;
      asm volatile("s_waitcnt vmcnt(5)\n\ts_memtime %0\n\ts_waitcnt vmcnt(4)\n\ts_memtime %1\n\ts_waitcnt lgkmcnt(0)" : "=&s"(t1), "=&s"(t2)::"memory");
      const u32 gap = (u32)t2 - (u32)t1;
      const u32 vote = (gap > 48u && gap < 700u) ? 1u + ((w & 1) ^ half_) : 0u;
      if (lane == 0) __hip_atomic_store((GAS u64*)(wsa + WS_VOTE + vidx * 8u), ((u64)vote << 32) | (u32)tag, RLX_AGENT);
      drain();
    }
    {
      const int tsrc = T - 2 + (w >> 1);
      const u32 f1 = opqv(rd(flag_off(l, 0, (tsrc < 0 ? T : tsrc) * 16 + head), nb));
      const u32 so0 = opq(tsrc >= 0 ? ph(kvbase + (u32)(4 * T - 8 + 2 * w) * 4096u) : NOWHERE);
      const float rs = rstd * (0.18898223650461362f * 1.4426950408889634f), ms = nmr * (0.18898223650461362f * 1.4426950408889634f);
      Poll pq;
      f32x16 qt, cq;
#define ABOVE(i) (((i) & 3) + 8 * ((i) >> 2) > dg)
      const int dg = r - 4 * h;
#pragma unroll
      for (int i = 0; i < 16; ++i) qt[i] = 0.f;
      auto q_step = [&](auto I) {
        constexpr int k = I;
        qt = mfma(wq[k], X[k], qt);
        SB();
        if constexpr (k + 2 * PD < 28) wq[k + 2 * PD] = WFQ(k + 2 * PD);
        if constexpr (k < 4) { const f32x4 cs = *(LAS f32x4*)(L + L_CSF + w * 512 + (8 * k + 4 * h) * 4); cq[4 * k] = cs[0]; cq[4 * k + 1] = cs[1]; cq[4 * k + 2] = cs[2]; cq[4 * k + 3] = cs[3]; }
        if constexpr (k == QP0) pq.a = poll_one(wsa, f1);
        if constexpr (k == 27) pq.b = poll_one(wsa, f1);
        if constexpr (k >= 8 && k < 24) cq[k - 8] = mul1(cq[k - 8], ms);
        SB();
      };
      SB();
      sfor<0, QS>(q_step);
      asm volatile("" : "+a"(qt));
      publish(wsa, flag_off(l, 0, T * 16 + head), tag + ((kvq >> 13) & 1), w);
      touch_done(tc2); touch_done(tc3); touch_done(tc5);
      asm volatile("" : "+v"(xr2[0]), "+v"(xr2[1]), "+v"(xr2[2]), "+v"(xr2[3]));
#pragma unroll
      for (int i = 0; i < 4; ++i) { xres[2 * i] = xr2[i].x; xres[2 * i + 1] = xr2[i].y; }
      {
        lptr own = kvl + (8 + w) * 4096;
        *(LAS v4u*)own = K0; *(LAS v4u*)(own + 1024) = K1; *(LAS v4u*)(own + 2048) = V0; *(LAS v4u*)(own + 3072) = V1;
      }
      SB();
      sfor<QS, 28>(q_step);
      v4u Q0, Q1;
      {
        u32 o[8];
#pragma unroll
        for (int i = 0; i < 8; ++i) o[i] = pk(qt[2 * i] * rs + cq[2 * i], qt[2 * i + 1] * rs + cq[2 * i + 1]);
        if (h) { o[6] = 0x00003F80u; o[7] = 0u; }
        Q0 = v4u{o[0], o[1], o[2], o[3]}; Q1 = v4u{o[4], o[5], o[6], o[7]};
      }
      asm volatile("" : "+v"(Q0), "+v"(Q1));
      v4u t[8];
      {
        pq.c = poll_one(wsa, f1);
        const u64 sd1 = poll_finish(pq, wsa, f1, tag);
        const u32 so = so0 | sbit(sd1, 0);
        t[0] = lda<0>(rws, lane * 16, so); t[1] = lda<1>(rws, lane * 16, so); t[2] = lda<4>(rws, lane * 16, so); t[3] = lda<5>(rws, lane * 16, so);
        t[4] = lda<2>(rws, lane * 16, so); t[5] = lda<3>(rws, lane * 16, so); t[6] = lda<6>(rws, lane * 16, so); t[7] = lda<7>(rws, lane * 16, so);
        SB();
      }
      float nm;
      f32x16 cm, c0, o;
      {
        f32x16 s8 = qk_own(K0, K1, Q0, Q1);
        tie_far(s8);
#pragma unroll
        for (int i = 0; i < 16; ++i) s8[i] = ABOVE(i) ? -1e30f : s8[i];
        float m4[4];
#pragma unroll
        for (int i = 0; i < 4; ++i) m4[i] = fmaxf(fmaxf(s8[4 * i], s8[4 * i + 1]), fmaxf(s8[4 * i + 2], s8[4 * i + 3]));
        nm = -half_max(fmaxf(fmaxf(m4[0], m4[1]), fmaxf(m4[2], m4[3])));
        u32 pp[8];
#pragma unroll
        for (int i = 0; i < 8; ++i) pp[i] = pk(__builtin_amdgcn_exp2f(s8[2 * i] + nm), __builtin_amdgcn_exp2f(s8[2 * i + 1] + nm));
#pragma unroll
        for (int i = 0; i < 16; ++i) o[i] = 0.f;
        o = mfma(V0, v4u{pp[0], pp[1], pp[2], pp[3]}, o);
        o = mfma(V1, v4u{pp[4], pp[5], pp[6], pp[7]}, o);
#pragma unroll
        for (int i = 0; i < 16; ++i) { cm[i] = nm; c0[i] = ABOVE(i) ? nm : -1e30f; }
        asm volatile("" : "+v"(cm), "+v"(c0), "+a"(o));
      }
      const u32 w2v = opqv(dmarow * 4096u + dmach + (u32)st * 128u), w2s = opq((u32)(448 * w) * 4096u), w2l = opq(lds0 + ringw);
      lptr kq = kvl + w * 4096;
      SB();
      {
        lptr dst = kvl + w * 8192;
        vmw<4>(t[0], t[1], t[2], t[3]);
        poll_done(pq);
        if (tsrc < 0 && h) { t[1][2] = 0xF149u; t[3][2] = 0xF149u; }
#pragma unroll
        for (int i = 0; i < 4; ++i) *(LAS v4u*)(dst + (i & 1) * 1024 + (i >> 1) * 4096) = t[i];
        vmw<0>(t[4], t[5], t[6], t[7]);
#pragma unroll
        for (int i = 0; i < 4; ++i) *(LAS v4u*)(dst + 2048 + (i & 1) * 1024 + (i >> 1) * 4096) = t[4 + i];
      }
      bar();
      v4u vt;
      {
        v4u Kr[8][2], Vr[8][2];
        f32x16 s[8];
        u32 pp[8][8];
        float e[16];
#define KLD(j) do { Kr[j][0] = *(LAS v4u*)(kq + (j) * 4096); Kr[j][1] = *(LAS v4u*)(kq + (j) * 4096 + 1024); } while (0)
#define VLD(j) do { Vr[j][0] = *(LAS v4u*)(kq + (j) * 4096 + 2048); Vr[j][1] = *(LAS v4u*)(kq + (j) * 4096 + 3072); } while (0)
#define W2P(i) w_issue<i, (i) + 1, 4096u>(rw2, w2v, w2s, w2l)
        KLD(0); KLD(1); KLD(2); VLD(0);
        vt = ldg<0>(rws, lane * 16 + (bi & 1) * 1024, (l == 0 && w == 0) ? WS_VOTE : NOWHERE);
        SB();
        s[0] = qk1(Kr[0][0], Q0, c0); qk2(s[0], Kr[0][1], Q1);
        s[1] = qk1(Kr[1][0], Q0, cm); qk2(s[1], Kr[1][1], Q1);
        SB();
        sfor<0, 8>([&](auto I) {
          constexpr int c = I;
          tie(s[c]);
          auto ex = [&](int q) {
#pragma unroll
            for (int i = 0; i < 4; ++i) e[4 * q + i] = __builtin_amdgcn_exp2f(s[c][4 * q + i]);
          };
          auto cv = [&](int q) { pp[c][2 * q] = pk(e[4 * q], e[4 * q + 1]); pp[c][2 * q + 1] = pk(e[4 * q + 2], e[4 * q + 3]); };
          SB(); ex(0); if constexpr (c < 6) W2P(4 * c); if constexpr (c < 5) KLD(c + 3); SB();
          if constexpr (c < 6) s[c + 2] = qk1(Kr[c + 2][0], Q0, cm);
          SB(); ex(1); cv(0); if constexpr (c < 6) W2P(4 * c + 1); if constexpr (c < 7) VLD(c + 1); SB();
          if constexpr (c < 6) qk2(s[c + 2], Kr[c + 2][1], Q1);
          SB(); cv(1); ex(2); if constexpr (c < 6) W2P(4 * c + 2); SB();
          o = mfma(Vr[c][0], v4u{pp[c][0], pp[c][1], pp[c][2], pp[c][3]}, o);
          SB(); ex(3); cv(2); cv(3); if constexpr (c < 6) W2P(4 * c + 3); SB();
          o = mfma(Vr[c][1], v4u{pp[c][4], pp[c][5], pp[c][6], pp[c][7]}, o);
        });
      }
      float lsum = half_add(h ? o[12] : 0.f);
      if (!__all(lsum < 1.0e18f)) {
        float mx = -nm, mn = mx;
#pragma unroll 1
        for (int pass = 0; pass < 2; ++pass) {
#pragma unroll
          for (int i = 0; i < 16; ++i) o[i] = 0.f;
#pragma unroll 1
          for (int j = 0; j < 9; ++j) {
            f32x16 sj = qk_own(*(LAS v4u*)(kq + j * 4096), *(LAS v4u*)(kq + j * 4096 + 1024), Q0, Q1);
            tie_far(sj);
            const int lo = j == 0 ? dg : -64, hi = j == 8 ? dg : 64;
            u32 pp[8];
#pragma unroll
            for (int i = 0; i < 16; ++i) { const int key = (i & 3) + 8 * (i >> 2); sj[i] = ((key > lo) & (key <= hi)) ? sj[i] : -1e30f; mn = fmaxf(mn, sj[i]); }
#pragma unroll
            for (int i = 0; i < 8; ++i) pp[i] = pk(__builtin_amdgcn_exp2f(sj[2 * i] - mx), __builtin_amdgcn_exp2f(sj[2 * i + 1] - mx));
            o = mfma(*(LAS v4u*)(kq + j * 4096 + 2048), v4u{pp[0], pp[1], pp[2], pp[3]}, o);
            o = mfma(*(LAS v4u*)(kq + j * 4096 + 3072), v4u{pp[4], pp[5], pp[6], pp[7]}, o);
          }
          mx = half_max(mn);
        }
        lsum = half_add(h ? o[12] : 0.f);
      }
      const float inv = __builtin_amdgcn_rcpf(lsum);
      u32 oo[8];
#pragma unroll
      for (int i = 0; i < 8; ++i) oo[i] = pk(o[2 * i] * inv, o[2 * i + 1] * inv);
      if (h) { oo[6] = 0u; oo[7] = 0u; }
      st_sc1(v4u{oo[0], oo[1], oo[2], oo[3]}, rws, ob, 0);
      st_sc1(v4u{oo[4], oo[5], oo[6], oo[7]}, rws, ob + 1024, 0);
      {
        T5_PTRS
        vmw0<2>();
        w_take<0, 12, 1024>(t0, t1, bw5);
        vmw<2>(vt);
        if (l == 0) {
          const u32 tl = (u32)tag;
          const int n0 = __popcll(__ballot(vt.x == tl && vt.y == 1u)) + __popcll(__ballot(vt.z == tl && vt.w == 1u));
          const int n1 = __popcll(__ballot(vt.x == tl && vt.y == 2u)) + __popcll(__ballot(vt.z == tl && vt.w == 2u));
          if (w == 0) *(LAS u32*)(L + L_NB) = n0 > 2 * n1 + 3 ? half_ : n1 > 2 * n0 + 3 ? half_ ^ 1u : nb;
        }
      }
      publish(wsa, flag_off(l, 1, T * 16 + head), tag + ((ob >> 13) & 1), w);
      if (l == 0) nb = __builtin_amdgcn_readfirstlane(*(LAS u32*)(L + L_NB));
    }
    }
    if (p35) {
      v2u bias1[4];
      u32 bias2;
      {
      IDS
      const u32 w1v = opqv(dmarow * 3584u + dmach + (u32)ft * 128u), w1s = opq((u32)(512 * w) * 3584u), w1a = opq((u32)(512 * w + 192 * l) * 3584u), w1l = opq(lds0 + ringw);
      if (l == 0) w_issue<24 - AX, 24, 3584u>(rw1, w1v, w1s, w1l);
      w_issue<0, 4, 3584u>(rw1, w1v, w1a, w1l);
      const rsrc_t rb1 = mkrs(p.b1, F_ * 2), rb2 = mkrs(p.b2, S_ * 2);
      const u32 bo1 = opqv((u32)(ft * 64 + wa * 32 + 4 * h) * 2u), bo2 = opqv((u32)((2 * st + wb) * 32 + r) * 2u);
      const u32 f2 = rd(flag_off(l, 1, (st >> 1) * 16 + 4 * w + (lane >> 4)), nb);
      Poll pq;
      f32x16 init;
#pragma unroll
      for (int i = 0; i < 8; ++i) { init[2 * i] = blo(xres[i]); init[2 * i + 1] = bhi(xres[i]); }
      asm volatile("" : "+a"(init));
      w_issue<4, PA0, 3584u>(rw1, w1v, w1a, w1l); pq.a = poll_one(wsa, f2);
      const u32 o0 = opq(ph(WS_OF + l * OF_L + (u32)st * 65536u + (u32)wb * 32768u + (u32)w * 8192u));
      const u32 o1 = opq(o0 ^ 65536u), l16 = opqv(lane * 16);
      const u32 sb = opq(rd(WS_STATS + l * STATS_L + (u32)(2 * st + wb) * 4096u, nb)), sbw = opq((sb & ~8192u) + (u32)(dt * 512 + wa * 8)), sbv = opqv(r * 16 + h * 8192);
      w_issue<PA0, PA1, 3584u>(rw1, w1v, w1a, w1l); pq.b = poll_one(wsa, f2);
      w_issue<PA1, 24 - AX, 3584u>(rw1, w1v, w1a, w1l); pq.c = poll_one(wsa, f2);
      const u64 sd2 = poll_finish_mixed<24 - AX - PA0 + 2, 24 - AX - PA1 + 1, 0>(pq, wsa, f2, tag);
      const f32x16 y = gemm_main<8, 8, 0, true, 12, 4, false>(L, w, lane, l16, rws, o0, o1, sd2, L, L, wf, init, pq, [&](auto) {},
                                                           [&](int e) { if (e) { bias2 = ldu16(rb2, bo2); sfor<0, 4>([&](auto I) { constexpr int i = I; bias1[i] = ldp2<16 * i>(rb1, bo1); }); } });
      float s1 = 0.f, s2 = 0.f;
#pragma unroll
      for (int i = 0; i < 8; ++i) {
        xres[i] = pk(y[2 * i], y[2 * i + 1]);
        s1 = __builtin_amdgcn_fdot2_f32_bf16(asbf2(xres[i]), asbf2(0x3F803F80u), s1, false);
        s2 = __builtin_amdgcn_fdot2_f32_bf16(asbf2(xres[i]), asbf2(xres[i]), s2, false);
      }
      s1 = half_add(s1); s2 = half_add(s2);
      __builtin_amdgcn_raw_buffer_store_b64(v2u{asu(s1), asu(s2) | 1u}, rws, sbv, sbw, 16);
      SB();
      T4_PTRS
      asm volatile("" : "+v"(n0), "+v"(n1));
      if (l == 0) {
#pragma unroll
        for (int i = NB; i < 20; ++i) { asm volatile("" : "=v"(bw4[i][0])); asm volatile("" : "=v"(bw4[i][1])); }
        w_take<0, NB, 512>(n0, n1, bw4);
        lgk0();
        w_issue<24, 24 + PB0, 3584u>(rw1, w1v, w1s, w1l);
      } else {
        w_take<NB, 20, 512>(n0, n1, bw4);
        lgk0();
        w_issue<24 + 2 * NB, 28 + 2 * NB, 3584u>(rw1, w1v, w1s, w1l);
      }
      f32x16 xv;
#pragma unroll
      for (int i = 0; i < 8; ++i) { xv[2 * i] = blo(xres[i]); xv[2 * i + 1] = bhi(xres[i]); }
      lptr tw = L + L_TR + w * 2304 + r * 72 + h * 8, tr = L + L_TR + w * 2304 + (8 * h + tq) * 72 + ch16 * 32 + tp * 8;
      asm volatile("" : "+v"(xv), "+v"(tw), "+v"(tr));
      const u32 hb = opq(rd(WS_HF + l * HF_L + (u32)((2 * dt + wa) * 128 + (2 * st + wb) * 2) * 1024u, nb ^ 1u) ^ swz((st >> 3) + dt));
      float t1s, t2s;
      {
        v4u gr[4];
        const u32 gv = (u32)h * 2048u + r * 16, gv3 = h ? NOWHERE : gv;
        gr[0] = ldg<0>(rws, gv, sb); gr[1] = ldg<512>(rws, gv, sb); gr[2] = ldg<1024>(rws, gv, sb); gr[3] = ldg<1536>(rws, gv3, sb);
        if (l == 0) { w_issue<24 + PB0, 24 + PBN, 3584u>(rw1, w1v, w1s, w1l); vmw0<PBN - PB0>(); } else drain();
        vmw<63>(gr[0], gr[1], gr[2], gr[3]);
        asm volatile("" : "+v"(bias1[0]), "+v"(bias1[1]), "+v"(bias1[2]), "+v"(bias1[3]), "+v"(bias2));
        for (u32 spins = 0;;) {
          const u32 ok = (gr[0].y & gr[0].w & gr[1].y) & (gr[1].w & gr[2].y & gr[2].w) & ((gr[3].y & gr[3].w) | (u32)h);
          if (__all(ok & 1u)) break;
          if (spin_out(wsa, spins, (u32)tag)) break;
          asm volatile("" ::: "memory");
          gr[0] = ld_sc1(rws, gv, sb); gr[1] = ld_sc1(rws, gv + 512, sb); gr[2] = ld_sc1(rws, gv + 1024, sb); gr[3] = ld_sc1(rws, gv3 + 1536, sb);
        }
        t1s = ((asf(gr[0].x) + asf(gr[0].z)) + (asf(gr[1].x) + asf(gr[1].z))) + ((asf(gr[2].x) + asf(gr[2].z)) + (asf(gr[3].x) + asf(gr[3].z)));
        t2s = ((asf(gr[0].y) + asf(gr[0].w)) + (asf(gr[1].y) + asf(gr[1].w))) + ((asf(gr[2].y) + asf(gr[2].w)) + (asf(gr[3].y) + asf(gr[3].w)));
      }
      t1s = half_add(t1s); t2s = half_add(t2s);
      const float m1 = t1s * (1.f / 448.f);
      const float var = fmaxf(t2s * (1.f / 448.f) - m1 * m1, 0.f);
      const float r1 = __builtin_amdgcn_rsqf(var + EPS);
      const float r2 = __builtin_amdgcn_rsqf(var * r1 * r1 + EPS);
      const float rr = r1 * r2, mm = m1 * rr;
#pragma unroll
      for (int i = 0; i < 4; ++i)
        *(LAS v2u*)(tw + i * 16) = v2u{pk(xv[4 * i] * rr - mm, xv[4 * i + 1] * rr - mm), pk(xv[4 * i + 2] * rr - mm, xv[4 * i + 3] * rr - mm)};
      asm volatile("" ::: "memory");
      const v4u h0 = trd2(tr, tr + 4 * 72), h1 = trd2(tr + 16 * 72, tr + 20 * 72);
      st_sc1(h0, rws, l16, hb); st_sc1(h1, rws, l16 + 1024, hb);
      st_sc1(h0, rws, l16, hb ^ 8192u); st_sc1(h1, rws, l16 + 1024, hb ^ 8192u);
      if (l == 0) {
        vmw0<4>();
        w_take<NB, 20, 512>(n0, n1, bw4);
      }
      publish(wsa, flag_off(l, 2, dt * 32 + st), tag, w);
      }
      if (p4) {
        IDS
        T4_PTRS
        const u32 w1v = opqv(dmarow * 3584u + dmach + (u32)ft * 128u), w1s = opq((u32)(512 * w) * 3584u), w1l = opq(lds0 + ringw);
        if (l == 0) w_issue<24 + PBN, 28 + 2 * NB, 3584u>(rw1, w1v, w1s, w1l); else w_issue<48 - AX, 48, 3584u>(rw1, w1v, w1s, w1l);
        const u32 f3 = rd(flag_off(l, 2, dt4 * 32 + 8 * w + (lane >> 3)), nb);
        Poll pq;
        f32x16 init;
#pragma unroll
        for (int i = 0; i < 8; ++i) { const u32 bb = (i & 1) ? bias1[i >> 1].y : bias1[i >> 1].x; init[2 * i] = blo(bb); init[2 * i + 1] = bhi(bb); }
        asm volatile("" : "+a"(init));
        w_issue<28 + 2 * NB, 64 - PC0, 3584u>(rw1, w1v, w1s, w1l); pq.a = poll_one(wsa, f3);
        const u32 hb = WS_HF + l * HF_L + (u32)(32 * w) * 1024u;
        const u32 h0 = opq(rd(hb + (u32)(2 * dt4 + wb) * 131072u, nb) ^ swz(w + dt4)), h1 = opq(h0 ^ 262144u);
        v4u raw[12];
        const u32 l16 = opqv(lane * 16);
        asm volatile("" : "+v"(n0), "+v"(n1));
        const u32 ub = opq(rd(WS_UF + l * UF_L + (u32)((2 * dt4 + wb) * 128 + 4 * ft + 4 * pj + 2 * wa) * 1024u, nb ^ 1u) ^ swz(pj + dt4));
        const u32 w2v = opqv(dmarow * 4096u + dmach + (u32)st * 128u), w2s = opq((u32)(448 * w + 192) * 4096u), w2l = opq(lds0 + ringw);
        w_issue<64 - PC0, 64 - PC1, 3584u>(rw1, w1v, w1s, w1l); pq.b = poll_one(wsa, f3);
        w_issue<64 - PC1, 64, 3584u>(rw1, w1v, w1s, w1l); pq.c = poll_one(wsa, f3);
        (void)poll_finish_mixed<PC0 + 2, PC1 + 1, 0>(pq, wsa, f3, tag);
        const f32x16 y = gemm_main<32, 20, 512, true, 12, 6, true>(L, w, lane, l16, rws, h0, h1, 0, n0, n1, bw4, init, pq,
                                                                   [&](auto I) { constexpr int i = I; raw[i] = ldr<i * 32768>(rw2, w2v, w2s); }, [&](int e) { if (e) w_issue<12, 12 + PX, 4096u>(rw2, w2v, w2s, w2l); });
        u32 o[8];
#pragma unroll
        for (int i = 0; i < 8; ++i) {
          const f32x2 u = {y[2 * i], y[2 * i + 1]};
          const f32x2 z = u * (u * u * -0.1029432397f - 2.302208198f);
          const f32x2 d = f32x2{__builtin_amdgcn_exp2f(z[0]), __builtin_amdgcn_exp2f(z[1])} + 1.f;
          const f32x2 gv = u * f32x2{__builtin_amdgcn_rcpf(d[0]), __builtin_amdgcn_rcpf(d[1])};
          o[i] = pk(gv[0], gv[1]);
        }
        st2(v4u{o[0], o[1], o[2], o[3]}, rws, lane * 16, ub);
        st2(v4u{o[4], o[5], o[6], o[7]}, rws, lane * 16 + 1024, ub);
        T5_PTRS
        asm volatile("s_waitcnt vmcnt(4)" : "+v"(raw[0]), "+v"(raw[1]), "+v"(raw[2]), "+v"(raw[3]), "+v"(raw[4]), "+v"(raw[5]), "+v"(raw[6]), "+v"(raw[7]), "+v"(raw[8]), "+v"(raw[9]), "+v"(raw[10]), "+v"(raw[11])::"memory");
#pragma unroll
        for (int i = 0; i < 12; ++i) *(LAS v4u*)(L + ringw + i * 1024 + lane * 16) = raw[i];
        asm volatile("" ::: "memory");
        w_take<12, 16, 1024>(t0, t1, bw5);
        publish(wsa, flag_off(l, 3, dt4 * 32 + ft), tag, w);
      } else {
        IDS
        T5_PTRS
        w_issue<24, 36 + PX, 4096u>(W2_ARGS);
        drain();
        w_take<12, 16, 1024>(t0, t1, bw5);
        lgk0();
        __builtin_amdgcn_s_sleep(90);
      }
      IDS
      T5_PTRS
      w_issue<36 + PX, 40 + PX, 4096u>(W2_ARGS);
      const u32 f4 = rd(flag_off(l, 3, dt * 32 + 7 * w + (lane < 56 ? lane >> 3 : 6)), nb);
      Poll pq;
      f32x16 init;
      const float bv = blo(bias2);
#pragma unroll
      for (int i = 0; i < 8; ++i) { init[2 * i] = blo(xres[i]) + bv; init[2 * i + 1] = bhi(xres[i]) + bv; }
      asm volatile("" : "+a"(init));
      w_issue<40 + PX, 56 - PD0, 4096u>(W2_ARGS); pq.a = poll_one(wsa, f4);
      const u32 ub = WS_UF + l * UF_L + (u32)(32 * w) * 1024u;
      const u32 u0 = opq(rd(ub + (u32)(2 * dt + wa) * 131072u, nb) ^ swz(w + dt)), u1 = opq(u0 ^ 262144u);
      const u32 wis = opq(l == 0 ? rd(WS_WI + (u32)(head * 4 + w) * 32768u, nb) : NOWHERE);
      const u32 l16 = opqv(lane * 16);
      const u32 xb = opq(rd(WS_XF + (u32)((2 * st + wb) * 32 + 4 * dt) * 1024u + wa * 512, nb) ^ swz(2 * st + wb));
      const u32 xq = opq(ph(WS_XF + (u32)((2 * st + wb) * 32 + 28) * 1024u + (u32)(dt * 512 + wa * 8)) ^ swz(2 * st + wb));
      asm volatile("" : "+v"(t0), "+v"(t1));
      w_issue<56 - PD0, 56 - PD1, 4096u>(W2_ARGS); pq.b = poll_one(wsa, f4);
      w_issue<56 - PD1, 56, 4096u>(W2_ARGS); pq.c = poll_one(wsa, f4);
      (void)poll_finish_mixed<PD0 + 2, PD1 + 1, 0>(pq, wsa, f4, tag);
      v4u wi[21];
      const f32x16 y = gemm_main<28, 16, 1024, false, 12, 6, true>(L, w, lane, l16, rws, u0, u1, 0, t0, t1, bw5, init, pq,
                                                                   [&](auto I) { constexpr int i = I; wi[i] = lda<i>(rws, lane * 16, wis); }, [](int) {});
      asm volatile("s_waitcnt vmcnt(0)" : "+v"(wi[0]), "+v"(wi[1]), "+v"(wi[2]), "+v"(wi[3]), "+v"(wi[4]), "+v"(wi[5]), "+v"(wi[6]), "+v"(wi[7]), "+v"(wi[8]), "+v"(wi[9]), "+v"(wi[10]), "+v"(wi[11]));
#pragma unroll
      for (int i = 0; i < 8; ++i) xres[i] = pk(y[2 * i], y[2 * i + 1]);
      v4u xo[2];
#pragma unroll
      for (int s = 0; s < 2; ++s) {
        auto a = __builtin_amdgcn_permlane32_swap(xres[4 * s], xres[4 * s + 2], false, false);
        auto c = __builtin_amdgcn_permlane32_swap(xres[4 * s + 1], xres[4 * s + 3], false, false);
        xo[s] = v4u{a[0], c[0], a[1], c[1]};
      }
      if (l == 0) {
        st_sc1(xo[0], rws, h * 1024 + r * 16, xb);
        st_sc1(xo[1], rws, h * 1024 + r * 16 + 2048, xb);
        float q0 = 0.f, q1 = 0.f;
#pragma unroll
        for (int i = 0; i < 8; ++i) { q0 = __builtin_amdgcn_fdot2_f32_bf16(asbf2(xres[i]), asbf2(0x3F803F80u), q0, false); q1 = __builtin_amdgcn_fdot2_f32_bf16(asbf2(xres[i]), asbf2(xres[i]), q1, false); }
        q0 = half_add(q0); q1 = half_add(q1);
        __builtin_amdgcn_raw_buffer_store_b64(v2u{asu(q0), asu(q1)}, rws, r * 16 + h * 8192, xq, 16);
        asm volatile("" ::: "memory");
        sfor<0, 12>([&](auto I) { constexpr int i = I; if constexpr (i < 9) wi[12 + i] = lda<12 + i>(rws, l16, wis); *(LAS v4u*)(L + w * 21504 + i * 1024 + lane * 16) = wi[i]; SB(); });
        publish(wsa, flag_off(0, 4, st * 8 + dt), tag + ((xb >> 13) & 1), w);
        asm volatile("" : "+v"(wi[12]), "+v"(wi[13]), "+v"(wi[14]), "+v"(wi[15]), "+v"(wi[16]), "+v"(wi[17]), "+v"(wi[18]), "+v"(wi[19]), "+v"(wi[20]));
        sfor<12, 21>([&](auto I) { constexpr int i = I; *(LAS v4u*)(L + w * 21504 + i * 1024 + lane * 16) = wi[i]; });
      } else {
        const rsrc_t rout = mkrs(p.out, S_ * D_ * 2);
        const u32 oo = (u32)((2 * st + wb) * 32 + r) * 896u + (u32)(dt * 64 + wa * 32 + 8 * h) * 2u;
        __builtin_amdgcn_raw_buffer_store_b128(xo[0], rout, oo, 0, 0);
        __builtin_amdgcn_raw_buffer_store_b128(xo[1], rout, oo + 32, 0, 0);
        if (b == 0 && tid == 0) __hip_atomic_store((GAS u64*)(wsa + WS_EPOCH), tag + 1, RLX_AGENT);
      }
    } else {
      IDS
      const u32 wi_ = (u32)((bi - 28) * 4 + w);
      const rsrc_t r1 = mkrs(p.w1, S_ * F_ * 2), r2 = mkrs(p.w2, S_ * F_ * 2);
      const u32 v1 = (lane >> 4) * (512u * 3584u) + ((lane >> 2) & 3) * 3584u + (lane & 3) * 128u, c1 = (u32)(7 * pj + 4 * (g & 1)) * 128u;
      const u32 v2 = (lane >> 4) * (448u * 4096u) + ((lane >> 2) & 3) * 4096u + (lane & 3) * 128u, c2 = (u32)g * 512u;
      { u32 d[5];
#pragma unroll
        for (int i = 0; i < 5; ++i) d[i] = touch(r1, v1, (192 + 4 * wi_ + 64 * i) * 3584u + c1);
        drain();
#pragma unroll
        for (int i = 0; i < 5; ++i) touch_done(d[i]); }
      for (u32 row = 192 + 4 * wi_; row < 448; row += 64) { const u32 d = touch(r2, v2, row * 4096u + c2); drain(); touch_done(d); }
      if (l == 0) {
        const Poll pq = wait_flags(wsa, rd(flag_off(0, 1, ((lane >> 1) & 15) * 16 + head - 4 * h), nb), tag);
        const u32 wis = rd(WS_WI + (u32)(head * 4 + w) * 32768u, nb);
        v4u wi[21];
        sfor<0, 21>([&](auto I) { constexpr int i = I; wi[i] = lda<i>(rws, lane * 16, wis); });
        sfor<0, 21>([&](auto I) { constexpr int i = I; vmw<20 - i>(wi[i]); *(LAS v4u*)(L + w * 21504 + i * 1024 + lane * 16) = wi[i]; });
        poll_done(pq);
        for (u32 i = 0; i < 3; ++i) { const u32 d = touch_ws(rws, lane * 128, rd(WS_WI + (u32)((head - 4) * 4 + w) * 32768u + i * 8192u, nb)); drain(); touch_done(d); }
        for (u32 row = 4 * wi_; row < 192; row += 64) { const u32 d = touch(r2, v2, row * 4096u + c2); drain(); touch_done(d); }
        for (u32 row = 192 + 4 * wi_; row < 384; row += 64) { const u32 d = touch(r1, v1, row * 3584u + c1); drain(); touch_done(d); }
        GAS u64* f4 = (GAS u64*)(wsa + rd(flag_off(0, 3, 7 * pj + 4 * (g & 1)), nb));
        for (u32 spins = 0; spins < 256 && __hip_atomic_load(f4, RLX_AGENT) != tag; ++spins) __builtin_amdgcn_s_sleep(12);
        __builtin_amdgcn_s_sleep(70);
      }
    }
  }
  asm volatile(".Lfwd_last:");
}

extern "C" void kernel_launch(void* const* d_in, const int* in_sizes, int n_in, void* d_out, int out_size, void* d_ws, size_t ws_size,
                              hipStream_t stream) {
  Params p;
  p.x = d_in[0]; p.wqkv = d_in[1]; p.wo = d_in[2]; p.w1 = d_in[4]; p.b1 = d_in[5]; p.w2 = d_in[6]; p.b2 = d_in[7];
  p.out = d_out; p.ws = (unsigned char*)d_ws;
  (void)hipFuncSetAttribute((const void*)fused_fwd, hipFuncAttributeMaxDynamicSharedMemorySize, L_END);
  hipLaunchKernelGGL(fused_fwd, dim3(256), dim3(256), L_END, stream, p);
}
```
